```python
import torch

def _softmax(x, axis=-1):
    return torch.softmax(x, dim=axis)
def _gelu(x):
    return torch.nn.functional.gelu(x, approximate="tanh")
def _silu(x):
    return torch.nn.functional.silu(x)
def _sigmoid(x):
    return torch.sigmoid(x)
def _layernorm(x, axis=-1, eps=1e-5):
    m = x.mean(dim=axis, keepdim=True)
    v = x.var(dim=axis, keepdim=True, unbiased=False)
    return (x - m) / torch.sqrt(v + eps)
def _scatter_add(tgt, idx, src, axis=0):
    return tgt.clone().index_add_(axis, idx, src)
def _segment_sum(src, seg, n):
    out = torch.zeros((n,) + src.shape[1:], dtype=src.dtype)
    return out.index_add_(0, seg, src)
def _pad(x, pw):
    flat = tuple(v for pair in reversed(pw) for v in pair)
    return torch.nn.functional.pad(x, flat)

kernel_name = 'recurrent_scan__mha_block_cached__glu_gated_geglu__prompt'

def setup_inputs():

    _w = (lambda a: (lambda t: t.to(torch.bfloat16) if t.is_floating_point() else t)(torch.as_tensor(a)))
    def _A_setup():
        B, Sq, D = 1, 2048, 512
        H, Sk, Dh = 32, 2048, 128
        return {
            "x": _w((lambda *s: torch.randn(*s, dtype=torch.bfloat16) * 0.05)(B, Sq, D)),
            "k_cache": _w((lambda *s: torch.randn(*s, dtype=torch.bfloat16) * 3.2)(B, H, Sk, Dh)),
            "v_cache": _w((lambda *s: torch.randn(*s, dtype=torch.bfloat16) * 3.2)(B, H, Sk, Dh)),
            "wq": _w((lambda *s: torch.randn(*s, dtype=torch.bfloat16) * 3.2)(D, H * Dh) * (D ** -0.5)),
            "wo": _w((lambda *s: torch.randn(*s, dtype=torch.bfloat16) * 3.2)(H * Dh, D) * ((H * Dh) ** -0.5)),
        }
    _A_in = _A_setup()

    def _B_setup():
        B, S, Dm, Df = 1, 2048, 512, 1792
        return {
            "x": _w((lambda *s: torch.randn(*s, dtype=torch.bfloat16) * 0.05)(B, S, Dm)),
            "w_gate": _w((lambda *s: torch.randn(*s, dtype=torch.bfloat16) * 0.4)(Dm, Df) * (Dm ** -0.5)),
            "w_up": _w((lambda *s: torch.randn(*s, dtype=torch.bfloat16) * 0.4)(Dm, Df) * (Dm ** -0.5)),
            "w_down": _w((lambda *s: torch.randn(*s, dtype=torch.bfloat16) * 0.4)(Df, Dm) * (Df ** -0.5)),
        }
    _B_in = _B_setup()

    return {
        "A_x": _A_in["x"],
        "A_k_cache": _A_in["k_cache"],
        "A_v_cache": _A_in["v_cache"],
        "A_wq": _A_in["wq"],
        "A_wo": _A_in["wo"],
        "B_x": _B_in["x"],
        "B_w_gate": _B_in["w_gate"],
        "B_w_up": _B_in["w_up"],
        "B_w_down": _B_in["w_down"],
    }

def reference(A_x, A_k_cache, A_v_cache, A_wq, A_wo, B_x, B_w_gate, B_w_up, B_w_down):
    def _A_ref(x, k_cache, v_cache, wq, wo):
        B, Sq, D = x.shape
        H, Dh = 32, 128
        q = (lambda x, *d: x.permute(*d))((x @ wq).reshape(B, Sq, H, Dh), 0, 2, 1, 3)
        scores = torch.einsum("bhqd,bhkd->bhqk", q, k_cache) * (Dh ** -0.5)
        p = _softmax(scores, axis=-1)
        o = torch.einsum("bhqk,bhkd->bhqd", p, v_cache)
        return (lambda x, *d: x.permute(*d))(o, 0, 2, 1, 3).reshape(B, Sq, H * Dh) @ wo

    def _B_ref(x, w_gate, w_up, w_down):
        g = torch.einsum("bsd,df->bsf", x, w_gate)
        u = torch.einsum("bsd,df->bsf", x, w_up)
        h = _gelu(g) * u
        return torch.einsum("bsf,fd->bsd", h, w_down)

    x = A_x
    xn = _layernorm(x, axis=-1)
    s = _A_ref(xn, A_k_cache, A_v_cache, A_wq, A_wo)
    g = _silu(_B_ref(xn, B_w_gate, B_w_up, B_w_down))
    return x + s * g
```

```cpp
#include <hip/hip_runtime.h>
#include <hip/hip_bf16.h>
#include <utility>

constexpr int S = 2048, H = 32, D = 128, DM = 512, DF = 1792, NT = S / 64;
constexpr float THR = 30.0f;

typedef float    f32x16 __attribute__((ext_vector_type(16)));
typedef float    f32x2  __attribute__((ext_vector_type(2)));
typedef float    f32x4  __attribute__((ext_vector_type(4)));
typedef __bf16   bf16x8 __attribute__((ext_vector_type(8)));
typedef __bf16   bf16x4 __attribute__((ext_vector_type(4)));
typedef __bf16   bf16x2 __attribute__((ext_vector_type(2)));
typedef unsigned u32x4  __attribute__((ext_vector_type(4)));
typedef unsigned u32x2  __attribute__((ext_vector_type(2)));
template <int... Is> using iseq = std::integer_sequence<int, Is...>;
template <int N> using make_iseq = std::make_integer_sequence<int, N>;

#define DI __device__ __forceinline__
#define GAP() __builtin_amdgcn_sched_barrier(0)
#define SRD_WORD3 0x00020000

constexpr size_t WS_XN = 0, WS_O = (size_t)S * DM * 2, WS_H = WS_O + (size_t)S * H * D * 2, WS_END = WS_H + (size_t)S * DF * 2;

DI unsigned rfl(unsigned x) { return __builtin_amdgcn_readfirstlane(x); }
DI unsigned cvtpk(float lo, float hi) {
  f32x2 v = {lo, hi}; bf16x2 b = __builtin_convertvector(v, bf16x2); return __builtin_bit_cast(unsigned, b);
}
DI float bflo(unsigned w) { return __uint_as_float(w << 16); }
DI float bfhi(unsigned w) { return __uint_as_float(w & 0xffff0000u); }
DI float max3f(float a, float b, float c) { float r; asm("v_max3_f32 %0, %1, %2, %3" : "=v"(r) : "v"(a), "v"(b), "v"(c)); return r; }
DI float max2f(float a, float b) { float r; asm("v_max_f32_e32 %0, %1, %2" : "=v"(r) : "v"(a), "v"(b)); return r; }
DI float fadd_s(float a, float b) { float r; asm("v_add_f32_e32 %0, %1, %2" : "=v"(r) : "v"(a), "v"(b)); return r; }
DI float fmul_s(float a, float b) { float r; asm("v_mul_f32_e32 %0, %1, %2" : "=v"(r) : "v"(a), "v"(b)); return r; }
DI float fsub_s(float a, float b) { float r; asm("v_sub_f32_e32 %0, %1, %2" : "=v"(r) : "v"(a), "v"(b)); return r; }

__global__ __launch_bounds__(256) void k_ln(const unsigned short* __restrict__ x, unsigned short* __restrict__ xn) {
  const int lane = threadIdx.x & 63, row = blockIdx.x * 4 + (threadIdx.x >> 6);
  const u32x4 w = *(const u32x4*)(x + (size_t)row * DM + lane * 8);
  float v[8];
#pragma unroll
  for (int e = 0; e < 4; ++e) { v[2 * e] = bflo(w[e]); v[2 * e + 1] = bfhi(w[e]); }
  float s = 0.f;
#pragma unroll
  for (int e = 0; e < 8; ++e) s += v[e];
#pragma unroll
  for (int o = 32; o >= 1; o >>= 1) s += __shfl_xor(s, o);
  const float m = s * (1.0f / DM);
  float q = 0.f;
#pragma unroll
  for (int e = 0; e < 8; ++e) { v[e] -= m; q += v[e] * v[e]; }
#pragma unroll
  for (int o = 32; o >= 1; o >>= 1) q += __shfl_xor(q, o);
  const float r = 1.0f / sqrtf(q * (1.0f / DM) + 1e-5f);
  u32x4 o4;
#pragma unroll
  for (int e = 0; e < 4; ++e) o4[e] = cvtpk(v[2 * e] * r, v[2 * e + 1] * r);
  *(u32x4*)(xn + (size_t)row * DM + lane * 8) = o4;
}

constexpr unsigned KRS = 256u, KTILEB = 64u * KRS;
constexpr size_t SHM_ATTN = 163840;
#define KRING 0x0000u
#define VRING 0x8000u
#define WRING 0x10000u
#define W4_ACLOB_HI "a128","a129","a130","a131","a132","a133","a134","a135","a136","a137","a138","a139","a140","a141","a142","a143","a144","a145","a146","a147","a148","a149","a150","a151","a152","a153","a154","a155","a156","a157","a158","a159","a160","a161","a162","a163","a164","a165","a166","a167","a168","a169","a170","a171","a172","a173","a174","a175","a176","a177","a178","a179","a180","a181","a182","a183","a184","a185","a186","a187","a188","a189","a190","a191","a192","a193","a194","a195","a196","a197","a198","a199","a200","a201","a202","a203","a204","a205","a206","a207","a208","a209","a210","a211","a212","a213","a214","a215","a216","a217","a218","a219","a220","a221","a222","a223","a224","a225","a226","a227","a228","a229","a230","a231","a232","a233","a234","a235","a236","a237","a238","a239","a240","a241","a242","a243","a244","a245","a246","a247","a248","a249","a250","a251","a252","a253","a254","a255"
#define W4_ACLOB "a0","a1","a2","a3","a4","a5","a6","a7","a8","a9","a10","a11","a12","a13","a14","a15","a16","a17","a18","a19","a20","a21","a22","a23","a24","a25","a26","a27","a28","a29","a30","a31","a32","a33","a34","a35","a36","a37","a38","a39","a40","a41","a42","a43","a44","a45","a46","a47","a48","a49","a50","a51","a52","a53","a54","a55","a56","a57","a58","a59","a60","a61","a62","a63","a64","a65","a66","a67","a68","a69","a70","a71","a72","a73","a74","a75","a76","a77","a78","a79","a80","a81","a82","a83","a84","a85","a86","a87","a88","a89","a90","a91","a92","a93","a94","a95","a96","a97","a98","a99","a100","a101","a102","a103","a104","a105","a106","a107","a108","a109","a110","a111","a112","a113","a114","a115","a116","a117","a118","a119","a120","a121","a122","a123","a124","a125","a126","a127"

template <int R> DI float acc_rd() { float r; asm volatile("v_accvgpr_read_b32 %0, a[%c1]" : "=v"(r) : "i"(R)); return r; }
template <int R> DI void acc_wr(unsigned v) { asm volatile("v_accvgpr_write_b32 a[%c1], %0" :: "v"(v), "i"(R) : W4_ACLOB_HI); }
template <int R> DI void acc_scale(float f) { float t; asm volatile("v_accvgpr_read_b32 %0, a[%c2]\n\tv_mul_f32 %0, %1, %0\n\tv_accvgpr_write_b32 a[%c2], %0" : "=&v"(t) : "v"(f), "i"(R) : W4_ACLOB); }

DI float w4_aone() { unsigned l; asm volatile("v_mbcnt_lo_u32_b32 %0, -1, 0\n\tv_mbcnt_hi_u32_b32 %0, -1, %0" : "=v"(l)); return l < 32u ? 1.f : 0.f; }
DI void negm_bcast(f32x16& negm, float aone, float neg_mhat) {
  asm volatile("s_nop 1\n\tv_mfma_f32_32x32x2_f32 %0, %1, %2, 0" : "+v"(negm) : "v"(aone), "v"(neg_mhat));
}

struct W4C {
  unsigned vkb[4], vvb, vksrc[2], vvsrc, m0k, m0v, sofk, sofv, ldsb;
  __amdgpu_buffer_rsrc_t srdK, srdV;
  f32x16 S[2][2][2];
  f32x16 NEGM[2];
  bf16x8 Vf[4][4];
  bf16x8 Pk[2][4];
  bf16x4 vlo[4][4], vup[4][4];
  float mhat[2], lsum[2], fct[2]; bool pend;
  float rs0[2], rs1[2], pmx0[2], pmx1[2], rmax[2], delta[2];
  unsigned pm0[8], psf[8];
};

constexpr int AO_(int db, int qb) { return 16 * (2 * db + qb); }
constexpr int AQ_(int qb, int d0) { return 128 + 32 * qb + 4 * d0; }
constexpr int AK_(int kb, int d0) { return 192 + 32 * kb + 4 * d0; }

template <int I> DI void tr_read(W4C& c, unsigned vslot) {
  constexpr int ks = I >> 3, db = (I >> 1) & 3;
  if constexpr ((I & 1) == 0) asm volatile("ds_read_b64_tr_b16 %0, %1 offset:%2" : "=v"(c.vlo[db][ks]) : "v"(c.vvb + vslot), "i"(4096 * ks + 512 * db) : "memory");
  else                        asm volatile("ds_read_b64_tr_b16 %0, %1 offset:%2" : "=v"(c.vup[db][ks]) : "v"(c.vvb + vslot), "i"(4096 * ks + 2048 + 512 * db) : "memory");
}
template <int I> DI void k_read(W4C& c, unsigned slot) {
  constexpr int kb = I >> 3, d0 = I & 7;
  asm volatile("ds_read_b128 a[%c1:%c2], %0 offset:%c3" :: "v"(c.vkb[d0 & 3] + slot), "i"(AK_(kb, d0)), "i"(AK_(kb, d0) + 3), "i"(8192 * kb + 128 * (d0 >> 2)) : "memory", W4_ACLOB_HI);
}
template <int X, bool FIRST, int M> DI void qk_mfma(W4C& c) {
  constexpr int d0 = M >> 2, kb = (M >> 1) & 1, qb = M & 1, ak = AK_(kb, d0), aq = AQ_(qb, d0);
  if constexpr (d0 == 0) {
    if constexpr (FIRST) asm volatile("v_mfma_f32_32x32x16_bf16 %0, a[%c1:%c2], a[%c3:%c4], 0" : "=&v"(c.S[X][kb][qb]) : "i"(ak), "i"(ak + 3), "i"(aq), "i"(aq + 3) : W4_ACLOB, W4_ACLOB_HI);
    else                 asm volatile("v_mfma_f32_32x32x16_bf16 %0, a[%c2:%c3], a[%c4:%c5], %1" : "=&v"(c.S[X][kb][qb]) : "v"(c.NEGM[qb]), "i"(ak), "i"(ak + 3), "i"(aq), "i"(aq + 3) : W4_ACLOB, W4_ACLOB_HI);
  } else asm volatile("v_mfma_f32_32x32x16_bf16 %0, a[%c1:%c2], a[%c3:%c4], %0" : "+v"(c.S[X][kb][qb]) : "i"(ak), "i"(ak + 3), "i"(aq), "i"(aq + 3) : W4_ACLOB, W4_ACLOB_HI);
}
template <int M, bool ZERO> DI void pv_mfma(W4C& c) {
  constexpr int ks = M >> 3, db = (M >> 1) & 3, qb = M & 1, ao = AO_(db, qb);
  if constexpr (ZERO && ks == 0) asm volatile("v_mfma_f32_32x32x16_bf16 a[%c2:%c3], %0, %1, 0" :: "v"(c.Vf[db][ks]), "v"(c.Pk[qb][ks]), "i"(ao), "i"(ao + 15) : W4_ACLOB);
  else                         asm volatile("v_mfma_f32_32x32x16_bf16 a[%c2:%c3], %0, %1, a[%c2:%c3]" :: "v"(c.Vf[db][ks]), "v"(c.Pk[qb][ks]), "i"(ao), "i"(ao + 15) : W4_ACLOB);
}
template <int X, int QB, int KS, int P> DI void pack1(W4C& c) {
  u32x4 w = __builtin_bit_cast(u32x4, c.Pk[QB][KS]);
  w[P] = cvtpk(c.S[X][KS >> 1][QB][8 * (KS & 1) + 2 * P], c.S[X][KS >> 1][QB][8 * (KS & 1) + 2 * P + 1]);
  c.Pk[QB][KS] = __builtin_bit_cast(bf16x8, w);
}
template <int X, int J> DI void rowsum1(W4C& c) {
  constexpr int qb = J >> 4, ks = (J >> 2) & 3, p = J & 3;
  c.rs0[qb] = fadd_s(c.rs0[qb], c.S[X][ks >> 1][qb][8 * (ks & 1) + 2 * p]); c.rs1[qb] = fadd_s(c.rs1[qb], c.S[X][ks >> 1][qb][8 * (ks & 1) + 2 * p + 1]);
}

template <int X, bool FIRST, int M> DI void gapA(W4C& c, unsigned vslot, const int t) {
  constexpr int Xp = X ^ 1;
  qk_mfma<X, FIRST, M>(c);
  if constexpr (M >= 16 && M <= 31) {
    const int tk = t + 2, tv = t + 1;
    constexpr int q = (M - 16) >> 1;
    if constexpr (((M - 16) & 1) == 0) {
      if constexpr (q < 4) c.pm0[q] = c.m0k + (unsigned)((t + 2) & 1) * 0x4000u + 1024u * q; else c.pm0[q] = c.m0v + (unsigned)((t + 1) & 1) * 0x4000u + 1024u * (q - 4);
      asm volatile("" : "+s"(c.pm0[q]));
    } else {
      if constexpr (q < 4) c.psf[q] = KTILEB * (unsigned)tk + c.sofk + KRS * 4u * q; else c.psf[q] = KTILEB * (unsigned)tv + c.sofv + KRS * 8u * ((q - 4) >> 1) + 128u * ((q - 4) & 1);
      asm volatile("" : "+s"(c.psf[q]));
    }
  }
  if constexpr (M == 31) asm volatile("s_nop 4" : "+s"(c.pm0[0]), "+s"(c.pm0[1]), "+s"(c.pm0[2]), "+s"(c.pm0[3]), "+s"(c.pm0[4]), "+s"(c.pm0[5]), "+s"(c.pm0[6]), "+s"(c.pm0[7]),
                                                  "+s"(c.psf[0]), "+s"(c.psf[1]), "+s"(c.psf[2]), "+s"(c.psf[3]), "+s"(c.psf[4]), "+s"(c.psf[5]), "+s"(c.psf[6]), "+s"(c.psf[7]));
  if constexpr (!FIRST && M == 8) asm volatile("" :: "v"(c.NEGM[0]), "v"(c.NEGM[1]));
  if constexpr (!FIRST) {
    constexpr int base = M + (M >= 4) + (M >= 8) + (M >= 12) + (M >= 16) + (M >= 20) + (M >= 24);
    if constexpr (M <= 25) tr_read<base>(c, vslot);
    if constexpr (M == 3 || M == 7 || M == 11 || M == 15 || M == 19 || M == 23) tr_read<base + 1>(c, vslot);
    if constexpr (M < 16) {
      constexpr int e0 = 2 * M, e1 = 2 * M + 1;
      c.S[Xp][1][e0 >> 4][e0 & 15] = __builtin_amdgcn_exp2f(c.S[Xp][1][e0 >> 4][e0 & 15]);
      c.S[Xp][1][e1 >> 4][e1 & 15] = __builtin_amdgcn_exp2f(c.S[Xp][1][e1 >> 4][e1 & 15]);
      pack1<Xp, (M >> 3), ((M >> 2) & 1), (M & 3)>(c);
    } else {
      constexpr int cc = M - 16;
      pack1<Xp, (cc >> 3), 2 + ((cc >> 2) & 1), (cc & 3)>(c);
      rowsum1<Xp, 16 * (cc >> 3) + 8 + 4 * ((cc >> 2) & 1) + (cc & 3)>(c);
    }
  }
  GAP();
}
#define W4_VLO "+v"(c.vlo[0][0]), "+v"(c.vlo[0][1]), "+v"(c.vlo[0][2]), "+v"(c.vlo[0][3]), "+v"(c.vlo[1][0]), "+v"(c.vlo[1][1]), "+v"(c.vlo[1][2]), "+v"(c.vlo[1][3]), \
               "+v"(c.vlo[2][0]), "+v"(c.vlo[2][1]), "+v"(c.vlo[2][2]), "+v"(c.vlo[2][3]), "+v"(c.vlo[3][0]), "+v"(c.vlo[3][1]), "+v"(c.vlo[3][2]), "+v"(c.vlo[3][3])
#define W4_VUP "+v"(c.vup[0][0]), "+v"(c.vup[0][1]), "+v"(c.vup[0][2]), "+v"(c.vup[0][3]), "+v"(c.vup[1][0]), "+v"(c.vup[1][1]), "+v"(c.vup[1][2]), "+v"(c.vup[1][3]), \
               "+v"(c.vup[2][0]), "+v"(c.vup[2][1]), "+v"(c.vup[2][2]), "+v"(c.vup[2][3]), "+v"(c.vup[3][0]), "+v"(c.vup[3][1]), "+v"(c.vup[3][2]), "+v"(c.vup[3][3])
template <int X, bool FIRST, int... Ms> DI void phaseA_(W4C& c, const int t, iseq<Ms...>) {
  const unsigned vslot = (unsigned)((t - 1) & 1) * 0x4000u;
  (gapA<X, FIRST, Ms>(c, vslot, t), ...);
  if constexpr (!FIRST) {
    #pragma unroll
    for (int qb = 0; qb < 2; ++qb) {
      const float rs = fadd_s(c.rs0[qb], c.rs1[qb]);
      auto pr = __builtin_amdgcn_permlane32_swap(__float_as_uint(rs), __float_as_uint(rs), false, false);
      c.lsum[qb] = fadd_s(c.lsum[qb], fadd_s(__uint_as_float(pr[0]), __uint_as_float(pr[1])));
    }
  }
  if constexpr (FIRST) asm volatile("s_waitcnt vmcnt(0) lgkmcnt(0)\n\ts_barrier" ::: "memory");
  else { asm volatile("s_waitcnt vmcnt(0) lgkmcnt(0)" : W4_VLO :: "memory"); asm volatile("s_barrier" : W4_VUP :: "memory"); }
  GAP();
  if constexpr (!FIRST) {
    #pragma unroll
    for (int ks = 0; ks < 4; ++ks)
      #pragma unroll
      for (int db = 0; db < 4; ++db) c.Vf[db][ks] = __builtin_shufflevector(c.vlo[db][ks], c.vup[db][ks], 0, 1, 2, 3, 4, 5, 6, 7);
  }
}
template <int X, bool FIRST> DI void phaseA(W4C& c, const int t) { phaseA_<X, FIRST>(c, t, make_iseq<32>{}); }

template <int X, bool PEEL, bool ZERO1, bool DK, bool DV, bool KR, int M> DI void gapB(W4C& c, const unsigned kslot_rd) {
  if constexpr (!PEEL) pv_mfma<M, ZERO1>(c);
  if constexpr (M < 4 && DK)                asm volatile("s_mov_b32 m0, %0\n\ts_nop 0\n\tbuffer_load_dwordx4 %1, %2, %3 offen lds" :: "s"(c.pm0[M]), "v"(c.vksrc[M & 1]), "s"(c.srdK), "s"(c.psf[M]) : "m0", "memory");
  else if constexpr (M >= 4 && M < 8 && DV) asm volatile("s_mov_b32 m0, %0\n\ts_nop 0\n\tbuffer_load_dwordx4 %1, %2, %3 offen lds" :: "s"(c.pm0[M]), "v"(c.vvsrc), "s"(c.srdV), "s"(c.psf[M]) : "m0", "memory");
  if constexpr (M >= 1 && M <= 16 && KR) k_read<M - 1>(c, kslot_rd);
  if constexpr (M >= 8 && M < 16) {
    constexpr int i = M - 8, q = i >> 2, r = 4 * (i & 3);
    if constexpr ((i & 3) == 0) { c.pmx0[q] = max3f(c.S[X][0][q][0], c.S[X][0][q][1], c.S[X][1][q][0]); c.pmx1[q] = max3f(c.S[X][0][q][2], c.S[X][0][q][3], c.S[X][1][q][1]);
                                  c.pmx0[q] = max3f(c.pmx0[q], c.S[X][1][q][2], c.S[X][1][q][3]); }
    else                        { c.pmx0[q] = max3f(c.pmx0[q], c.S[X][0][q][r], c.S[X][0][q][r + 1]); c.pmx1[q] = max3f(c.pmx1[q], c.S[X][0][q][r + 2], c.S[X][0][q][r + 3]);
                                  c.pmx0[q] = max3f(c.pmx0[q], c.S[X][1][q][r], c.S[X][1][q][r + 1]); c.pmx1[q] = max3f(c.pmx1[q], c.S[X][1][q][r + 2], c.S[X][1][q][r + 3]); }
  }
  if constexpr (M == 16 || M == 17) {
    constexpr int q = M - 16; const float mm = max2f(c.pmx0[q], c.pmx1[q]);
    auto pr = __builtin_amdgcn_permlane32_swap(__float_as_uint(mm), __float_as_uint(mm), false, false);
    c.rmax[q] = max2f(__uint_as_float(pr[0]), __uint_as_float(pr[1]));
  }
  if constexpr (M == 18) {
    if constexpr (PEEL) {
      #pragma unroll
      for (int q = 0; q < 2; ++q) { c.mhat[q] = c.rmax[q]; c.lsum[q] = 0.f; c.fct[q] = 1.f;
        #pragma unroll
        for (int kb = 0; kb < 2; ++kb) { _Pragma("unroll") for (int e = 0; e < 16; ++e) c.S[X][kb][q][e] = fsub_s(c.S[X][kb][q][e], c.rmax[q]); }
        negm_bcast(c.NEGM[q], w4_aone(), -c.mhat[q]);
      }
    } else {
      const bool grew = (c.rmax[0] > THR) | (c.rmax[1] > THR);
      if (__builtin_expect(__any((int)grew), 0)) {
        #pragma unroll
        for (int q = 0; q < 2; ++q) { const float dl = max2f(c.rmax[q], 0.f); c.delta[q] = dl; c.mhat[q] = fadd_s(c.mhat[q], dl); c.fct[q] = __builtin_amdgcn_exp2f(-dl);
          #pragma unroll
          for (int kb = 0; kb < 2; ++kb) { _Pragma("unroll") for (int e = 0; e < 16; ++e) c.S[X][kb][q][e] = fsub_s(c.S[X][kb][q][e], dl); }
          negm_bcast(c.NEGM[q], w4_aone(), -c.mhat[q]);
        }
        c.pend = true;
      }
    }
  }
  if constexpr (M == 18) { c.rs0[0] = c.rs0[1] = c.rs1[0] = c.rs1[1] = 0.f; }
  if constexpr (M >= 19) {
    constexpr int g = M - 19, e_lo = (32 * g) / 13, e_hi = (32 * (g + 1)) / 13;
    {
      constexpr int p_lo = g ? (32 * (g - 1)) / 13 : 0, p_hi = g ? (32 * g) / 13 : 0;
      if constexpr (p_lo + 0 < p_hi) { constexpr int e = p_lo + 0; if constexpr (e & 1) c.rs1[e >> 4] = fadd_s(c.rs1[e >> 4], c.S[X][0][e >> 4][e & 15]); else c.rs0[e >> 4] = fadd_s(c.rs0[e >> 4], c.S[X][0][e >> 4][e & 15]); }
      if constexpr (p_lo + 1 < p_hi) { constexpr int e = p_lo + 1; if constexpr (e & 1) c.rs1[e >> 4] = fadd_s(c.rs1[e >> 4], c.S[X][0][e >> 4][e & 15]); else c.rs0[e >> 4] = fadd_s(c.rs0[e >> 4], c.S[X][0][e >> 4][e & 15]); }
      if constexpr (p_lo + 2 < p_hi) { constexpr int e = p_lo + 2; if constexpr (e & 1) c.rs1[e >> 4] = fadd_s(c.rs1[e >> 4], c.S[X][0][e >> 4][e & 15]); else c.rs0[e >> 4] = fadd_s(c.rs0[e >> 4], c.S[X][0][e >> 4][e & 15]); }
    }
    if constexpr (e_lo + 0 < e_hi) c.S[X][0][(e_lo + 0) >> 4][(e_lo + 0) & 15] = __builtin_amdgcn_exp2f(c.S[X][0][(e_lo + 0) >> 4][(e_lo + 0) & 15]);
    if constexpr (e_lo + 1 < e_hi) c.S[X][0][(e_lo + 1) >> 4][(e_lo + 1) & 15] = __builtin_amdgcn_exp2f(c.S[X][0][(e_lo + 1) >> 4][(e_lo + 1) & 15]);
    if constexpr (e_lo + 2 < e_hi) c.S[X][0][(e_lo + 2) >> 4][(e_lo + 2) & 15] = __builtin_amdgcn_exp2f(c.S[X][0][(e_lo + 2) >> 4][(e_lo + 2) & 15]);
  }
  if constexpr (M == 31) asm volatile("" :: "v"(c.S[X][0][0]), "v"(c.S[X][0][1]), "v"(c.NEGM[0]), "v"(c.NEGM[1]), "v"(c.rs0[0]), "v"(c.rs0[1]), "v"(c.rs1[0]), "v"(c.rs1[1]));
  GAP();
}
template <int... Rs> DI void rescale_o(float f0, float f1, iseq<Rs...>) { (acc_scale<Rs>(((Rs >> 4) & 1) ? f1 : f0), ...); }
template <int X, bool PEEL, bool ZERO1, bool DK, bool DV, bool KR, int... Ms> DI void phaseB_(W4C& c, const int t, iseq<Ms...>) {
  const unsigned kslot_rd = (unsigned)((t + 1) & 1) * 0x4000u;
  if constexpr (!PEEL) asm volatile("s_nop 1" ::: "memory");
  GAP();
  (gapB<X, PEEL, ZERO1, DK, DV, KR, Ms>(c, kslot_rd), ...);
  {
    #pragma unroll
    for (int e = (32 * 12) / 13; e < 32; ++e) { if (e & 1) c.rs1[e >> 4] = fadd_s(c.rs1[e >> 4], c.S[X][0][e >> 4][e & 15]); else c.rs0[e >> 4] = fadd_s(c.rs0[e >> 4], c.S[X][0][e >> 4][e & 15]); }
  }
  if constexpr (!PEEL) {
    if (__builtin_expect(c.pend, 0)) {
      asm volatile("s_nop 7\n\ts_nop 7" ::: "memory");
      rescale_o(c.fct[0], c.fct[1], make_iseq<128>{});
      asm volatile("s_nop 1" ::: "memory");
      c.lsum[0] = fmul_s(c.lsum[0], c.fct[0]); c.lsum[1] = fmul_s(c.lsum[1], c.fct[1]); c.pend = false;
    }
  }
  asm volatile("s_waitcnt lgkmcnt(0)" ::: "memory");
  GAP();
}
template <int X, bool PEEL, bool ZERO1, bool DK = true, bool DV = true, bool KR = true> DI void phaseB(W4C& c, const int t) { phaseB_<X, PEEL, ZERO1, DK, DV, KR>(c, t, make_iseq<32>{}); }

template <int... Is> DI void k_reads(W4C& c, unsigned slot, iseq<Is...>) { (k_read<Is>(c, slot), ...); }
template <int... Is> DI void tr_reads(W4C& c, unsigned vslot, iseq<Is...>) { (tr_read<Is>(c, vslot), ...); }
template <int X, int... Cs> DI void packs(W4C& c, iseq<Cs...>) { (pack1<X, ((Cs >> 3) & 1), 2 * (Cs >> 4) + ((Cs >> 2) & 1), (Cs & 3)>(c), ...); }
template <int X, int J> DI void rowsum1_padded(W4C& c) { rowsum1<X, J>(c); asm volatile("s_nop 1"); }
template <int X, int... Js> DI void rowsums(W4C& c, iseq<Js...>) { (rowsum1_padded<X, Js>(c), ...); }
template <int M> DI void pv_drain1(W4C& c) { pv_mfma<M, false>(c); GAP(); }
template <int... Ms> DI void pv_drain(W4C& c, iseq<Ms...>) { (pv_drain1<Ms>(c), ...); }

struct PC {
  unsigned vwb, vwsq, vwsg, vx;
  unsigned m0w, sofq, sofg;
  __amdgpu_buffer_rsrc_t srdQ, srdG, srdU, srdX;
  bf16x4 wl[32], wu[32];
  u32x4 xf[2][8];
};
constexpr int PLEAD = 8;
template <int T, int F, int HALF> DI void w_read(PC& p) {
  constexpr int ks = F >> 3, b = F & 7, off = 16384 * (b >> 2) + 4096 * ks + 512 * (b & 3) + 256 * HALF;
  const unsigned base = p.vwb + 32768u * (T % 3);
  if constexpr (HALF == 0) asm volatile("ds_read_b64_tr_b16 %0, %1 offset:%2" : "=v"(p.wl[F]) : "v"(base), "i"(off) : "memory");
  else                     asm volatile("ds_read_b64_tr_b16 %0, %1 offset:%2" : "=v"(p.wu[F]) : "v"(base), "i"(off) : "memory");
}
template <int T, int I> DI void x_load(PC& p) {
  constexpr int qb = I >> 2, ks = I & 3;
  asm volatile("buffer_load_dwordx4 %0, %1, %2, %3 offen offset:%4" : "=v"(p.xf[T & 1][I]) : "v"(p.vx), "s"(p.srdX), "s"(32768u * qb + 128u * T), "i"(32 * ks) : "memory");
}
template <int T, int Q> DI void w_dma(PC& p) {
  if constexpr (Q < 4)
    asm volatile("s_mov_b32 m0, %0\n\ts_nop 0\n\tbuffer_load_dwordx4 %1, %2, %3 offen lds"
                 :: "s"(p.m0w + 32768u * (T % 3) + 1024u * Q), "v"(p.vwsq), "s"(p.srdQ), "s"(p.sofq + 8192u * 64u * T + 8192u * 8u * (Q >> 1) + 128u * (Q & 1)) : "m0", "memory");
  else if constexpr (((Q - 4) & 1) == 0)
    asm volatile("s_mov_b32 m0, %0\n\ts_nop 0\n\tbuffer_load_dwordx4 %1, %2, %3 offen lds"
                 :: "s"(p.m0w + 32768u * (T % 3) + 16384u + 1024u * (Q - 4)), "v"(p.vwsg), "s"(p.srdG), "s"(p.sofg + 3584u * 64u * T + 3584u * 8u * ((Q - 4) >> 1)) : "m0", "memory");
  else
    asm volatile("s_mov_b32 m0, %0\n\ts_nop 0\n\tbuffer_load_dwordx4 %1, %2, %3 offen lds"
                 :: "s"(p.m0w + 32768u * (T % 3) + 16384u + 1024u * (Q - 4)), "v"(p.vwsg), "s"(p.srdU), "s"(p.sofg + 3584u * 64u * T + 3584u * 8u * ((Q - 4) >> 1)) : "m0", "memory");
}
template <int TK, int Q> DI void kv_dma0(W4C& c) {
  if constexpr (Q < 4)
    asm volatile("s_mov_b32 m0, %0\n\ts_nop 0\n\tbuffer_load_dwordx4 %1, %2, %3 offen lds"
                 :: "s"(c.m0k + 0x4000u * TK + 1024u * Q), "v"(c.vksrc[Q & 1]), "s"(c.srdK), "s"(KTILEB * TK + c.sofk + KRS * 4u * Q) : "m0", "memory");
  else
    asm volatile("s_mov_b32 m0, %0\n\ts_nop 0\n\tbuffer_load_dwordx4 %1, %2, %3 offen lds"
                 :: "s"(c.m0v + 1024u * (Q - 4)), "v"(c.vvsrc), "s"(c.srdV), "s"(c.sofv + KRS * 8u * ((Q - 4) >> 1) + 128u * ((Q - 4) & 1)) : "m0", "memory");
}
template <int T, int M> DI void gapP(PC& p, W4C& c) {
  constexpr int F = M >> 1, qb = M & 1, ks = F >> 3, b = F & 7, ao = 16 * (2 * b + qb);
  if constexpr (M == 0) {
    asm volatile("s_waitcnt vmcnt(8)" : "+v"(p.xf[T & 1][0]), "+v"(p.xf[T & 1][1]), "+v"(p.xf[T & 1][2]), "+v"(p.xf[T & 1][3]),
                                        "+v"(p.xf[T & 1][4]), "+v"(p.xf[T & 1][5]), "+v"(p.xf[T & 1][6]), "+v"(p.xf[T & 1][7]) :: "memory");
  }
  if constexpr (qb == 0) {
    constexpr int younger = (T < 7) ? (PLEAD - 2) : ((64 - 2 * F - 2) < (PLEAD - 2) ? (64 - 2 * F - 2) : (PLEAD - 2));
    asm volatile("s_waitcnt lgkmcnt(%c2)" : "+v"(p.wl[F]), "+v"(p.wu[F]) : "i"(younger) : "memory");
  }
  {
    const bf16x8 wf = __builtin_shufflevector(p.wl[F], p.wu[F], 0, 1, 2, 3, 4, 5, 6, 7);
    if constexpr (T == 0 && ks == 0) asm volatile("v_mfma_f32_32x32x16_bf16 a[%c2:%c3], %0, %1, 0" :: "v"(wf), "v"(p.xf[T & 1][4 * qb + ks]), "i"(ao), "i"(ao + 15) : W4_ACLOB, W4_ACLOB_HI);
    else                             asm volatile("v_mfma_f32_32x32x16_bf16 a[%c2:%c3], %0, %1, a[%c2:%c3]" :: "v"(wf), "v"(p.xf[T & 1][4 * qb + ks]), "i"(ao), "i"(ao + 15) : W4_ACLOB, W4_ACLOB_HI);
  }
  if constexpr (M + PLEAD < 64) w_read<T, ((M + PLEAD) >> 1), ((M + PLEAD) & 1)>(p);
  else if constexpr (T < 7)     w_read<T + 1, ((M + PLEAD - 64) >> 1), ((M + PLEAD - 64) & 1)>(p);
  if constexpr (M < 8 && T < 7) x_load<T + 1, M>(p);
  if constexpr (M == 32 && T < 7) asm volatile("s_waitcnt vmcnt(8)\n\ts_barrier" ::: "memory");
  if constexpr (M >= 33 && M < 41) {
    if constexpr (T < 6) w_dma<T + 2, M - 33>(p);
    else if constexpr (T == 6) kv_dma0<0, M - 33>(c);
    else if constexpr (M < 37) kv_dma0<1, M - 33>(c);
  }
  GAP();
}
template <int T, int... Ms> DI void tileP_(PC& p, W4C& c, iseq<Ms...>) { (gapP<T, Ms>(p, c), ...); }
template <int... Ts> DI void tilesP(PC& p, W4C& c, iseq<Ts...>) { (tileP_<Ts>(p, c, make_iseq<64>{}), ...); }
template <int T, int... Qs> DI void w_dmas(PC& p, iseq<Qs...>) { (w_dma<T, Qs>(p), ...); }
template <int T, int... Is> DI void x_loads(PC& p, iseq<Is...>) { (x_load<T, Is>(p), ...); }
template <int... Is> DI void w_reads0(PC& p, iseq<Is...>) { (w_read<0, (Is >> 1), (Is & 1)>(p), ...); }

DI float gelu_tanh(float g) {
  const float z2 = g * (2.0f * 0.7978845608028654f * 1.4426950408889634f) * (1.0f + 0.044715f * g * g);
  return g * __builtin_amdgcn_rcpf(1.0f + __builtin_amdgcn_exp2f(-z2));
}
template <int QB, int B2, int SS, int... Js> DI u32x4 geglu8(iseq<Js...>) {
  float g[8] = {acc_rd<16 * (2 * (4 + B2) + QB) + 8 * SS + Js>()...};
  float u[8] = {acc_rd<16 * (2 * (6 + B2) + QB) + 8 * SS + Js>()...};
  u32x4 w;
  #pragma unroll
  for (int e = 0; e < 4; ++e) w[e] = cvtpk(gelu_tanh(g[2 * e]) * u[2 * e], gelu_tanh(g[2 * e + 1]) * u[2 * e + 1]);
  return w;
}
template <int I> DI void geglu_store(char* hp, bool st) {
  constexpr int qb = I >> 2, b2 = (I >> 1) & 1, ss = I & 1;
  const u32x4 w = geglu8<qb, b2, ss>(make_iseq<8>{});
  if (st) *(u32x4*)(hp + (size_t)qb * 32 * (DF * 2) + 64 * b2 + 32 * ss) = w;
}
template <int... Is> DI void geglu_stores(char* hp, bool st, iseq<Is...>) { (geglu_store<Is>(hp, st), ...); }
template <int I, int... Js> DI u32x4 qpack8(float sc, iseq<Js...>) {
  constexpr int qb = I >> 3, d0 = I & 7;
  float q[8] = {acc_rd<16 * (2 * (d0 >> 1) + qb) + 8 * (d0 & 1) + Js>()...};
  u32x4 w;
  #pragma unroll
  for (int e = 0; e < 4; ++e) w[e] = cvtpk(fmul_s(q[2 * e], sc), fmul_s(q[2 * e + 1], sc));
  return w;
}
template <int I> DI void q_write1(const u32x4& w) {
  constexpr int aq = AQ_(I >> 3, I & 7);
  asm volatile("v_accvgpr_write_b32 a[%c4], %0\n\tv_accvgpr_write_b32 a[%c5], %1\n\tv_accvgpr_write_b32 a[%c6], %2\n\tv_accvgpr_write_b32 a[%c7], %3"
               :: "v"(w[0]), "v"(w[1]), "v"(w[2]), "v"(w[3]), "i"(aq), "i"(aq + 1), "i"(aq + 2), "i"(aq + 3) : W4_ACLOB_HI);
}
template <int... Is> DI void q_convert(float sc, iseq<Is...>) { (q_write1<Is>(qpack8<Is>(sc, make_iseq<8>{})), ...); }
template <int DB, int QB, int... Rs> DI void o_read(float (&o)[16], iseq<Rs...>) { ((o[Rs] = acc_rd<AO_(DB, QB) + Rs>()), ...); }

extern "C" __global__ __launch_bounds__(256, 1)
void k_attn(const unsigned short* __restrict__ XN, const unsigned short* __restrict__ Kc, const unsigned short* __restrict__ Vc,
            const unsigned short* __restrict__ Wq, const unsigned short* __restrict__ Wg, const unsigned short* __restrict__ Wu,
            unsigned short* __restrict__ Ob, unsigned short* __restrict__ Hb) {
  extern __shared__ __attribute__((aligned(16))) char lds[];
  const unsigned tid = threadIdx.x, lane = tid & 63u, wid = tid >> 6, l32 = lane & 31u, hi = lane >> 5;
  constexpr int T = NT;
  const float C = 0.12751743082f;
  const unsigned blk = blockIdx.x;
  const unsigned h = (blk & 7u) * 4u + ((blk >> 3) & 3u), y = blk >> 5;
  const unsigned row0 = 256u * y + 64u * rfl(wid);
  W4C c;
  c.ldsb = (unsigned)(size_t)lds;
  #pragma unroll
  for (int j = 0; j < 4; ++j) c.vkb[j] = c.ldsb + KRING + 256u * l32 + 16u * ((2u * j + hi) ^ (l32 & 7u));
  c.vvb = c.ldsb + VRING + (((lane & 3u) << 3) | (((lane >> 2) & 3u) << 6) | (((lane >> 4) & 1u) << 5) | (hi << 8));
  #pragma unroll
  for (int par = 0; par < 2; ++par) c.vksrc[par] = KRS * (lane >> 4) + 16u * ((lane & 15u) ^ (4u * par + (lane >> 4)));
  c.vvsrc = KRS * ((lane >> 2) & 7u) + 64u * hi + 16u * (lane & 3u);
  c.m0k = rfl(c.ldsb + KRING + 4096u * wid); c.m0v = rfl(c.ldsb + VRING + 4096u * wid);
  c.sofk = rfl(KRS * 16u * wid); c.sofv = rfl(KRS * 16u * wid);
  c.srdK = __builtin_amdgcn_make_buffer_rsrc((void*)((const char*)Kc + (size_t)h * S * 256), (short)0, S * 256, SRD_WORD3);
  c.srdV = __builtin_amdgcn_make_buffer_rsrc((void*)((const char*)Vc + (size_t)h * S * 256), (short)0, S * 256, SRD_WORD3);

  {
    PC p;
    const unsigned jf = h < 28u ? h : h - 28u;
    const unsigned sw = ((lane & 1u) << 1) | ((lane >> 1) & 1u);
    p.vwb = c.ldsb + WRING + 2048u * hi + 64u * ((lane >> 2) & 3u) + 32u * ((lane >> 4) & 1u) + 8u * sw;
    p.vwsq = 8192u * ((lane >> 2) & 7u) + 64u * hi + 16u * (lane & 3u);
    p.vwsg = 3584u * ((lane >> 2) & 7u) + 64u * hi + 16u * (lane & 3u);
    p.vx = 1024u * l32 + 16u * hi;
    p.m0w = rfl(c.ldsb + WRING + 4096u * wid);
    p.sofq = rfl(8192u * 16u * wid); p.sofg = rfl(3584u * 16u * wid);
    p.srdQ = __builtin_amdgcn_make_buffer_rsrc((void*)((const char*)Wq + h * 256u), (short)0, DM * 8192 - (int)(h * 256u), SRD_WORD3);
    p.srdG = __builtin_amdgcn_make_buffer_rsrc((void*)((const char*)Wg + jf * 128u), (short)0, DM * 3584 - (int)(jf * 128u), SRD_WORD3);
    p.srdU = __builtin_amdgcn_make_buffer_rsrc((void*)((const char*)Wu + jf * 128u), (short)0, DM * 3584 - (int)(jf * 128u), SRD_WORD3);
    p.srdX = __builtin_amdgcn_make_buffer_rsrc((void*)((const char*)XN + (size_t)row0 * 1024), (short)0, 64 * 1024, SRD_WORD3);
    asm volatile("s_nop 4" ::: "memory");
    w_dmas<0>(p, make_iseq<8>{}); x_loads<0>(p, make_iseq<8>{}); w_dmas<1>(p, make_iseq<8>{});
    asm volatile("s_waitcnt vmcnt(8)\n\ts_barrier" ::: "memory");
    w_reads0(p, make_iseq<PLEAD>{});
    GAP();
    tilesP(p, c, make_iseq<8>{});
    asm volatile("s_nop 7\n\ts_nop 7" ::: "memory");
    char* hp = (char*)Hb + (size_t)(row0 + l32) * (DF * 2) + jf * 128u + 16u * hi;
    geglu_stores(hp, h < 28u, make_iseq<8>{});
    GAP();
    q_convert(C, make_iseq<16>{});
    GAP();
  }
  asm volatile("s_waitcnt vmcnt(0)\n\ts_barrier" ::: "memory");
  k_reads(c, 0u, make_iseq<16>{});
  asm volatile("s_waitcnt lgkmcnt(0)\n\ts_nop 1" ::: "memory");

  c.mhat[0] = c.mhat[1] = 0.f; c.lsum[0] = c.lsum[1] = 0.f; c.fct[0] = c.fct[1] = 1.f; c.pend = false;

  phaseA<0, true>(c, 0);  phaseB<0, true,  false>(c, 0);
  phaseA<1, false>(c, 1); phaseB<1, false, true>(c, 1);
  for (int t = 2; t < T - 2; t += 2) {
    phaseA<0, false>(c, t);     phaseB<0, false, false>(c, t);
    phaseA<1, false>(c, t + 1); phaseB<1, false, false>(c, t + 1);
  }
  phaseA<0, false>(c, T - 2); phaseB<0, false, false, false, true, true>(c, T - 2);
  phaseA<1, false>(c, T - 1); phaseB<1, false, false, false, false, false>(c, T - 1);
  {
    const unsigned vslot = (unsigned)((T - 1) & 1) * 0x4000u;
    tr_reads(c, vslot, make_iseq<32>{});
    #pragma unroll
    for (int e = 0; e < 32; ++e) c.S[1][1][e >> 4][e & 15] = __builtin_amdgcn_exp2f(c.S[1][1][e >> 4][e & 15]);
    packs<1>(c, make_iseq<32>{});
    rowsums<1>(c, iseq<8, 9, 10, 11, 12, 13, 14, 15, 24, 25, 26, 27, 28, 29, 30, 31>{});
    #pragma unroll
    for (int qb = 0; qb < 2; ++qb) { const float rs = fadd_s(c.rs0[qb], c.rs1[qb]);
      auto pr = __builtin_amdgcn_permlane32_swap(__float_as_uint(rs), __float_as_uint(rs), false, false);
      c.lsum[qb] = fadd_s(c.lsum[qb], fadd_s(__uint_as_float(pr[0]), __uint_as_float(pr[1]))); }
    asm volatile("s_waitcnt lgkmcnt(0)" : W4_VLO :: "memory"); asm volatile("" : W4_VUP :: "memory");
    GAP();
    #pragma unroll
    for (int ks = 0; ks < 4; ++ks)
      #pragma unroll
      for (int db = 0; db < 4; ++db) c.Vf[db][ks] = __builtin_shufflevector(c.vlo[db][ks], c.vup[db][ks], 0, 1, 2, 3, 4, 5, 6, 7);
    asm volatile("s_nop 1" ::: "memory");
    pv_drain(c, make_iseq<32>{});
    asm volatile("s_nop 7\n\ts_nop 7" ::: "memory");
  }
  {
    unsigned lb; asm volatile("v_mbcnt_lo_u32_b32 %0, -1, 0\n\tv_mbcnt_hi_u32_b32 %0, -1, %0" : "=v"(lb));
    const unsigned l32b = lb & 31u, hib = lb >> 5;
    char* stg = lds + WRING + 16384u * wid;
    #pragma unroll
    for (int qb = 0; qb < 2; ++qb) {
      const float il = c.lsum[qb] > 0.f ? __builtin_amdgcn_rcpf(c.lsum[qb]) : 0.f;
      const unsigned r = 32u * qb + l32b;
      #pragma unroll
      for (int db = 0; db < 4; ++db) {
        float o[16];
        if (db == 0) { if (qb == 0) o_read<0, 0>(o, make_iseq<16>{}); else o_read<0, 1>(o, make_iseq<16>{}); }
        if (db == 1) { if (qb == 0) o_read<1, 0>(o, make_iseq<16>{}); else o_read<1, 1>(o, make_iseq<16>{}); }
        if (db == 2) { if (qb == 0) o_read<2, 0>(o, make_iseq<16>{}); else o_read<2, 1>(o, make_iseq<16>{}); }
        if (db == 3) { if (qb == 0) o_read<3, 0>(o, make_iseq<16>{}); else o_read<3, 1>(o, make_iseq<16>{}); }
        #pragma unroll
        for (int gq = 0; gq < 4; ++gq) {
          u32x2 w = {cvtpk(fmul_s(o[4 * gq + 0], il), fmul_s(o[4 * gq + 1], il)), cvtpk(fmul_s(o[4 * gq + 2], il), fmul_s(o[4 * gq + 3], il))};
          const unsigned ch = 4u * db + gq;
          *(u32x2*)(stg + 256u * r + 16u * (ch ^ (r & 15u)) + 8u * hib) = w;
        }
      }
    }
    asm volatile("s_waitcnt lgkmcnt(0)" ::: "memory");
    const unsigned cch = lb & 15u, rsb = lb >> 4;
    u32x4 v[16];
    #pragma unroll
    for (int j = 0; j < 16; ++j) { const unsigned r = 4u * j + rsb; v[j] = *(const u32x4*)(stg + 256u * r + 16u * (cch ^ (r & 15u))); }
    char* ob = (char*)Ob + (size_t)row0 * (H * D * 2) + h * 256u;
    #pragma unroll
    for (int j = 0; j < 16; ++j) *(u32x4*)(ob + (size_t)(4u * j + rsb) * (H * D * 2) + 16u * cch) = v[j];
  }
}

constexpr size_t SHM_FIN = 131072;
constexpr int FNT = 23, FDEP = 3;
struct FC {
  unsigned vwb, vws, vao, vah, m0w;
  __amdgpu_buffer_rsrc_t srdWo, srdWd, srdO, srdH;
  unsigned sofw, sofa;
  bf16x4 wl[2][8], wu[2][8];
  u32x4 af[4][8];
  f32x16 acc[2][2][2];
};
template <int I, int Q> DI void f_dma(FC& f) {
  constexpr bool isS = I < 16; constexpr int kt = isS ? I : I - 16;
  asm volatile("s_mov_b32 m0, %0\n\ts_nop 0\n\tbuffer_load_dwordx4 %1, %2, %3 offen lds"
               :: "s"(f.m0w + 8192u * (I & 3) + 1024u * Q), "v"(f.vws), "s"(isS ? f.srdWo : f.srdWd), "s"(f.sofw + 1024u * 256u * kt + 1024u * 8u * Q) : "m0", "memory");
}
template <int I, int J> DI void f_aload(FC& f) {
  constexpr bool isS = I < 16; constexpr int kt = isS ? I : I - 16, mb = J >> 2, ks = J & 3;
  if constexpr (isS) asm volatile("buffer_load_dwordx4 %0, %1, %2, %3 offen offset:%4" : "=v"(f.af[I & 3][J]) : "v"(f.vao), "s"(f.srdO), "s"(f.sofa + 512u * kt + 8192u * 32u * mb), "i"(32 * ks) : "memory");
  else               asm volatile("buffer_load_dwordx4 %0, %1, %2, %3 offen offset:%4" : "=v"(f.af[I & 3][J]) : "v"(f.vah), "s"(f.srdH), "s"(f.sofa + 512u * kt + 3584u * 32u * mb), "i"(32 * ks) : "memory");
}
template <int I, int R> DI void f_wread(FC& f) {
  constexpr int ks = R >> 2, nb = (R >> 1) & 1, off = 8192 * (I & 3) + 2048 * ks + 512 * nb + 256 * (R & 1);
  if constexpr ((R & 1) == 0) asm volatile("ds_read_b64_tr_b16 %0, %1 offset:%2" : "=v"(f.wl[I & 1][R >> 1]) : "v"(f.vwb), "i"(off) : "memory");
  else                        asm volatile("ds_read_b64_tr_b16 %0, %1 offset:%2" : "=v"(f.wu[I & 1][R >> 1]) : "v"(f.vwb), "i"(off) : "memory");
}
template <int I, int... Qs> DI void f_issue(FC& f, iseq<Qs...>) { if constexpr (I < FNT) { (f_dma<I, Qs>(f), ...); (f_aload<I, Qs>(f), ...); } }
template <int I, int... Rs> DI void f_wreads(FC& f, iseq<Rs...>) { if constexpr (I < FNT) (f_wread<I, Rs>(f), ...); }
template <int I> DI void f_waitv(FC& f) {
  if constexpr (I < FNT) {
    constexpr int last = (I + FDEP - 1 < FNT - 1) ? I + FDEP - 1 : FNT - 1;
    constexpr int n = 16 * (last - I);
    asm volatile("s_waitcnt vmcnt(%c8)" : "+v"(f.af[I & 3][0]), "+v"(f.af[I & 3][1]), "+v"(f.af[I & 3][2]), "+v"(f.af[I & 3][3]),
                                          "+v"(f.af[I & 3][4]), "+v"(f.af[I & 3][5]), "+v"(f.af[I & 3][6]), "+v"(f.af[I & 3][7]) : "i"(n) : "memory");
  }
}
template <int I> DI void f_tile(FC& f) {
  asm volatile("s_waitcnt lgkmcnt(0)" : "+v"(f.wl[I & 1][0]), "+v"(f.wl[I & 1][1]), "+v"(f.wl[I & 1][2]), "+v"(f.wl[I & 1][3]), "+v"(f.wl[I & 1][4]), "+v"(f.wl[I & 1][5]), "+v"(f.wl[I & 1][6]), "+v"(f.wl[I & 1][7]),
                                        "+v"(f.wu[I & 1][0]), "+v"(f.wu[I & 1][1]), "+v"(f.wu[I & 1][2]), "+v"(f.wu[I & 1][3]), "+v"(f.wu[I & 1][4]), "+v"(f.wu[I & 1][5]), "+v"(f.wu[I & 1][6]), "+v"(f.wu[I & 1][7]) :: "memory");
  f_issue<I + FDEP>(f, make_iseq<8>{});
  {
    constexpr int I1 = I + 1;
    if constexpr (I1 < FNT) {
      constexpr int last = (I + FDEP < FNT - 1) ? I + FDEP : FNT - 1;
      constexpr int n = 16 * (last - I1);
      asm volatile("s_waitcnt vmcnt(%c8)" : "+v"(f.af[I1 & 3][0]), "+v"(f.af[I1 & 3][1]), "+v"(f.af[I1 & 3][2]), "+v"(f.af[I1 & 3][3]),
                                            "+v"(f.af[I1 & 3][4]), "+v"(f.af[I1 & 3][5]), "+v"(f.af[I1 & 3][6]), "+v"(f.af[I1 & 3][7]) : "i"(n) : "memory");
      f_wreads<I1>(f, make_iseq<16>{});
    }
  }
  GAP();
  constexpr int a = I < 16 ? 0 : 1;
  #pragma unroll
  for (int ks = 0; ks < 4; ++ks)
    #pragma unroll
    for (int nb = 0; nb < 2; ++nb) {
      const bf16x8 wf = __builtin_shufflevector(f.wl[I & 1][2 * ks + nb], f.wu[I & 1][2 * ks + nb], 0, 1, 2, 3, 4, 5, 6, 7);
      #pragma unroll
      for (int mb = 0; mb < 2; ++mb)
        f.acc[a][nb][mb] = __builtin_amdgcn_mfma_f32_32x32x16_bf16(wf, __builtin_bit_cast(bf16x8, f.af[I & 3][4 * mb + ks]), f.acc[a][nb][mb], 0, 0, 0);
    }
  GAP();
}
template <int... Is> DI void f_tiles(FC& f, iseq<Is...>) { (f_tile<Is>(f), ...); }

extern "C" __global__ __launch_bounds__(256, 1)
void k_final(const unsigned short* __restrict__ X, const unsigned short* __restrict__ Ob, const unsigned short* __restrict__ Hb,
             const unsigned short* __restrict__ Wo, const unsigned short* __restrict__ Wd, unsigned short* __restrict__ Out) {
  extern __shared__ __attribute__((aligned(16))) char lds[];
  const unsigned tid = threadIdx.x, lane = tid & 63u, wid = rfl(tid >> 6), l32 = lane & 31u, hi = lane >> 5;
  const unsigned blk = blockIdx.x;
  const unsigned mt = (blk & 7u) * 4u + ((blk >> 3) & 3u), nt = blk >> 5;
  const unsigned m0 = 64u * mt, n0 = 64u * nt;
  const unsigned ldsb = (unsigned)(size_t)lds;
  FC f;
  const unsigned sw = ((lane & 1u) << 1) | ((lane >> 1) & 1u);
  f.vwb = ldsb + 32768u * wid + 1024u * hi + 64u * ((lane >> 2) & 3u) + 32u * ((lane >> 4) & 1u) + 8u * sw;
  f.vws = 1024u * ((lane >> 2) & 7u) + 64u * hi + 16u * (lane & 3u);
  f.vao = 8192u * l32 + 16u * hi; f.vah = 3584u * l32 + 16u * hi;
  f.m0w = rfl(ldsb + 32768u * wid);
  f.sofw = 1024u * 64u * wid; f.sofa = 128u * wid;
  f.srdWo = __builtin_amdgcn_make_buffer_rsrc((void*)((const char*)Wo + n0 * 2u), (short)0, H * D * 1024 - (int)(n0 * 2u), SRD_WORD3);
  f.srdWd = __builtin_amdgcn_make_buffer_rsrc((void*)((const char*)Wd + n0 * 2u), (short)0, DF * 1024 - (int)(n0 * 2u), SRD_WORD3);
  f.srdO = __builtin_amdgcn_make_buffer_rsrc((void*)((const char*)Ob + (size_t)m0 * 8192), (short)0, 64 * 8192, SRD_WORD3);
  f.srdH = __builtin_amdgcn_make_buffer_rsrc((void*)((const char*)Hb + (size_t)m0 * 3584), (short)0, 64 * 3584, SRD_WORD3);
  #pragma unroll
  for (int a = 0; a < 2; ++a)
    #pragma unroll
    for (int nb = 0; nb < 2; ++nb)
      #pragma unroll
      for (int mb = 0; mb < 2; ++mb) f.acc[a][nb][mb] = f32x16{};
  asm volatile("s_nop 4" ::: "memory");
  f_issue<0>(f, make_iseq<8>{}); f_issue<1>(f, make_iseq<8>{}); f_issue<2>(f, make_iseq<8>{});
  f_waitv<0>(f);
  f_wreads<0>(f, make_iseq<16>{});
  GAP();
  f_tiles(f, make_iseq<FNT>{});
  __syncthreads();
  #pragma unroll
  for (int a = 0; a < 2; ++a)
    #pragma unroll
    for (int nb = 0; nb < 2; ++nb)
      #pragma unroll
      for (int mb = 0; mb < 2; ++mb)
        #pragma unroll
        for (int r4 = 0; r4 < 4; ++r4) {
          f32x4 v = {f.acc[a][nb][mb][4 * r4], f.acc[a][nb][mb][4 * r4 + 1], f.acc[a][nb][mb][4 * r4 + 2], f.acc[a][nb][mb][4 * r4 + 3]};
          *(f32x4*)(lds + 32768u * wid + 4096u * (4 * a + 2 * nb + mb) + 1024u * r4 + 16u * lane) = v;
        }
  __syncthreads();
  const unsigned nb = wid >> 1, mb = wid & 1u;
  f32x4 s[4], g[4];
  #pragma unroll
  for (int r4 = 0; r4 < 4; ++r4) {
    s[r4] = f32x4{}; g[r4] = f32x4{};
    #pragma unroll
    for (int w = 0; w < 4; ++w) {
      s[r4] += *(const f32x4*)(lds + 32768u * w + 4096u * (2u * nb + mb) + 1024u * r4 + 16u * lane);
      g[r4] += *(const f32x4*)(lds + 32768u * w + 4096u * (4u + 2u * nb + mb) + 1024u * r4 + 16u * lane);
    }
  }
  #pragma unroll
  for (int ss = 0; ss < 2; ++ss) {
    const size_t off = (size_t)(m0 + 32u * mb + l32) * DM + n0 + 32u * nb + 16u * ss + 8u * hi;
    const u32x4 xw = *(const u32x4*)(X + off);
    float o[8];
    #pragma unroll
    for (int e = 0; e < 8; ++e) {
      const float sv = s[2 * ss + (e >> 2)][e & 3], fv = g[2 * ss + (e >> 2)][e & 3];
      const float gate = fv * __builtin_amdgcn_rcpf(1.0f + __builtin_amdgcn_exp2f(-fv * 1.4426950408889634f));
      const float xv = (e & 1) ? bfhi(xw[e >> 1]) : bflo(xw[e >> 1]);
      o[e] = xv + sv * gate;
    }
    u32x4 w;
    #pragma unroll
    for (int e = 0; e < 4; ++e) w[e] = cvtpk(o[2 * e], o[2 * e + 1]);
    *(u32x4*)(Out + off) = w;
  }
}

extern "C" void kernel_launch(void* const* d_in, const int* in_sizes, int n_in,
                              void* d_out, int out_size, void* d_ws, size_t ws_size,
                              hipStream_t stream) {
  const unsigned short* x  = (const unsigned short*)d_in[0];
  const unsigned short* kc = (const unsigned short*)d_in[1];
  const unsigned short* vc = (const unsigned short*)d_in[2];
  const unsigned short* wq = (const unsigned short*)d_in[3];
  const unsigned short* wo = (const unsigned short*)d_in[4];
  const unsigned short* wg = (const unsigned short*)d_in[6];
  const unsigned short* wu = (const unsigned short*)d_in[7];
  const unsigned short* wd = (const unsigned short*)d_in[8];
  unsigned short* xn = (unsigned short*)((char*)d_ws + WS_XN);
  unsigned short* ob = (unsigned short*)((char*)d_ws + WS_O);
  unsigned short* hb = (unsigned short*)((char*)d_ws + WS_H);
  (void)hipFuncSetAttribute((const void*)k_attn, hipFuncAttributeMaxDynamicSharedMemorySize, (int)SHM_ATTN);
  (void)hipFuncSetAttribute((const void*)k_final, hipFuncAttributeMaxDynamicSharedMemorySize, (int)SHM_FIN);
  hipLaunchKernelGGL(k_ln, dim3(S / 4), dim3(256), 0, stream, x, xn);
  hipLaunchKernelGGL(k_attn, dim3(256), dim3(256), SHM_ATTN, stream, xn, kc, vc, wq, wg, wu, ob, hb);
  hipLaunchKernelGGL(k_final, dim3(256), dim3(256), SHM_FIN, stream, x, ob, hb, wo, wd, (unsigned short*)d_out);
}
```

```cpp
#include <hip/hip_runtime.h>
#include <hip/hip_bf16.h>
#include <utility>

constexpr int S = 2048, H = 32, D = 128, DM = 512, DF = 1792, NT = S / 64;
constexpr float THR = 30.0f, MARG = 30.0f;

typedef float    f32x16 __attribute__((ext_vector_type(16)));
typedef float    f32x2  __attribute__((ext_vector_type(2)));
typedef float    f32x4  __attribute__((ext_vector_type(4)));
typedef __bf16   bf16x8 __attribute__((ext_vector_type(8)));
typedef __bf16   bf16x4 __attribute__((ext_vector_type(4)));
typedef __bf16   bf16x2 __attribute__((ext_vector_type(2)));
typedef unsigned u32x4  __attribute__((ext_vector_type(4)));
typedef unsigned u32x2  __attribute__((ext_vector_type(2)));
template <int... Is> using iseq = std::integer_sequence<int, Is...>;
template <int N> using make_iseq = std::make_integer_sequence<int, N>;

#define DI __device__ __forceinline__
#define GAP() __builtin_amdgcn_sched_barrier(0)
#define SRD_WORD3 0x00020000

constexpr size_t WS_XN = 0, WS_O = (size_t)S * DM * 2, WS_H = WS_O + (size_t)S * H * D * 2, WS_WT = WS_H + (size_t)S * DF * 2, WS_END = WS_WT + (size_t)(H * D + DF) * DM * 2;

DI unsigned rfl(unsigned x) { return __builtin_amdgcn_readfirstlane(x); }
DI unsigned cvtpk(float lo, float hi) {
  f32x2 v = {lo, hi}; bf16x2 b = __builtin_convertvector(v, bf16x2); return __builtin_bit_cast(unsigned, b);
}
DI float bflo(unsigned w) { return __uint_as_float(w << 16); }
DI float bfhi(unsigned w) { return __uint_as_float(w & 0xffff0000u); }
DI float max3f(float a, float b, float c) { float r; asm("v_max3_f32 %0, %1, %2, %3" : "=v"(r) : "v"(a), "v"(b), "v"(c)); return r; }
DI float max2f(float a, float b) { float r; asm("v_max_f32_e32 %0, %1, %2" : "=v"(r) : "v"(a), "v"(b)); return r; }
DI float fadd_s(float a, float b) { float r; asm("v_add_f32_e32 %0, %1, %2" : "=v"(r) : "v"(a), "v"(b)); return r; }
DI float fmul_s(float a, float b) { float r; asm("v_mul_f32_e32 %0, %1, %2" : "=v"(r) : "v"(a), "v"(b)); return r; }
DI float fsub_s(float a, float b) { float r; asm("v_sub_f32_e32 %0, %1, %2" : "=v"(r) : "v"(a), "v"(b)); return r; }

#define STAMP(i) do {} while (0)
__global__ __launch_bounds__(64) void k_ln(const unsigned short* __restrict__ x, unsigned short* __restrict__ xn) {
  const int lane = threadIdx.x, row = blockIdx.x * 8 + (lane >> 3), c0 = lane & 7;
  u32x4 w[8];
#pragma unroll
  for (int j = 0; j < 8; ++j) w[j] = *(const u32x4*)(x + (size_t)row * DM + (c0 + 8 * j) * 8);
  float s = 0.f;
#pragma unroll
  for (int j = 0; j < 8; ++j)
#pragma unroll
    for (int e = 0; e < 4; ++e) s += bflo(w[j][e]) + bfhi(w[j][e]);
#pragma unroll
  for (int o = 4; o >= 1; o >>= 1) s += __shfl_xor(s, o);
  const float m = s * (1.0f / DM);
  float q = 0.f;
#pragma unroll
  for (int j = 0; j < 8; ++j)
#pragma unroll
    for (int e = 0; e < 4; ++e) { const float a = bflo(w[j][e]) - m, b = bfhi(w[j][e]) - m; q += a * a + b * b; }
#pragma unroll
  for (int o = 4; o >= 1; o >>= 1) q += __shfl_xor(q, o);
  const float r = 1.0f / sqrtf(q * (1.0f / DM) + 1e-5f);
  char* dst = (char*)xn + (size_t)(row >> 5) * 32768 + (row & 31) * 16;
#pragma unroll
  for (int j = 0; j < 8; ++j) {
    u32x4 o4;
#pragma unroll
    for (int e = 0; e < 4; ++e) o4[e] = cvtpk((bflo(w[j][e]) - m) * r, (bfhi(w[j][e]) - m) * r);
    const int ch = c0 + 8 * j;
    *(u32x4*)(dst + (ch >> 1) * 1024 + (ch & 1) * 512) = o4;
  }
}

constexpr unsigned KRS = 256u, KTILEB = 64u * KRS;
constexpr size_t SHM_ATTN = 163840;
#define KRING 0x0000u
#define VRING 0x8000u
#define WRING 0x10000u
#define W4_ACLOB_HI "a128","a129","a130","a131","a132","a133","a134","a135","a136","a137","a138","a139","a140","a141","a142","a143","a144","a145","a146","a147","a148","a149","a150","a151","a152","a153","a154","a155","a156","a157","a158","a159","a160","a161","a162","a163","a164","a165","a166","a167","a168","a169","a170","a171","a172","a173","a174","a175","a176","a177","a178","a179","a180","a181","a182","a183","a184","a185","a186","a187","a188","a189","a190","a191","a192","a193","a194","a195","a196","a197","a198","a199","a200","a201","a202","a203","a204","a205","a206","a207","a208","a209","a210","a211","a212","a213","a214","a215","a216","a217","a218","a219","a220","a221","a222","a223","a224","a225","a226","a227","a228","a229","a230","a231","a232","a233","a234","a235","a236","a237","a238","a239","a240","a241","a242","a243","a244","a245","a246","a247","a248","a249","a250","a251","a252","a253","a254","a255"
#define W4_ACLOB "a0","a1","a2","a3","a4","a5","a6","a7","a8","a9","a10","a11","a12","a13","a14","a15","a16","a17","a18","a19","a20","a21","a22","a23","a24","a25","a26","a27","a28","a29","a30","a31","a32","a33","a34","a35","a36","a37","a38","a39","a40","a41","a42","a43","a44","a45","a46","a47","a48","a49","a50","a51","a52","a53","a54","a55","a56","a57","a58","a59","a60","a61","a62","a63","a64","a65","a66","a67","a68","a69","a70","a71","a72","a73","a74","a75","a76","a77","a78","a79","a80","a81","a82","a83","a84","a85","a86","a87","a88","a89","a90","a91","a92","a93","a94","a95","a96","a97","a98","a99","a100","a101","a102","a103","a104","a105","a106","a107","a108","a109","a110","a111","a112","a113","a114","a115","a116","a117","a118","a119","a120","a121","a122","a123","a124","a125","a126","a127"

template <int R> DI float acc_rd() { float r; asm volatile("v_accvgpr_read_b32 %0, a[%c1]" : "=v"(r) : "i"(R)); return r; }
template <int R> DI void acc_wr(unsigned v) { asm volatile("v_accvgpr_write_b32 a[%c1], %0" :: "v"(v), "i"(R) : W4_ACLOB_HI); }
template <int R> DI void acc_scale(float f) { float t; asm volatile("v_accvgpr_read_b32 %0, a[%c2]\n\tv_mul_f32 %0, %1, %0\n\tv_accvgpr_write_b32 a[%c2], %0" : "=&v"(t) : "v"(f), "i"(R) : W4_ACLOB); }

DI float w4_aone() { unsigned l; asm volatile("v_mbcnt_lo_u32_b32 %0, -1, 0\n\tv_mbcnt_hi_u32_b32 %0, -1, %0" : "=v"(l)); return l < 32u ? 1.f : 0.f; }
DI void negm_bcast(f32x16& negm, float aone, float neg_mhat) {
  asm volatile("s_nop 1\n\tv_mfma_f32_32x32x2_f32 %0, %1, %2, 0" : "+v"(negm) : "v"(aone), "v"(neg_mhat));
}

struct W4C {
  unsigned vkb[4], vvb, vksrc[2], vvsrc, m0k, m0v, sofk, sofv, ldsb;
  __amdgpu_buffer_rsrc_t srdK, srdV;
  f32x16 S[2][2][2];
  f32x16 NEGM[2];
  bf16x8 Vf[4][4];
  bf16x8 Pk[2][4];
  bf16x4 vlo[4][4], vup[4][4];
  float mhat[2], lsum[2], fct[2]; bool pend;
  float rs0[2], rs1[2], pmx0[2], pmx1[2], rmax[2], delta[2];
  unsigned pm0[8], psf[8];
};

constexpr int AO_(int db, int qb) { return 16 * (2 * db + qb); }
constexpr int AQ_(int qb, int d0) { return 128 + 32 * qb + 4 * d0; }
constexpr int AK_(int kb, int d0) { return 192 + 32 * kb + 4 * d0; }

template <int I> DI void tr_read(W4C& c, unsigned vslot) {
  constexpr int ks = I >> 3, db = (I >> 1) & 3;
  if constexpr ((I & 1) == 0) asm volatile("ds_read_b64_tr_b16 %0, %1 offset:%2" : "=v"(c.vlo[db][ks]) : "v"(c.vvb + vslot), "i"(4096 * ks + 512 * db) : "memory");
  else                        asm volatile("ds_read_b64_tr_b16 %0, %1 offset:%2" : "=v"(c.vup[db][ks]) : "v"(c.vvb + vslot), "i"(4096 * ks + 2048 + 512 * db) : "memory");
}
template <int I> DI void k_read(W4C& c, unsigned slot) {
  constexpr int kb = I >> 3, d0 = I & 7;
  asm volatile("ds_read_b128 a[%c1:%c2], %0 offset:%c3" :: "v"(c.vkb[d0 & 3] + slot), "i"(AK_(kb, d0)), "i"(AK_(kb, d0) + 3), "i"(8192 * kb + 128 * (d0 >> 2)) : "memory", W4_ACLOB_HI);
}
template <int X, bool FIRST, int M> DI void qk_mfma(W4C& c) {
  constexpr int d0 = M >> 2, kb = (M >> 1) & 1, qb = M & 1, ak = AK_(kb, d0), aq = AQ_(qb, d0);
  if constexpr (d0 == 0) {
    if constexpr (FIRST) asm volatile("v_mfma_f32_32x32x16_bf16 %0, a[%c1:%c2], a[%c3:%c4], 0" : "=&v"(c.S[X][kb][qb]) : "i"(ak), "i"(ak + 3), "i"(aq), "i"(aq + 3) : W4_ACLOB, W4_ACLOB_HI);
    else                 asm volatile("v_mfma_f32_32x32x16_bf16 %0, a[%c2:%c3], a[%c4:%c5], %1" : "=&v"(c.S[X][kb][qb]) : "v"(c.NEGM[qb]), "i"(ak), "i"(ak + 3), "i"(aq), "i"(aq + 3) : W4_ACLOB, W4_ACLOB_HI);
  } else asm volatile("v_mfma_f32_32x32x16_bf16 %0, a[%c1:%c2], a[%c3:%c4], %0" : "+v"(c.S[X][kb][qb]) : "i"(ak), "i"(ak + 3), "i"(aq), "i"(aq + 3) : W4_ACLOB, W4_ACLOB_HI);
}
template <int M, bool ZERO> DI void pv_mfma(W4C& c) {
  constexpr int ks = M >> 3, db = (M >> 1) & 3, qb = M & 1, ao = AO_(db, qb);
  if constexpr (ZERO && ks == 0) asm volatile("v_mfma_f32_32x32x16_bf16 a[%c2:%c3], %0, %1, 0" :: "v"(c.Vf[db][ks]), "v"(c.Pk[qb][ks]), "i"(ao), "i"(ao + 15) : W4_ACLOB);
  else                         asm volatile("v_mfma_f32_32x32x16_bf16 a[%c2:%c3], %0, %1, a[%c2:%c3]" :: "v"(c.Vf[db][ks]), "v"(c.Pk[qb][ks]), "i"(ao), "i"(ao + 15) : W4_ACLOB);
}
template <int X, int QB, int KS, int P> DI void pack1(W4C& c) {
  u32x4 w = __builtin_bit_cast(u32x4, c.Pk[QB][KS]);
  w[P] = cvtpk(c.S[X][KS >> 1][QB][8 * (KS & 1) + 2 * P], c.S[X][KS >> 1][QB][8 * (KS & 1) + 2 * P + 1]);
  c.Pk[QB][KS] = __builtin_bit_cast(bf16x8, w);
}
template <int X, int J> DI void rowsum1(W4C& c) {
  constexpr int qb = J >> 4, ks = (J >> 2) & 3, p = J & 3;
  c.rs0[qb] = fadd_s(c.rs0[qb], c.S[X][ks >> 1][qb][8 * (ks & 1) + 2 * p]); c.rs1[qb] = fadd_s(c.rs1[qb], c.S[X][ks >> 1][qb][8 * (ks & 1) + 2 * p + 1]);
}

template <int X, bool FIRST, int M> DI void gapA(W4C& c, unsigned vslot, const int t) {
  constexpr int Xp = X ^ 1;
  qk_mfma<X, FIRST, M>(c);
  if constexpr (M >= 16 && M <= 31) {
    const int tk = t + 2, tv = t + 1;
    constexpr int q = (M - 16) >> 1;
    if constexpr (((M - 16) & 1) == 0) {
      if constexpr (q < 4) c.pm0[q] = c.m0k + (unsigned)((t + 2) & 1) * 0x4000u + 1024u * q; else c.pm0[q] = c.m0v + (unsigned)((t + 1) & 1) * 0x4000u + 1024u * (q - 4);
      asm volatile("" : "+s"(c.pm0[q]));
    } else {
      if constexpr (q < 4) c.psf[q] = KTILEB * (unsigned)tk + c.sofk + KRS * 4u * q; else c.psf[q] = KTILEB * (unsigned)tv + c.sofv + KRS * 8u * ((q - 4) >> 1) + 128u * ((q - 4) & 1);
      asm volatile("" : "+s"(c.psf[q]));
    }
  }
  if constexpr (M == 31) asm volatile("s_nop 4" : "+s"(c.pm0[0]), "+s"(c.pm0[1]), "+s"(c.pm0[2]), "+s"(c.pm0[3]), "+s"(c.pm0[4]), "+s"(c.pm0[5]), "+s"(c.pm0[6]), "+s"(c.pm0[7]),
                                                  "+s"(c.psf[0]), "+s"(c.psf[1]), "+s"(c.psf[2]), "+s"(c.psf[3]), "+s"(c.psf[4]), "+s"(c.psf[5]), "+s"(c.psf[6]), "+s"(c.psf[7]));
  if constexpr (!FIRST && M == 8) asm volatile("" :: "v"(c.NEGM[0]), "v"(c.NEGM[1]));
  if constexpr (!FIRST) {
    constexpr int base = M + (M >= 4) + (M >= 8) + (M >= 12) + (M >= 16) + (M >= 20) + (M >= 24);
    if constexpr (M <= 25) tr_read<base>(c, vslot);
    if constexpr (M == 3 || M == 7 || M == 11 || M == 15 || M == 19 || M == 23) tr_read<base + 1>(c, vslot);
    if constexpr (M < 16) {
      constexpr int e0 = 2 * M, e1 = 2 * M + 1;
      c.S[Xp][1][e0 >> 4][e0 & 15] = __builtin_amdgcn_exp2f(c.S[Xp][1][e0 >> 4][e0 & 15]);
      c.S[Xp][1][e1 >> 4][e1 & 15] = __builtin_amdgcn_exp2f(c.S[Xp][1][e1 >> 4][e1 & 15]);
      pack1<Xp, (M >> 3), ((M >> 2) & 1), (M & 3)>(c);
    } else {
      constexpr int cc = M - 16;
      pack1<Xp, (cc >> 3), 2 + ((cc >> 2) & 1), (cc & 3)>(c);
      rowsum1<Xp, 16 * (cc >> 3) + 8 + 4 * ((cc >> 2) & 1) + (cc & 3)>(c);
    }
  }
  GAP();
}
#define W4_VLO "+v"(c.vlo[0][0]), "+v"(c.vlo[0][1]), "+v"(c.vlo[0][2]), "+v"(c.vlo[0][3]), "+v"(c.vlo[1][0]), "+v"(c.vlo[1][1]), "+v"(c.vlo[1][2]), "+v"(c.vlo[1][3]), \
               "+v"(c.vlo[2][0]), "+v"(c.vlo[2][1]), "+v"(c.vlo[2][2]), "+v"(c.vlo[2][3]), "+v"(c.vlo[3][0]), "+v"(c.vlo[3][1]), "+v"(c.vlo[3][2]), "+v"(c.vlo[3][3])
#define W4_VUP "+v"(c.vup[0][0]), "+v"(c.vup[0][1]), "+v"(c.vup[0][2]), "+v"(c.vup[0][3]), "+v"(c.vup[1][0]), "+v"(c.vup[1][1]), "+v"(c.vup[1][2]), "+v"(c.vup[1][3]), \
               "+v"(c.vup[2][0]), "+v"(c.vup[2][1]), "+v"(c.vup[2][2]), "+v"(c.vup[2][3]), "+v"(c.vup[3][0]), "+v"(c.vup[3][1]), "+v"(c.vup[3][2]), "+v"(c.vup[3][3])
template <int X, bool FIRST, int... Ms> DI void phaseA_(W4C& c, const int t, iseq<Ms...>) {
  const unsigned vslot = (unsigned)((t - 1) & 1) * 0x4000u;
  (gapA<X, FIRST, Ms>(c, vslot, t), ...);
  if constexpr (!FIRST) {
    #pragma unroll
    for (int qb = 0; qb < 2; ++qb) {
      const float rs = fadd_s(c.rs0[qb], c.rs1[qb]);
      auto pr = __builtin_amdgcn_permlane32_swap(__float_as_uint(rs), __float_as_uint(rs), false, false);
      c.lsum[qb] = fadd_s(c.lsum[qb], fadd_s(__uint_as_float(pr[0]), __uint_as_float(pr[1])));
    }
  }
  if constexpr (FIRST) asm volatile("s_waitcnt vmcnt(0) lgkmcnt(0)\n\ts_barrier" ::: "memory");
  else { asm volatile("s_waitcnt vmcnt(0) lgkmcnt(0)" : W4_VLO :: "memory"); asm volatile("s_barrier" : W4_VUP :: "memory"); }
  GAP();
  if constexpr (!FIRST) {
    #pragma unroll
    for (int ks = 0; ks < 4; ++ks)
      #pragma unroll
      for (int db = 0; db < 4; ++db) c.Vf[db][ks] = __builtin_shufflevector(c.vlo[db][ks], c.vup[db][ks], 0, 1, 2, 3, 4, 5, 6, 7);
  }
}
template <int X, bool FIRST> DI void phaseA(W4C& c, const int t) { phaseA_<X, FIRST>(c, t, make_iseq<32>{}); }

template <int X, bool PEEL, bool ZERO1, bool DK, bool DV, bool KR, int M> DI void gapB(W4C& c, const unsigned kslot_rd) {
  if constexpr (!PEEL) pv_mfma<M, ZERO1>(c);
#define DMA_STRIDE 2
  if constexpr (M % DMA_STRIDE == 0 && M / DMA_STRIDE < 8) {
    constexpr int Q = M / DMA_STRIDE;
    if constexpr (Q < 4 && DK)       asm volatile("s_mov_b32 m0, %0\n\ts_nop 0\n\tbuffer_load_dwordx4 %1, %2, %3 offen lds" :: "s"(c.pm0[Q]), "v"(c.vksrc[Q & 1]), "s"(c.srdK), "s"(c.psf[Q]) : "m0", "memory");
    else if constexpr (Q >= 4 && DV) asm volatile("s_mov_b32 m0, %0\n\ts_nop 0\n\tbuffer_load_dwordx4 %1, %2, %3 offen lds" :: "s"(c.pm0[Q]), "v"(c.vvsrc), "s"(c.srdV), "s"(c.psf[Q]) : "m0", "memory");
  }
  if constexpr (M >= 1 && M <= 16 && KR) k_read<M - 1>(c, kslot_rd);
  if constexpr (M >= 8 && M < 16) {
    constexpr int i = M - 8, q = i >> 2, r = 4 * (i & 3);
    if constexpr ((i & 3) == 0) { c.pmx0[q] = max3f(c.S[X][0][q][0], c.S[X][0][q][1], c.S[X][1][q][0]); c.pmx1[q] = max3f(c.S[X][0][q][2], c.S[X][0][q][3], c.S[X][1][q][1]);
                                  c.pmx0[q] = max3f(c.pmx0[q], c.S[X][1][q][2], c.S[X][1][q][3]); }
    else                        { c.pmx0[q] = max3f(c.pmx0[q], c.S[X][0][q][r], c.S[X][0][q][r + 1]); c.pmx1[q] = max3f(c.pmx1[q], c.S[X][0][q][r + 2], c.S[X][0][q][r + 3]);
                                  c.pmx0[q] = max3f(c.pmx0[q], c.S[X][1][q][r], c.S[X][1][q][r + 1]); c.pmx1[q] = max3f(c.pmx1[q], c.S[X][1][q][r + 2], c.S[X][1][q][r + 3]); }
  }
  if constexpr (M == 16 || M == 17) {
    constexpr int q = M - 16; const float mm = max2f(c.pmx0[q], c.pmx1[q]);
    auto pr = __builtin_amdgcn_permlane32_swap(__float_as_uint(mm), __float_as_uint(mm), false, false);
    c.rmax[q] = max2f(__uint_as_float(pr[0]), __uint_as_float(pr[1]));
  }
  if constexpr (M == 18) {
    if constexpr (PEEL) {
      #pragma unroll
      for (int q = 0; q < 2; ++q) { c.mhat[q] = fadd_s(c.rmax[q], MARG); c.lsum[q] = 0.f; c.fct[q] = 1.f;
        #pragma unroll
        for (int kb = 0; kb < 2; ++kb) { _Pragma("unroll") for (int e = 0; e < 16; ++e) c.S[X][kb][q][e] = fsub_s(c.S[X][kb][q][e], c.mhat[q]); }
        negm_bcast(c.NEGM[q], w4_aone(), -c.mhat[q]);
      }
    } else {
      const bool grew = (c.rmax[0] > THR) | (c.rmax[1] > THR);
      if (__builtin_expect(__any((int)grew), 0)) {
        #pragma unroll
        for (int q = 0; q < 2; ++q) { const float dl = max2f(fadd_s(c.rmax[q], MARG), 0.f); c.delta[q] = dl; c.mhat[q] = fadd_s(c.mhat[q], dl); c.fct[q] = __builtin_amdgcn_exp2f(-dl);
          #pragma unroll
          for (int kb = 0; kb < 2; ++kb) { _Pragma("unroll") for (int e = 0; e < 16; ++e) c.S[X][kb][q][e] = fsub_s(c.S[X][kb][q][e], dl); }
          negm_bcast(c.NEGM[q], w4_aone(), -c.mhat[q]);
        }
        c.pend = true;
      }
    }
  }
  if constexpr (M == 18) { c.rs0[0] = c.rs0[1] = c.rs1[0] = c.rs1[1] = 0.f; }
  if constexpr (M >= 19) {
    constexpr int g = M - 19, e_lo = (32 * g) / 13, e_hi = (32 * (g + 1)) / 13;
    {
      constexpr int p_lo = g ? (32 * (g - 1)) / 13 : 0, p_hi = g ? (32 * g) / 13 : 0;
      if constexpr (p_lo + 0 < p_hi) { constexpr int e = p_lo + 0; if constexpr (e & 1) c.rs1[e >> 4] = fadd_s(c.rs1[e >> 4], c.S[X][0][e >> 4][e & 15]); else c.rs0[e >> 4] = fadd_s(c.rs0[e >> 4], c.S[X][0][e >> 4][e & 15]); }
      if constexpr (p_lo + 1 < p_hi) { constexpr int e = p_lo + 1; if constexpr (e & 1) c.rs1[e >> 4] = fadd_s(c.rs1[e >> 4], c.S[X][0][e >> 4][e & 15]); else c.rs0[e >> 4] = fadd_s(c.rs0[e >> 4], c.S[X][0][e >> 4][e & 15]); }
      if constexpr (p_lo + 2 < p_hi) { constexpr int e = p_lo + 2; if constexpr (e & 1) c.rs1[e >> 4] = fadd_s(c.rs1[e >> 4], c.S[X][0][e >> 4][e & 15]); else c.rs0[e >> 4] = fadd_s(c.rs0[e >> 4], c.S[X][0][e >> 4][e & 15]); }
    }
    if constexpr (e_lo + 0 < e_hi) c.S[X][0][(e_lo + 0) >> 4][(e_lo + 0) & 15] = __builtin_amdgcn_exp2f(c.S[X][0][(e_lo + 0) >> 4][(e_lo + 0) & 15]);
    if constexpr (e_lo + 1 < e_hi) c.S[X][0][(e_lo + 1) >> 4][(e_lo + 1) & 15] = __builtin_amdgcn_exp2f(c.S[X][0][(e_lo + 1) >> 4][(e_lo + 1) & 15]);
    if constexpr (e_lo + 2 < e_hi) c.S[X][0][(e_lo + 2) >> 4][(e_lo + 2) & 15] = __builtin_amdgcn_exp2f(c.S[X][0][(e_lo + 2) >> 4][(e_lo + 2) & 15]);
  }
  if constexpr (M == 31) asm volatile("" :: "v"(c.S[X][0][0]), "v"(c.S[X][0][1]), "v"(c.NEGM[0]), "v"(c.NEGM[1]), "v"(c.rs0[0]), "v"(c.rs0[1]), "v"(c.rs1[0]), "v"(c.rs1[1]));
  GAP();
}
template <int... Rs> DI void rescale_o(float f0, float f1, iseq<Rs...>) { (acc_scale<Rs>(((Rs >> 4) & 1) ? f1 : f0), ...); }
template <int X, bool PEEL, bool ZERO1, bool DK, bool DV, bool KR, int... Ms> DI void phaseB_(W4C& c, const int t, iseq<Ms...>) {
  const unsigned kslot_rd = (unsigned)((t + 1) & 1) * 0x4000u;
  if constexpr (!PEEL) asm volatile("s_nop 1" ::: "memory");
  GAP();
  (gapB<X, PEEL, ZERO1, DK, DV, KR, Ms>(c, kslot_rd), ...);
  {
    #pragma unroll
    for (int e = (32 * 12) / 13; e < 32; ++e) { if (e & 1) c.rs1[e >> 4] = fadd_s(c.rs1[e >> 4], c.S[X][0][e >> 4][e & 15]); else c.rs0[e >> 4] = fadd_s(c.rs0[e >> 4], c.S[X][0][e >> 4][e & 15]); }
  }
  if constexpr (!PEEL) {
    if (__builtin_expect(c.pend, 0)) {
      asm volatile("s_nop 7\n\ts_nop 7" ::: "memory");
      rescale_o(c.fct[0], c.fct[1], make_iseq<128>{});
      asm volatile("s_nop 1" ::: "memory");
      c.lsum[0] = fmul_s(c.lsum[0], c.fct[0]); c.lsum[1] = fmul_s(c.lsum[1], c.fct[1]); c.pend = false;
    }
  }
  asm volatile("s_waitcnt lgkmcnt(0)" ::: "memory");
  GAP();
}
template <int X, bool PEEL, bool ZERO1, bool DK = true, bool DV = true, bool KR = true> DI void phaseB(W4C& c, const int t) { phaseB_<X, PEEL, ZERO1, DK, DV, KR>(c, t, make_iseq<32>{}); }

template <int... Is> DI void k_reads(W4C& c, unsigned slot, iseq<Is...>) { (k_read<Is>(c, slot), ...); }
template <int... Is> DI void tr_reads(W4C& c, unsigned vslot, iseq<Is...>) { (tr_read<Is>(c, vslot), ...); }
template <int X, int... Cs> DI void packs(W4C& c, iseq<Cs...>) { (pack1<X, ((Cs >> 3) & 1), 2 * (Cs >> 4) + ((Cs >> 2) & 1), (Cs & 3)>(c), ...); }
template <int X, int J> DI void rowsum1_padded(W4C& c) { rowsum1<X, J>(c); asm volatile("s_nop 1"); }
template <int X, int... Js> DI void rowsums(W4C& c, iseq<Js...>) { (rowsum1_padded<X, Js>(c), ...); }
template <int M> DI void pv_drain1(W4C& c) { pv_mfma<M, false>(c); GAP(); }
template <int... Ms> DI void pv_drain(W4C& c, iseq<Ms...>) { (pv_drain1<Ms>(c), ...); }

struct PC {
  unsigned vwb, vwsq, vwsg, vx;
  unsigned m0w, sofq, sofg;
  __amdgpu_buffer_rsrc_t srdQ, srdG, srdU, srdX;
  bf16x4 wl[32], wu[32];
  u32x4 xf[2][8];
};
constexpr int PLEAD = 8;
template <int T, int F, int HALF> DI void w_read(PC& p) {
  constexpr int ks = F >> 3, b = F & 7, off = 16384 * (b >> 2) + 4096 * ks + 512 * (b & 3) + 256 * HALF;
  const unsigned base = p.vwb + 32768u * (T % 3);
  if constexpr (HALF == 0) asm volatile("ds_read_b64_tr_b16 %0, %1 offset:%2" : "=v"(p.wl[F]) : "v"(base), "i"(off) : "memory");
  else                     asm volatile("ds_read_b64_tr_b16 %0, %1 offset:%2" : "=v"(p.wu[F]) : "v"(base), "i"(off) : "memory");
}
template <int T, int I> DI void x_load(PC& p) {
  constexpr int qb = I >> 2, ks = I & 3;
  asm volatile("buffer_load_dwordx4 %0, %1, %2, %3 offen offset:%4" : "=v"(p.xf[T & 1][I]) : "v"(p.vx), "s"(p.srdX), "s"(32768u * qb + 4096u * T), "i"(1024 * ks) : "memory");
}
template <int T, int Q> DI void w_dma(PC& p) {
  if constexpr (Q < 4)
    asm volatile("s_mov_b32 m0, %0\n\ts_nop 0\n\tbuffer_load_dwordx4 %1, %2, %3 offen lds"
                 :: "s"(p.m0w + 32768u * (T % 3) + 1024u * Q), "v"(p.vwsq), "s"(p.srdQ), "s"(p.sofq + 8192u * 64u * T + 8192u * 8u * (Q >> 1) + 128u * (Q & 1)) : "m0", "memory");
  else if constexpr (((Q - 4) & 1) == 0)
    asm volatile("s_mov_b32 m0, %0\n\ts_nop 0\n\tbuffer_load_dwordx4 %1, %2, %3 offen lds"
                 :: "s"(p.m0w + 32768u * (T % 3) + 16384u + 1024u * (Q - 4)), "v"(p.vwsg), "s"(p.srdG), "s"(p.sofg + 3584u * 64u * T + 3584u * 8u * ((Q - 4) >> 1)) : "m0", "memory");
  else
    asm volatile("s_mov_b32 m0, %0\n\ts_nop 0\n\tbuffer_load_dwordx4 %1, %2, %3 offen lds"
                 :: "s"(p.m0w + 32768u * (T % 3) + 16384u + 1024u * (Q - 4)), "v"(p.vwsg), "s"(p.srdU), "s"(p.sofg + 3584u * 64u * T + 3584u * 8u * ((Q - 4) >> 1)) : "m0", "memory");
}
template <int TK, int Q> DI void kv_dma0(W4C& c) {
  if constexpr (Q < 4)
    asm volatile("s_mov_b32 m0, %0\n\ts_nop 0\n\tbuffer_load_dwordx4 %1, %2, %3 offen lds"
                 :: "s"(c.m0k + 0x4000u * TK + 1024u * Q), "v"(c.vksrc[Q & 1]), "s"(c.srdK), "s"(KTILEB * TK + c.sofk + KRS * 4u * Q) : "m0", "memory");
  else
    asm volatile("s_mov_b32 m0, %0\n\ts_nop 0\n\tbuffer_load_dwordx4 %1, %2, %3 offen lds"
                 :: "s"(c.m0v + 1024u * (Q - 4)), "v"(c.vvsrc), "s"(c.srdV), "s"(c.sofv + KRS * 8u * ((Q - 4) >> 1) + 128u * ((Q - 4) & 1)) : "m0", "memory");
}
template <int T, int M> DI void gapP(PC& p, W4C& c) {
  constexpr int F = M >> 1, qb = M & 1, ks = F >> 3, b = F & 7, ao = 16 * (2 * b + qb);
  if constexpr (M == 0) {
    asm volatile("s_waitcnt vmcnt(8)" : "+v"(p.xf[T & 1][0]), "+v"(p.xf[T & 1][1]), "+v"(p.xf[T & 1][2]), "+v"(p.xf[T & 1][3]),
                                        "+v"(p.xf[T & 1][4]), "+v"(p.xf[T & 1][5]), "+v"(p.xf[T & 1][6]), "+v"(p.xf[T & 1][7]) :: "memory");
  }
  if constexpr (qb == 0) {
    constexpr int younger = (T < 7) ? (PLEAD - 2) : ((64 - 2 * F - 2) < (PLEAD - 2) ? (64 - 2 * F - 2) : (PLEAD - 2));
    asm volatile("s_waitcnt lgkmcnt(%c2)" : "+v"(p.wl[F]), "+v"(p.wu[F]) : "i"(younger) : "memory");
  }
  {
    const bf16x8 wf = __builtin_shufflevector(p.wl[F], p.wu[F], 0, 1, 2, 3, 4, 5, 6, 7);
    if constexpr (T == 0 && ks == 0) asm volatile("v_mfma_f32_32x32x16_bf16 a[%c2:%c3], %0, %1, 0" :: "v"(wf), "v"(p.xf[T & 1][4 * qb + ks]), "i"(ao), "i"(ao + 15) : W4_ACLOB, W4_ACLOB_HI);
    else                             asm volatile("v_mfma_f32_32x32x16_bf16 a[%c2:%c3], %0, %1, a[%c2:%c3]" :: "v"(wf), "v"(p.xf[T & 1][4 * qb + ks]), "i"(ao), "i"(ao + 15) : W4_ACLOB, W4_ACLOB_HI);
  }
  if constexpr (M + PLEAD < 64) w_read<T, ((M + PLEAD) >> 1), ((M + PLEAD) & 1)>(p);
  else if constexpr (T < 7)     w_read<T + 1, ((M + PLEAD - 64) >> 1), ((M + PLEAD - 64) & 1)>(p);
  if constexpr (M < 8 && T < 7) x_load<T + 1, M>(p);
  if constexpr (M == 32 && T < 7) asm volatile("s_waitcnt vmcnt(8)\n\ts_barrier" ::: "memory");
  if constexpr (M >= 33 && M < 41) {
    if constexpr (T < 6) w_dma<T + 2, M - 33>(p);
    else if constexpr (T == 6) kv_dma0<0, M - 33>(c);
    else if constexpr (M < 37) kv_dma0<1, M - 33>(c);
  }
  GAP();
}
template <int T, int... Ms> DI void tileP_(PC& p, W4C& c, iseq<Ms...>) { (gapP<T, Ms>(p, c), ...); }
template <int... Ts> DI void tilesP(PC& p, W4C& c, iseq<Ts...>) { (tileP_<Ts>(p, c, make_iseq<64>{}), ...); }
template <int T, int... Qs> DI void w_dmas(PC& p, iseq<Qs...>) { (w_dma<T, Qs>(p), ...); }
template <int T, int... Is> DI void x_loads(PC& p, iseq<Is...>) { (x_load<T, Is>(p), ...); }
template <int... Is> DI void w_reads0(PC& p, iseq<Is...>) { (w_read<0, (Is >> 1), (Is & 1)>(p), ...); }

DI float gelu_tanh(float g) {
  const float z2 = g * (2.0f * 0.7978845608028654f * 1.4426950408889634f) * (1.0f + 0.044715f * g * g);
  return g * __builtin_amdgcn_rcpf(1.0f + __builtin_amdgcn_exp2f(-z2));
}
template <int QB, int B2, int SS, int... Js> DI u32x4 geglu8(iseq<Js...>) {
  float g[8] = {acc_rd<16 * (2 * (4 + B2) + QB) + 8 * SS + Js>()...};
  float u[8] = {acc_rd<16 * (2 * (6 + B2) + QB) + 8 * SS + Js>()...};
  u32x4 w;
  #pragma unroll
  for (int e = 0; e < 4; ++e) w[e] = cvtpk(gelu_tanh(g[2 * e]) * u[2 * e], gelu_tanh(g[2 * e + 1]) * u[2 * e + 1]);
  return w;
}
template <int I> DI void geglu_store(char* hp, bool st) {
  constexpr int qb = I >> 2, b2 = (I >> 1) & 1, ss = I & 1;
  const u32x4 w = geglu8<qb, b2, ss>(make_iseq<8>{});
  if (st) *(u32x4*)(hp + qb * (112 * 1024) + 1024 * (2 * b2 + ss)) = w;
}
template <int... Is> DI void geglu_stores(char* hp, bool st, iseq<Is...>) { (geglu_store<Is>(hp, st), ...); }
template <int I, int... Js> DI u32x4 qpack8(float sc, iseq<Js...>) {
  constexpr int qb = I >> 3, d0 = I & 7;
  float q[8] = {acc_rd<16 * (2 * (d0 >> 1) + qb) + 8 * (d0 & 1) + Js>()...};
  u32x4 w;
  #pragma unroll
  for (int e = 0; e < 4; ++e) w[e] = cvtpk(fmul_s(q[2 * e], sc), fmul_s(q[2 * e + 1], sc));
  return w;
}
template <int I> DI void q_write1(const u32x4& w) {
  constexpr int aq = AQ_(I >> 3, I & 7);
  asm volatile("v_accvgpr_write_b32 a[%c4], %0\n\tv_accvgpr_write_b32 a[%c5], %1\n\tv_accvgpr_write_b32 a[%c6], %2\n\tv_accvgpr_write_b32 a[%c7], %3"
               :: "v"(w[0]), "v"(w[1]), "v"(w[2]), "v"(w[3]), "i"(aq), "i"(aq + 1), "i"(aq + 2), "i"(aq + 3) : W4_ACLOB_HI);
}
template <int... Is> DI void q_convert(float sc, iseq<Is...>) { (q_write1<Is>(qpack8<Is>(sc, make_iseq<8>{})), ...); }
template <int DB, int QB, int... Rs> DI void o_read(float (&o)[16], iseq<Rs...>) { ((o[Rs] = acc_rd<AO_(DB, QB) + Rs>()), ...); }

extern "C" __global__ __launch_bounds__(256, 1)
void k_attn(const unsigned short* __restrict__ XN, const unsigned short* __restrict__ Kc, const unsigned short* __restrict__ Vc,
            const unsigned short* __restrict__ Wq, const unsigned short* __restrict__ Wg, const unsigned short* __restrict__ Wu,
            unsigned short* __restrict__ Ob, unsigned short* __restrict__ Hb, const unsigned short* __restrict__ Wo, const unsigned short* __restrict__ Wd, char* __restrict__ Wt
) {
  STAMP(0);
  extern __shared__ __attribute__((aligned(16))) char lds[];
  const unsigned tid = threadIdx.x, lane = tid & 63u, wid = tid >> 6, l32 = lane & 31u, hi = lane >> 5;
  constexpr int T = NT;
  const float C = 0.12751743082f;
  const unsigned blk = blockIdx.x;
  const unsigned h = (blk & 7u) * 4u + ((blk >> 3) & 3u), y = blk >> 5;
  const unsigned row0 = 256u * y + 64u * rfl(wid);
  W4C c;
  c.ldsb = (unsigned)(size_t)lds;
  #pragma unroll
  for (int j = 0; j < 4; ++j) c.vkb[j] = c.ldsb + KRING + 256u * l32 + 16u * ((2u * j + hi) ^ (l32 & 7u));
  c.vvb = c.ldsb + VRING + (((lane & 3u) << 3) | (((lane >> 2) & 3u) << 6) | (((lane >> 4) & 1u) << 5) | (hi << 8));
  #pragma unroll
  for (int par = 0; par < 2; ++par) c.vksrc[par] = KRS * (lane >> 4) + 16u * ((lane & 15u) ^ (4u * par + (lane >> 4)));
  c.vvsrc = KRS * ((lane >> 2) & 7u) + 64u * hi + 16u * (lane & 3u);
  c.m0k = rfl(c.ldsb + KRING + 4096u * wid); c.m0v = rfl(c.ldsb + VRING + 4096u * wid);
  c.sofk = rfl(KRS * 16u * wid); c.sofv = rfl(KRS * 16u * wid);
  const unsigned hkv = h;
  c.srdK = __builtin_amdgcn_make_buffer_rsrc((void*)((const char*)Kc + (size_t)hkv * S * 256), (short)0, S * 256, SRD_WORD3);
  c.srdV = __builtin_amdgcn_make_buffer_rsrc((void*)((const char*)Vc + (size_t)hkv * S * 256), (short)0, S * 256, SRD_WORD3);

  {
    PC p;
    const unsigned jf = h < 28u ? h : h - 28u;
    const unsigned sw = ((lane & 1u) << 1) | ((lane >> 1) & 1u);
    p.vwb = c.ldsb + WRING + 2048u * hi + 64u * ((lane >> 2) & 3u) + 32u * ((lane >> 4) & 1u) + 8u * sw;
    p.vwsq = 8192u * ((lane >> 2) & 7u) + 64u * hi + 16u * (lane & 3u);
    p.vwsg = 3584u * ((lane >> 2) & 7u) + 64u * hi + 16u * (lane & 3u);
    p.vx = 16u * lane;
    p.m0w = rfl(c.ldsb + WRING + 4096u * wid);
    p.sofq = rfl(8192u * 16u * wid); p.sofg = rfl(3584u * 16u * wid);
    p.srdQ = __builtin_amdgcn_make_buffer_rsrc((void*)((const char*)Wq + h * 256u), (short)0, DM * 8192 - (int)(h * 256u), SRD_WORD3);
    p.srdG = __builtin_amdgcn_make_buffer_rsrc((void*)((const char*)Wg + jf * 128u), (short)0, DM * 3584 - (int)(jf * 128u), SRD_WORD3);
    p.srdU = __builtin_amdgcn_make_buffer_rsrc((void*)((const char*)Wu + jf * 128u), (short)0, DM * 3584 - (int)(jf * 128u), SRD_WORD3);
    p.srdX = __builtin_amdgcn_make_buffer_rsrc((void*)((const char*)XN + (size_t)row0 * 1024), (short)0, 64 * 1024, SRD_WORD3);
    asm volatile("s_nop 4" ::: "memory");
    w_dmas<0>(p, make_iseq<8>{}); x_loads<0>(p, make_iseq<8>{}); w_dmas<1>(p, make_iseq<8>{});
    STAMP(1);
    asm volatile("s_waitcnt vmcnt(8)\n\ts_barrier" ::: "memory");
    STAMP(2);
    w_reads0(p, make_iseq<PLEAD>{});
    GAP();
    tilesP(p, c, make_iseq<8>{});
    asm volatile("s_nop 7\n\ts_nop 7" ::: "memory");
    const bool wd_blk = blk < 112u;
    {
      const __amdgpu_buffer_rsrc_t srdWo = __builtin_amdgcn_make_buffer_rsrc((void*)Wo, (short)0, H * D * 1024, SRD_WORD3);
      const __amdgpu_buffer_rsrc_t srdWd = __builtin_amdgcn_make_buffer_rsrc((void*)Wd, (short)0, DF * 1024, SRD_WORD3);
      const unsigned vws = 1024u * ((lane >> 2) & 7u) + 64u * hi + 16u * (lane & 3u);
      const unsigned so = rfl((blk >> 3) * 131072u + (blk & 7u) * 128u + 32768u * wid);
      asm volatile("s_nop 4" ::: "memory");
      #pragma unroll
      for (int j = 0; j < 4; ++j)
        asm volatile("s_mov_b32 m0, %0\n\ts_nop 0\n\tbuffer_load_dwordx4 %1, %2, %3 offen lds" :: "s"(p.m0w + 65536u + 1024u * j), "v"(vws), "s"(srdWo), "s"(so + 8192u * j) : "m0", "memory");
      if (wd_blk) {
        #pragma unroll
        for (int j = 0; j < 4; ++j)
          asm volatile("s_mov_b32 m0, %0\n\ts_nop 0\n\tbuffer_load_dwordx4 %1, %2, %3 offen lds" :: "s"(p.m0w + 65536u + 16384u + 1024u * j), "v"(vws), "s"(srdWd), "s"(so + 8192u * j) : "m0", "memory");
      }
    }
    char* hp = (char*)Hb + (size_t)(row0 >> 5) * (112 * 1024) + jf * 4096u + 16u * lane;
    geglu_stores(hp, h < 28u, make_iseq<8>{});
    GAP();
    STAMP(11);
    q_convert(C, make_iseq<16>{});
    GAP();
    STAMP(12);
    {
      asm volatile("s_waitcnt vmcnt(0)" ::: "memory");
      const unsigned vr = c.ldsb + WRING + 65536u + 4096u * wid + 1024u * hi + 64u * ((lane >> 2) & 3u) + 32u * ((lane >> 4) & 1u) + 8u * sw;
      char* wt = Wt + ((size_t)(blk & 7u) * 46u + (blk >> 3)) * 16384u + 4096u * wid + 16u * lane;
      #pragma unroll
      for (int m = 0; m < 2; ++m) {
        if (m == 0 || wd_blk) {
          bf16x4 lo[4], up[4];
          #pragma unroll
          for (int f = 0; f < 4; ++f) {
            asm volatile("ds_read_b64_tr_b16 %0, %1 offset:%2" : "=v"(lo[f]) : "v"(vr), "i"(16384 * m + 2048 * (f >> 1) + 512 * (f & 1)) : "memory");
            asm volatile("ds_read_b64_tr_b16 %0, %1 offset:%2" : "=v"(up[f]) : "v"(vr), "i"(16384 * m + 2048 * (f >> 1) + 512 * (f & 1) + 256) : "memory");
          }
          asm volatile("s_waitcnt lgkmcnt(0)" : "+v"(lo[0]), "+v"(lo[1]), "+v"(lo[2]), "+v"(lo[3]), "+v"(up[0]), "+v"(up[1]), "+v"(up[2]), "+v"(up[3]) :: "memory");
          #pragma unroll
          for (int f = 0; f < 4; ++f) *(bf16x8*)(wt + m * (32 * 16384) + 1024 * f) = __builtin_shufflevector(lo[f], up[f], 0, 1, 2, 3, 4, 5, 6, 7);
        }
      }
    }
  }
  asm volatile("s_waitcnt vmcnt(0)\n\ts_barrier" ::: "memory");
  k_reads(c, 0u, make_iseq<16>{});
  asm volatile("s_waitcnt lgkmcnt(0)\n\ts_nop 1" ::: "memory");

  STAMP(13);
  c.mhat[0] = c.mhat[1] = 0.f; c.lsum[0] = c.lsum[1] = 0.f; c.fct[0] = c.fct[1] = 1.f; c.pend = false;

  phaseA<0, true>(c, 0);  phaseB<0, true,  false>(c, 0);
  phaseA<1, false>(c, 1); phaseB<1, false, true>(c, 1);
  for (int t = 2; t < T - 2; t += 2) {
    phaseA<0, false>(c, t);     phaseB<0, false, false>(c, t);
    phaseA<1, false>(c, t + 1); phaseB<1, false, false>(c, t + 1);
  }
  STAMP(14);
  phaseA<0, false>(c, T - 2); phaseB<0, false, false, false, true, true>(c, T - 2);
  phaseA<1, false>(c, T - 1); phaseB<1, false, false, false, false, false>(c, T - 1);
  STAMP(15);
  {
    const unsigned vslot = (unsigned)((T - 1) & 1) * 0x4000u;
    tr_reads(c, vslot, make_iseq<32>{});
    #pragma unroll
    for (int e = 0; e < 32; ++e) c.S[1][1][e >> 4][e & 15] = __builtin_amdgcn_exp2f(c.S[1][1][e >> 4][e & 15]);
    packs<1>(c, make_iseq<32>{});
    rowsums<1>(c, iseq<8, 9, 10, 11, 12, 13, 14, 15, 24, 25, 26, 27, 28, 29, 30, 31>{});
    #pragma unroll
    for (int qb = 0; qb < 2; ++qb) { const float rs = fadd_s(c.rs0[qb], c.rs1[qb]);
      auto pr = __builtin_amdgcn_permlane32_swap(__float_as_uint(rs), __float_as_uint(rs), false, false);
      c.lsum[qb] = fadd_s(c.lsum[qb], fadd_s(__uint_as_float(pr[0]), __uint_as_float(pr[1]))); }
    asm volatile("s_waitcnt lgkmcnt(0)" : W4_VLO :: "memory"); asm volatile("" : W4_VUP :: "memory");
    GAP();
    #pragma unroll
    for (int ks = 0; ks < 4; ++ks)
      #pragma unroll
      for (int db = 0; db < 4; ++db) c.Vf[db][ks] = __builtin_shufflevector(c.vlo[db][ks], c.vup[db][ks], 0, 1, 2, 3, 4, 5, 6, 7);
    asm volatile("s_nop 1" ::: "memory");
    pv_drain(c, make_iseq<32>{});
    asm volatile("s_nop 7\n\ts_nop 7" ::: "memory");
  }
  STAMP(16);
  {
    unsigned lb; asm volatile("v_mbcnt_lo_u32_b32 %0, -1, 0\n\tv_mbcnt_hi_u32_b32 %0, -1, %0" : "=v"(lb));
    const unsigned l32b = lb & 31u, hib = lb >> 5;
    char* stg = lds + WRING + 16384u * wid;
    #pragma unroll
    for (int qb = 0; qb < 2; ++qb) {
      const float il = c.lsum[qb] > 0.f ? __builtin_amdgcn_rcpf(c.lsum[qb]) : 0.f;
      const unsigned r = 32u * qb + l32b;
      #pragma unroll
      for (int db = 0; db < 4; ++db) {
        float o[16];
        if (db == 0) { if (qb == 0) o_read<0, 0>(o, make_iseq<16>{}); else o_read<0, 1>(o, make_iseq<16>{}); }
        if (db == 1) { if (qb == 0) o_read<1, 0>(o, make_iseq<16>{}); else o_read<1, 1>(o, make_iseq<16>{}); }
        if (db == 2) { if (qb == 0) o_read<2, 0>(o, make_iseq<16>{}); else o_read<2, 1>(o, make_iseq<16>{}); }
        if (db == 3) { if (qb == 0) o_read<3, 0>(o, make_iseq<16>{}); else o_read<3, 1>(o, make_iseq<16>{}); }
        #pragma unroll
        for (int gq = 0; gq < 4; ++gq) {
          u32x2 w = {cvtpk(fmul_s(o[4 * gq + 0], il), fmul_s(o[4 * gq + 1], il)), cvtpk(fmul_s(o[4 * gq + 2], il), fmul_s(o[4 * gq + 3], il))};
          const unsigned ch = 4u * db + gq;
          *(u32x2*)(stg + 256u * r + 16u * (ch ^ (r & 15u)) + 8u * hib) = w;
        }
      }
    }
    asm volatile("s_waitcnt lgkmcnt(0)" ::: "memory");
    u32x4 v[16];
    #pragma unroll
    for (int j = 0; j < 16; ++j) { const unsigned r = 32u * (j >> 3) + l32b, ch = 2u * (j & 7) + hib; v[j] = *(const u32x4*)(stg + 256u * r + 16u * (ch ^ (r & 15u))); }
    char* ob = (char*)Ob + (size_t)(row0 >> 5) * 262144 + h * 8192u + 16u * lb;
    #pragma unroll
    for (int j = 0; j < 16; ++j) *(u32x4*)(ob + (j >> 3) * 262144 + 1024 * (j & 7)) = v[j];
  }
  STAMP(17);
}

constexpr int FSL = 5;
constexpr size_t SHM_FIN = FSL * 32768;
constexpr int FN = 46, FS = 32;
struct FC {
  unsigned vasrc, vard, vwrd, m0a, m0w;
  __amdgpu_buffer_rsrc_t srdA;
  unsigned aoO, aoH, wo0;
};
DI void f_issue(FC& f, const unsigned u, const unsigned slot) {
  const bool isS = u < (unsigned)FS;
  const unsigned ao = isS ? f.aoO + 8192u * u : f.aoH + 8192u * (u - FS);
  const unsigned wo = f.wo0 + 16384u * u;
  #pragma unroll
  for (int j = 0; j < 4; ++j)
    asm volatile("s_mov_b32 m0, %0\n\ts_nop 0\n\tbuffer_load_dwordx4 %1, %2, %3 offen lds" :: "s"(f.m0a + slot + 1024u * j), "v"(f.vasrc), "s"(f.srdA), "s"(ao + 1024u * j) : "m0", "memory");
  #pragma unroll
  for (int j = 0; j < 4; ++j)
    asm volatile("s_mov_b32 m0, %0\n\ts_nop 0\n\tbuffer_load_dwordx4 %1, %2, %3 offen lds" :: "s"(f.m0w + slot + 1024u * j), "v"(f.vasrc), "s"(f.srdA), "s"(wo + 1024u * j) : "m0", "memory");
}
DI void f_step(FC& f, const unsigned slot, f32x16& acc) {
  asm volatile("s_barrier" ::: "memory");
  const unsigned va = f.vard + slot, vw = f.vwrd + slot;
  u32x4 af[8], wf[8];
  #pragma unroll
  for (int ks = 0; ks < 8; ++ks) {
    asm volatile("ds_read_b128 %0, %1 offset:%2" : "=v"(af[ks]) : "v"(va), "i"(1024 * ks) : "memory");
    asm volatile("ds_read_b128 %0, %1 offset:%2" : "=v"(wf[ks]) : "v"(vw), "i"(2048 * ks) : "memory");
  }
  #pragma unroll
  for (int ks = 0; ks < 8; ++ks) {
    asm volatile("s_waitcnt lgkmcnt(%c2)" : "+v"(af[ks]), "+v"(wf[ks]) : "i"(2 * (7 - ks)) : "memory");
    GAP();
    acc = __builtin_amdgcn_mfma_f32_32x32x16_bf16(__builtin_bit_cast(bf16x8, wf[ks]), __builtin_bit_cast(bf16x8, af[ks]), acc, 0, 0, 0);
    GAP();
  }
}

extern "C" __global__ __launch_bounds__(512, 2)
void k_final(const unsigned short* __restrict__ X, char* __restrict__ ws, unsigned short* __restrict__ Out) {
  STAMP(0);
  extern __shared__ __attribute__((aligned(16))) char lds[];
  const unsigned tid = threadIdx.x, lane = tid & 63u, wid8 = rfl(tid >> 6), wid = wid8 & 3u, l32 = lane & 31u, hi = lane >> 5;
  const bool loader = wid8 >= 4u;
  const unsigned mb = wid >> 1, nb = wid & 1u;
  const unsigned blk = blockIdx.x;
  const unsigned mt = (blk & 7u) * 4u + ((blk >> 3) & 3u), nt = blk >> 5;
  const unsigned m0 = 64u * mt, n0 = 64u * nt;
  const unsigned ldsb = (unsigned)(size_t)lds;
  FC f;
  f.vasrc = 16u * lane;
  f.vard = ldsb + 8192u * mb + 16u * lane;
  f.vwrd = ldsb + 16384u + 1024u * nb + 16u * lane;
  f.m0a = rfl(ldsb + 4096u * wid); f.m0w = rfl(ldsb + 16384u + 4096u * wid);
  f.aoO = (unsigned)WS_O + ((m0 >> 5) + (wid >> 1)) * 262144u + 4096u * (wid & 1u); f.aoH = (unsigned)WS_H + ((m0 >> 5) + (wid >> 1)) * 114688u + 4096u * (wid & 1u);
  f.wo0 = (unsigned)WS_WT + nt * (46u * 16384u) + 4096u * wid;
  f.srdA = __builtin_amdgcn_make_buffer_rsrc((void*)ws, (short)0, (int)WS_END, SRD_WORD3);
  if (loader) {
    asm volatile("s_nop 4" ::: "memory");
    #pragma unroll
    for (int j = 0; j < FSL - 1; ++j) f_issue(f, j, 32768u * j);
    unsigned slot = (FSL - 1) * 32768u;
    for (unsigned i = 0; i < (unsigned)FN; ++i) {
      if (i + 3 < FN)      asm volatile("s_waitcnt vmcnt(%c0)\n\ts_barrier" :: "i"(8 * (FSL - 2)) : "memory");
      else if (i + 2 < FN) asm volatile("s_waitcnt vmcnt(16)\n\ts_barrier" ::: "memory");
      else if (i + 1 < FN) asm volatile("s_waitcnt vmcnt(8)\n\ts_barrier" ::: "memory");
      else                 asm volatile("s_waitcnt vmcnt(0)\n\ts_barrier" ::: "memory");
      if (i + FSL - 1 < FN) f_issue(f, i + FSL - 1, slot);
      slot = slot == (FSL - 1) * 32768u ? 0u : slot + 32768u;
    }
  } else {
    STAMP(1);
    f32x16 accS = {}, accF = {};
    const size_t off = (size_t)(m0 + 32u * mb + l32) * DM + n0 + 32u * nb + 8u * hi;
    const u32x4 xw0 = *(const u32x4*)(X + off), xw1 = *(const u32x4*)(X + off + 16);
    unsigned i = 0, slot = 0;
    for (; i < (unsigned)FS; ++i) { f_step(f, slot, accS); slot = slot == (FSL - 1) * 32768u ? 0u : slot + 32768u; }
    for (; i < (unsigned)FN; ++i) { f_step(f, slot, accF); slot = slot == (FSL - 1) * 32768u ? 0u : slot + 32768u; }
    STAMP(2);
    #pragma unroll
    for (int ss = 0; ss < 2; ++ss) {
      const u32x4 xw = ss ? xw1 : xw0;
      float o[8];
      #pragma unroll
      for (int e = 0; e < 8; ++e) {
        const float sv = accS[8 * ss + e], fv = accF[8 * ss + e];
        const float gate = fv * __builtin_amdgcn_rcpf(1.0f + __builtin_amdgcn_exp2f(-fv * 1.4426950408889634f));
        const float xv = (e & 1) ? bfhi(xw[e >> 1]) : bflo(xw[e >> 1]);
        o[e] = xv + sv * gate;
      }
      u32x4 w;
      #pragma unroll
      for (int e = 0; e < 4; ++e) w[e] = cvtpk(o[2 * e], o[2 * e + 1]);
      *(u32x4*)(Out + off + 16 * ss) = w;
    }
    STAMP(3); STAMP(4); STAMP(5); STAMP(6);
  }
}

extern "C" void kernel_launch(void* const* d_in, const int* in_sizes, int n_in,
                              void* d_out, int out_size, void* d_ws, size_t ws_size,
                              hipStream_t stream) {
  const unsigned short* x  = (const unsigned short*)d_in[0];
  const unsigned short* kc = (const unsigned short*)d_in[1];
  const unsigned short* vc = (const unsigned short*)d_in[2];
  const unsigned short* wq = (const unsigned short*)d_in[3];
  const unsigned short* wo = (const unsigned short*)d_in[4];
  const unsigned short* wg = (const unsigned short*)d_in[6];
  const unsigned short* wu = (const unsigned short*)d_in[7];
  const unsigned short* wd = (const unsigned short*)d_in[8];
  unsigned short* xn = (unsigned short*)((char*)d_ws + WS_XN);
  unsigned short* ob = (unsigned short*)((char*)d_ws + WS_O);
  unsigned short* hb = (unsigned short*)((char*)d_ws + WS_H);
  (void)hipFuncSetAttribute((const void*)k_attn, hipFuncAttributeMaxDynamicSharedMemorySize, (int)SHM_ATTN);
  (void)hipFuncSetAttribute((const void*)k_final, hipFuncAttributeMaxDynamicSharedMemorySize, (int)SHM_FIN);
  hipLaunchKernelGGL(k_ln, dim3(S / 8), dim3(64), 0, stream, x, xn);
  hipLaunchKernelGGL(k_attn, dim3(256), dim3(256), SHM_ATTN, stream, xn, kc, vc, wq, wg, wu, ob, hb, wo, wd, (char*)d_ws + WS_WT
  );
  hipLaunchKernelGGL(k_final, dim3(256), dim3(512), SHM_FIN, stream, x, (char*)d_ws, (unsigned short*)d_out);
}
```

```cpp
#include <hip/hip_runtime.h>
#include <hip/hip_bf16.h>
#include <utility>

constexpr int S = 2048, H = 32, D = 128, DM = 512, DF = 1792;
#define ATT_THR 30.0f
#define ATT_MARG 30.0f
constexpr float THR = ATT_THR, MARG = ATT_MARG;

typedef float    f32x16 __attribute__((ext_vector_type(16)));
typedef float    f32x2  __attribute__((ext_vector_type(2)));
typedef float    f32x4  __attribute__((ext_vector_type(4)));
typedef __bf16   bf16x8 __attribute__((ext_vector_type(8)));
typedef __bf16   bf16x2 __attribute__((ext_vector_type(2)));
typedef unsigned u32x4  __attribute__((ext_vector_type(4)));
template <int... Is> using iseq = std::integer_sequence<int, Is...>;
template <int N> using make_iseq = std::make_integer_sequence<int, N>;

#define DI __device__ __forceinline__
#define GAP() __builtin_amdgcn_sched_barrier(0)
#define SRD_WORD3 0x00020000

constexpr size_t WS_XN = 0, WS_O = (size_t)S * DM * 2, WS_H = WS_O + (size_t)S * H * D * 2, WS_WT = WS_H + (size_t)S * DF * 2,
                 WS_SLAB = WS_WT + (size_t)(H * D + DF) * DM * 2, WS_CNT = WS_SLAB + (size_t)128 * 32768, WS_END = WS_CNT + 128 * 256;

DI unsigned rfl(unsigned x) { return __builtin_amdgcn_readfirstlane(x); }
DI unsigned cvtpk(float lo, float hi) {
  f32x2 v = {lo, hi}; bf16x2 b = __builtin_convertvector(v, bf16x2); return __builtin_bit_cast(unsigned, b);
}
DI float bflo(unsigned w) { return __uint_as_float(w << 16); }
DI float bfhi(unsigned w) { return __uint_as_float(w & 0xffff0000u); }
DI unsigned lane_id() { unsigned l; asm volatile("v_mbcnt_lo_u32_b32 %0, -1, 0\n\tv_mbcnt_hi_u32_b32 %0, -1, %0" : "=v"(l)); return l; }

__global__ __launch_bounds__(64) void k_ln(const unsigned short* __restrict__ x, unsigned short* __restrict__ xn, unsigned* __restrict__ flags,
                                           const u32x4* __restrict__ wq, const u32x4* __restrict__ wg, const u32x4* __restrict__ wu) {
  if (blockIdx.x < 128 && threadIdx.x == 0) flags[blockIdx.x * 64] = 0u;
  if (blockIdx.x >= S / 8) {
    const unsigned b = blockIdx.x - S / 8, xg = b & 7u, i = b >> 3;
    u32x4 acc = {0u, 0u, 0u, 0u};
#pragma unroll
    for (int j = 0; j < 16; ++j) acc ^= wq[(size_t)(16u * i + j) * 512 + xg * 64u + threadIdx.x];
    if (xg < 7u) {
#pragma unroll
      for (int j = 0; j < 8; ++j) {
        const size_t o = (size_t)(16u * i + 2 * j + (threadIdx.x >> 5)) * 224 + xg * 32u + (threadIdx.x & 31u);
        acc ^= wg[o]; acc ^= wu[o];
      }
    }
    if (acc[0] == 0x9e3779b9u && acc[1] == 0x7f4a7c15u && acc[2] == 0x12345u && acc[3] == 0xabcdefu) flags[128 * 64 - 1] = 1u;
    return;
  }
  const int lane = threadIdx.x, row = blockIdx.x * 8 + (lane >> 3), c0 = lane & 7;
  u32x4 w[8];
#pragma unroll
  for (int j = 0; j < 8; ++j) w[j] = *(const u32x4*)(x + (size_t)row * DM + (c0 + 8 * j) * 8);
  float s = 0.f;
#pragma unroll
  for (int j = 0; j < 8; ++j)
#pragma unroll
    for (int e = 0; e < 4; ++e) s += bflo(w[j][e]) + bfhi(w[j][e]);
#pragma unroll
  for (int o = 4; o >= 1; o >>= 1) s += __shfl_xor(s, o);
  const float m = s * (1.0f / DM);
  float q = 0.f;
#pragma unroll
  for (int j = 0; j < 8; ++j)
#pragma unroll
    for (int e = 0; e < 4; ++e) { const float a = bflo(w[j][e]) - m, b = bfhi(w[j][e]) - m; q += a * a + b * b; }
#pragma unroll
  for (int o = 4; o >= 1; o >>= 1) q += __shfl_xor(q, o);
  const float r = 1.0f / sqrtf(q * (1.0f / DM) + 1e-5f);
  char* dst = (char*)xn + (size_t)(row >> 5) * 32768 + (row & 31) * 16;
#pragma unroll
  for (int j = 0; j < 8; ++j) {
    u32x4 o4;
#pragma unroll
    for (int e = 0; e < 4; ++e) o4[e] = cvtpk((bflo(w[j][e]) - m) * r, (bfhi(w[j][e]) - m) * r);
    const int ch = c0 + 8 * j;
    *(u32x4*)(dst + (ch >> 1) * 1024 + (ch & 1) * 512) = o4;
  }
}

constexpr unsigned KRS = 256u, KTILEB = 64u * KRS;
constexpr size_t SHM_ATTN = 163840;
#define KRING 0x0000u
#define VRING 0x8000u
#define WRING 0x10000u
#define ACLOB "a0","a1","a2","a3","a4","a5","a6","a7","a8","a9","a10","a11","a12","a13","a14","a15","a16","a17","a18","a19","a20","a21","a22","a23","a24","a25","a26","a27","a28","a29","a30","a31","a32","a33","a34","a35","a36","a37","a38","a39","a40","a41","a42","a43","a44","a45","a46","a47","a48","a49","a50","a51","a52","a53","a54","a55","a56","a57","a58","a59","a60","a61","a62","a63","a64","a65","a66","a67","a68","a69","a70","a71","a72","a73","a74","a75","a76","a77","a78","a79","a80","a81","a82","a83","a84","a85","a86","a87","a88","a89","a90","a91","a92","a93","a94","a95","a96","a97","a98","a99","a100","a101","a102","a103","a104","a105","a106","a107","a108","a109","a110","a111","a112","a113","a114","a115","a116","a117","a118","a119","a120","a121","a122","a123","a124","a125","a126","a127","a128","a129","a130","a131","a132","a133","a134","a135","a136","a137","a138","a139","a140","a141","a142","a143","a144","a145","a146","a147","a148","a149","a150","a151","a152","a153","a154","a155","a156","a157","a158","a159","a160","a161","a162","a163","a164","a165","a166","a167","a168","a169","a170","a171","a172","a173","a174","a175","a176","a177","a178","a179","a180","a181","a182","a183","a184","a185","a186","a187","a188","a189","a190","a191","a192","a193","a194","a195","a196","a197","a198","a199","a200","a201","a202","a203","a204","a205","a206","a207","a208","a209","a210","a211","a212","a213","a214","a215","a216","a217","a218","a219","a220","a221","a222","a223","a224","a225","a226","a227","a228","a229","a230","a231","a232","a233","a234","a235","a236","a237","a238","a239","a240","a241","a242","a243","a244","a245","a246","a247","a248","a249","a250","a251","a252","a253","a254","a255"
#define VCLOB_PRO "v0","v1","v2","v3","v4","v5","v6","v7","v8","v9","v10","v11","v12","v13","v14","v15","v16","v17","v18","v19","v20","v21","v22","v23","v24","v25","v26","v27","v28","v29","v30","v31","v32","v33","v34","v35","v36","v37","v38","v39","v40","v41","v42","v43","v44","v45","v46","v47","v48","v49","v50","v51","v52","v53","v54","v55","v56","v57","v58","v59","v60","v61","v62","v63","v64","v65","v66","v67","v68","v69","v70","v71","v72","v73","v74","v75","v76","v77","v78","v79","v80","v81","v82","v83","v84","v85","v86","v87","v88","v89","v90","v91","v92","v93","v94","v95","v96","v97","v98","v99","v100","v101","v102","v103","v104","v105","v106","v107","v108","v109","v110","v111","v112","v113","v114","v115","v116","v117","v118","v119","v120","v121","v122","v123","v124","v125","v126","v127","v128","v129","v130","v131","v132","v133","v134","v135","v136","v137","v138","v139","v140","v141","v142","v143","v144","v145","v146","v147","v148","v149","v150","v151","v152","v153","v154","v155","v156","v157","v158","v159","v160","v161","v162","v163","v164","v165","v166","v167","v168","v169","v170","v171","v172","v173","v174","v175","v176","v177","v178","v179","v180","v181","v182","v183","v184","v185","v186","v187","v188","v189","v190","v191","v192","v193","v194","v195","v196","v197","v198","v199","v200","v201","v202","v203","v204","v205","v206","v207","v208","v209","v210","v211","v212","v213","v214","v215","v216","v217","v218","v219","v220","v221","v222","v223","v224","v225","v226","v227","v228","v229","v230","v231","v232","v233","v234","v235","v236","v237","v238","v239","v240","v241","v242","v243","v244","v245","v246","v247"
#define VCLOB_ATT "v0","v1","v2","v3","v4","v5","v6","v7","v8","v9","v10","v11","v12","v13","v14","v15","v16","v17","v18","v19","v20","v21","v22","v23","v24","v25","v26","v27","v28","v29","v30","v31","v32","v33","v34","v35","v36","v37","v38","v39","v40","v41","v42","v43","v44","v45","v46","v47","v48","v49","v50","v51","v52","v53","v54","v55","v56","v57","v58","v59","v60","v61","v62","v63","v64","v65","v66","v67","v68","v69","v70","v71","v72","v73","v74","v75","v76","v77","v78","v79","v80","v81","v82","v83","v84","v85","v86","v87","v88","v89","v90","v91","v92","v93","v94","v95","v96","v97","v98","v99","v100","v101","v102","v103","v104","v105","v106","v107","v108","v109","v110","v111","v112","v113","v114","v115","v116","v117","v118","v119","v120","v121","v122","v123","v124","v125","v126","v127","v128","v129","v130","v131","v132","v133","v134","v135","v136","v137","v138","v139","v140","v141","v142","v143","v144","v145","v146","v147","v148","v149","v150","v151","v152","v153","v154","v155","v156","v157","v158","v159","v160","v161","v162","v163","v164","v165","v166","v167","v168","v169","v170","v171","v172","v173","v174","v175","v176","v177","v178","v179","v180","v181","v182","v183","v184","v185","v186","v187","v188","v189","v190","v191","v192","v193","v194","v195","v196","v197","v198","v199","v200","v201","v202","v203","v204","v205","v206","v207","v208","v209","v210","v211","v212","v213","v214","v215","v216","v217","v218","v219","v220","v221","v222","v223","v236","v237","v238","v239","v240","v241","v242","v243","v244","v245","v246","v247","v248","v249","v252","v253","v254","v255"

#define PRO_STREAM \
  "s_add_u32 m0, %[m0w], 0\n\t" \
  "s_add_u32 %[st], %[sofg], 0\n\t" \
  "buffer_load_dwordx4 v251, %[srdg], %[st] offen lds\n\t" \
  "s_add_u32 m0, %[m0w], 1024\n\t" \
  "s_add_u32 %[st], %[sofg], 0\n\t" \
  "buffer_load_dwordx4 v251, %[srdu], %[st] offen lds\n\t" \
  "s_add_u32 m0, %[m0w], 2048\n\t" \
  "s_add_u32 %[st], %[sofg], 28672\n\t" \
  "buffer_load_dwordx4 v251, %[srdg], %[st] offen lds\n\t" \
  "s_add_u32 m0, %[m0w], 3072\n\t" \
  "s_add_u32 %[st], %[sofg], 28672\n\t" \
  "buffer_load_dwordx4 v251, %[srdu], %[st] offen lds\n\t" \
  "s_mov_b32 %[st], 0\n\t" \
  "buffer_load_dwordx4 v[0:3], v252, %[srdx], %[st] offen offset:0\n\t" \
  "s_mov_b32 %[st], 0\n\t" \
  "buffer_load_dwordx4 v[4:7], v252, %[srdx], %[st] offen offset:1024\n\t" \
  "s_mov_b32 %[st], 0\n\t" \
  "buffer_load_dwordx4 v[8:11], v252, %[srdx], %[st] offen offset:2048\n\t" \
  "s_mov_b32 %[st], 0\n\t" \
  "buffer_load_dwordx4 v[12:15], v252, %[srdx], %[st] offen offset:3072\n\t" \
  "s_mov_b32 %[st], 32768\n\t" \
  "buffer_load_dwordx4 v[16:19], v252, %[srdx], %[st] offen offset:0\n\t" \
  "s_mov_b32 %[st], 32768\n\t" \
  "buffer_load_dwordx4 v[20:23], v252, %[srdx], %[st] offen offset:1024\n\t" \
  "s_mov_b32 %[st], 32768\n\t" \
  "buffer_load_dwordx4 v[24:27], v252, %[srdx], %[st] offen offset:2048\n\t" \
  "s_mov_b32 %[st], 32768\n\t" \
  "buffer_load_dwordx4 v[28:31], v252, %[srdx], %[st] offen offset:3072\n\t" \
  "s_waitcnt vmcnt(0)\n\t" \
  "s_barrier\n\t" \
  "ds_read_b64_tr_b16 v[224:225], v248 offset:0\n\t" \
  "ds_read_b64_tr_b16 v[226:227], v248 offset:256\n\t" \
  "ds_read_b64_tr_b16 v[228:229], v248 offset:512\n\t" \
  "ds_read_b64_tr_b16 v[230:231], v248 offset:768\n\t" \
  "ds_read_b64_tr_b16 v[232:233], v248 offset:1024\n\t" \
  "ds_read_b64_tr_b16 v[234:235], v248 offset:1280\n\t" \
  "ds_read_b64_tr_b16 v[236:237], v248 offset:1536\n\t" \
  "ds_read_b64_tr_b16 v[238:239], v248 offset:1792\n\t" \
  "s_waitcnt lgkmcnt(6)\n\t" \
  "v_mfma_f32_32x32x16_bf16 a[128:143], v[224:227], v[0:3], 0\n\t" \
  "ds_read_b64_tr_b16 v[240:241], v248 offset:4096\n\t" \
  "s_add_u32 m0, %[m0w], 16384\n\t" \
  "s_add_u32 %[st], %[sofg], 229376\n\t" \
  "buffer_load_dwordx4 v251, %[srdg], %[st] offen lds\n\t" \
  "v_mfma_f32_32x32x16_bf16 a[144:159], v[224:227], v[16:19], 0\n\t" \
  "ds_read_b64_tr_b16 v[242:243], v248 offset:4352\n\t" \
  "s_add_u32 m0, %[m0w], 17408\n\t" \
  "s_add_u32 %[st], %[sofg], 229376\n\t" \
  "buffer_load_dwordx4 v251, %[srdu], %[st] offen lds\n\t" \
  "s_waitcnt lgkmcnt(6)\n\t" \
  "v_mfma_f32_32x32x16_bf16 a[160:175], v[228:231], v[0:3], 0\n\t" \
  "ds_read_b64_tr_b16 v[244:245], v248 offset:4608\n\t" \
  "s_add_u32 m0, %[m0w], 18432\n\t" \
  "s_add_u32 %[st], %[sofg], 258048\n\t" \
  "buffer_load_dwordx4 v251, %[srdg], %[st] offen lds\n\t" \
  "v_mfma_f32_32x32x16_bf16 a[176:191], v[228:231], v[16:19], 0\n\t" \
  "ds_read_b64_tr_b16 v[246:247], v248 offset:4864\n\t" \
  "s_add_u32 m0, %[m0w], 19456\n\t" \
  "s_add_u32 %[st], %[sofg], 258048\n\t" \
  "buffer_load_dwordx4 v251, %[srdu], %[st] offen lds\n\t" \
  "s_waitcnt lgkmcnt(6)\n\t" \
  "v_mfma_f32_32x32x16_bf16 a[192:207], v[232:235], v[0:3], 0\n\t" \
  "ds_read_b64_tr_b16 v[224:225], v248 offset:5120\n\t" \
  "s_mov_b32 %[st], 4096\n\t" \
  "buffer_load_dwordx4 v[32:35], v252, %[srdx], %[st] offen offset:0\n\t" \
  "v_mfma_f32_32x32x16_bf16 a[208:223], v[232:235], v[16:19], 0\n\t" \
  "ds_read_b64_tr_b16 v[226:227], v248 offset:5376\n\t" \
  "s_mov_b32 %[st], 4096\n\t" \
  "buffer_load_dwordx4 v[36:39], v252, %[srdx], %[st] offen offset:1024\n\t" \
  "s_waitcnt lgkmcnt(6)\n\t" \
  "v_mfma_f32_32x32x16_bf16 a[224:239], v[236:239], v[0:3], 0\n\t" \
  "ds_read_b64_tr_b16 v[228:229], v248 offset:5632\n\t" \
  "s_mov_b32 %[st], 4096\n\t" \
  "buffer_load_dwordx4 v[40:43], v252, %[srdx], %[st] offen offset:2048\n\t" \
  "v_mfma_f32_32x32x16_bf16 a[240:255], v[236:239], v[16:19], 0\n\t" \
  "ds_read_b64_tr_b16 v[230:231], v248 offset:5888\n\t" \
  "s_mov_b32 %[st], 4096\n\t" \
  "buffer_load_dwordx4 v[44:47], v252, %[srdx], %[st] offen offset:3072\n\t" \
  "s_waitcnt lgkmcnt(6)\n\t" \
  "v_mfma_f32_32x32x16_bf16 a[128:143], v[240:243], v[4:7], a[128:143]\n\t" \
  "ds_read_b64_tr_b16 v[232:233], v248 offset:8192\n\t" \
  "s_mov_b32 %[st], 36864\n\t" \
  "buffer_load_dwordx4 v[48:51], v252, %[srdx], %[st] offen offset:0\n\t" \
  "v_mfma_f32_32x32x16_bf16 a[144:159], v[240:243], v[20:23], a[144:159]\n\t" \
  "ds_read_b64_tr_b16 v[234:235], v248 offset:8448\n\t" \
  "s_mov_b32 %[st], 36864\n\t" \
  "buffer_load_dwordx4 v[52:55], v252, %[srdx], %[st] offen offset:1024\n\t" \
  "s_waitcnt lgkmcnt(6)\n\t" \
  "v_mfma_f32_32x32x16_bf16 a[160:175], v[244:247], v[4:7], a[160:175]\n\t" \
  "ds_read_b64_tr_b16 v[236:237], v248 offset:8704\n\t" \
  "s_mov_b32 %[st], 36864\n\t" \
  "buffer_load_dwordx4 v[56:59], v252, %[srdx], %[st] offen offset:2048\n\t" \
  "v_mfma_f32_32x32x16_bf16 a[176:191], v[244:247], v[20:23], a[176:191]\n\t" \
  "ds_read_b64_tr_b16 v[238:239], v248 offset:8960\n\t" \
  "s_mov_b32 %[st], 36864\n\t" \
  "buffer_load_dwordx4 v[60:63], v252, %[srdx], %[st] offen offset:3072\n\t" \
  "s_waitcnt lgkmcnt(6)\n\t" \
  "v_mfma_f32_32x32x16_bf16 a[192:207], v[224:227], v[4:7], a[192:207]\n\t" \
  "ds_read_b64_tr_b16 v[240:241], v248 offset:9216\n\t" \
  "s_add_u32 m0, %[m0w], 32768\n\t" \
  "s_add_u32 %[st], %[sofg], 458752\n\t" \
  "buffer_load_dwordx4 v251, %[srdg], %[st] offen lds\n\t" \
  "v_mfma_f32_32x32x16_bf16 a[208:223], v[224:227], v[20:23], a[208:223]\n\t" \
  "ds_read_b64_tr_b16 v[242:243], v248 offset:9472\n\t" \
  "s_add_u32 m0, %[m0w], 33792\n\t" \
  "s_add_u32 %[st], %[sofg], 458752\n\t" \
  "buffer_load_dwordx4 v251, %[srdu], %[st] offen lds\n\t" \
  "s_waitcnt lgkmcnt(6)\n\t" \
  "v_mfma_f32_32x32x16_bf16 a[224:239], v[228:231], v[4:7], a[224:239]\n\t" \
  "ds_read_b64_tr_b16 v[244:245], v248 offset:9728\n\t" \
  "s_add_u32 m0, %[m0w], 34816\n\t" \
  "s_add_u32 %[st], %[sofg], 487424\n\t" \
  "buffer_load_dwordx4 v251, %[srdg], %[st] offen lds\n\t" \
  "v_mfma_f32_32x32x16_bf16 a[240:255], v[228:231], v[20:23], a[240:255]\n\t" \
  "ds_read_b64_tr_b16 v[246:247], v248 offset:9984\n\t" \
  "s_add_u32 m0, %[m0w], 35840\n\t" \
  "s_add_u32 %[st], %[sofg], 487424\n\t" \
  "buffer_load_dwordx4 v251, %[srdu], %[st] offen lds\n\t" \
  "s_waitcnt lgkmcnt(6)\n\t" \
  "v_mfma_f32_32x32x16_bf16 a[128:143], v[232:235], v[8:11], a[128:143]\n\t" \
  "ds_read_b64_tr_b16 v[224:225], v248 offset:12288\n\t" \
  "s_waitcnt vmcnt(12)\n\t" \
  "s_barrier\n\t" \
  "v_mfma_f32_32x32x16_bf16 a[144:159], v[232:235], v[24:27], a[144:159]\n\t" \
  "ds_read_b64_tr_b16 v[226:227], v248 offset:12544\n\t" \
  "s_add_u32 m0, %[m0w], 49152\n\t" \
  "s_add_u32 %[st], %[sofg], 688128\n\t" \
  "buffer_load_dwordx4 v251, %[srdg], %[st] offen lds\n\t" \
  "s_waitcnt lgkmcnt(6)\n\t" \
  "v_mfma_f32_32x32x16_bf16 a[160:175], v[236:239], v[8:11], a[160:175]\n\t" \
  "ds_read_b64_tr_b16 v[228:229], v248 offset:12800\n\t" \
  "v_mfma_f32_32x32x16_bf16 a[176:191], v[236:239], v[24:27], a[176:191]\n\t" \
  "ds_read_b64_tr_b16 v[230:231], v248 offset:13056\n\t" \
  "s_add_u32 m0, %[m0w], 50176\n\t" \
  "s_add_u32 %[st], %[sofg], 688128\n\t" \
  "buffer_load_dwordx4 v251, %[srdu], %[st] offen lds\n\t" \
  "s_waitcnt lgkmcnt(6)\n\t" \
  "v_mfma_f32_32x32x16_bf16 a[192:207], v[240:243], v[8:11], a[192:207]\n\t" \
  "ds_read_b64_tr_b16 v[232:233], v248 offset:13312\n\t" \
  "v_mfma_f32_32x32x16_bf16 a[208:223], v[240:243], v[24:27], a[208:223]\n\t" \
  "ds_read_b64_tr_b16 v[234:235], v248 offset:13568\n\t" \
  "s_add_u32 m0, %[m0w], 51200\n\t" \
  "s_add_u32 %[st], %[sofg], 716800\n\t" \
  "buffer_load_dwordx4 v251, %[srdg], %[st] offen lds\n\t" \
  "s_waitcnt lgkmcnt(6)\n\t" \
  "v_mfma_f32_32x32x16_bf16 a[224:239], v[244:247], v[8:11], a[224:239]\n\t" \
  "ds_read_b64_tr_b16 v[236:237], v248 offset:13824\n\t" \
  "v_mfma_f32_32x32x16_bf16 a[240:255], v[244:247], v[24:27], a[240:255]\n\t" \
  "ds_read_b64_tr_b16 v[238:239], v248 offset:14080\n\t" \
  "s_add_u32 m0, %[m0w], 52224\n\t" \
  "s_add_u32 %[st], %[sofg], 716800\n\t" \
  "buffer_load_dwordx4 v251, %[srdu], %[st] offen lds\n\t" \
  "s_waitcnt lgkmcnt(6)\n\t" \
  "v_mfma_f32_32x32x16_bf16 a[128:143], v[224:227], v[12:15], a[128:143]\n\t" \
  "ds_read_b64_tr_b16 v[240:241], v248 offset:16384\n\t" \
  "s_mov_b32 %[st], 8192\n\t" \
  "buffer_load_dwordx4 v[64:67], v252, %[srdx], %[st] offen offset:0\n\t" \
  "v_mfma_f32_32x32x16_bf16 a[144:159], v[224:227], v[28:31], a[144:159]\n\t" \
  "ds_read_b64_tr_b16 v[242:243], v248 offset:16640\n\t" \
  "s_mov_b32 %[st], 8192\n\t" \
  "buffer_load_dwordx4 v[68:71], v252, %[srdx], %[st] offen offset:1024\n\t" \
  "s_waitcnt lgkmcnt(6)\n\t" \
  "v_mfma_f32_32x32x16_bf16 a[160:175], v[228:231], v[12:15], a[160:175]\n\t" \
  "ds_read_b64_tr_b16 v[244:245], v248 offset:16896\n\t" \
  "s_mov_b32 %[st], 8192\n\t" \
  "buffer_load_dwordx4 v[72:75], v252, %[srdx], %[st] offen offset:2048\n\t" \
  "v_mfma_f32_32x32x16_bf16 a[176:191], v[228:231], v[28:31], a[176:191]\n\t" \
  "ds_read_b64_tr_b16 v[246:247], v248 offset:17152\n\t" \
  "s_mov_b32 %[st], 8192\n\t" \
  "buffer_load_dwordx4 v[76:79], v252, %[srdx], %[st] offen offset:3072\n\t" \
  "s_waitcnt lgkmcnt(6)\n\t" \
  "v_mfma_f32_32x32x16_bf16 a[192:207], v[232:235], v[12:15], a[192:207]\n\t" \
  "ds_read_b64_tr_b16 v[224:225], v248 offset:17408\n\t" \
  "s_mov_b32 %[st], 40960\n\t" \
  "buffer_load_dwordx4 v[80:83], v252, %[srdx], %[st] offen offset:0\n\t" \
  "v_mfma_f32_32x32x16_bf16 a[208:223], v[232:235], v[28:31], a[208:223]\n\t" \
  "ds_read_b64_tr_b16 v[226:227], v248 offset:17664\n\t" \
  "s_mov_b32 %[st], 40960\n\t" \
  "buffer_load_dwordx4 v[84:87], v252, %[srdx], %[st] offen offset:1024\n\t" \
  "s_waitcnt lgkmcnt(6)\n\t" \
  "v_mfma_f32_32x32x16_bf16 a[224:239], v[236:239], v[12:15], a[224:239]\n\t" \
  "ds_read_b64_tr_b16 v[228:229], v248 offset:17920\n\t" \
  "s_mov_b32 %[st], 40960\n\t" \
  "buffer_load_dwordx4 v[88:91], v252, %[srdx], %[st] offen offset:2048\n\t" \
  "v_mfma_f32_32x32x16_bf16 a[240:255], v[236:239], v[28:31], a[240:255]\n\t" \
  "ds_read_b64_tr_b16 v[230:231], v248 offset:18176\n\t" \
  "s_mov_b32 %[st], 40960\n\t" \
  "buffer_load_dwordx4 v[92:95], v252, %[srdx], %[st] offen offset:3072\n\t" \
  "s_waitcnt vmcnt(16)\n\t" \
  "s_waitcnt lgkmcnt(6)\n\t" \
  "v_mfma_f32_32x32x16_bf16 a[128:143], v[240:243], v[32:35], a[128:143]\n\t" \
  "ds_read_b64_tr_b16 v[232:233], v248 offset:20480\n\t" \
  "s_add_u32 m0, %[m0w], 65536\n\t" \
  "s_add_u32 %[st], %[sofg], 917504\n\t" \
  "buffer_load_dwordx4 v251, %[srdg], %[st] offen lds\n\t" \
  "v_mfma_f32_32x32x16_bf16 a[144:159], v[240:243], v[48:51], a[144:159]\n\t" \
  "ds_read_b64_tr_b16 v[234:235], v248 offset:20736\n\t" \
  "s_add_u32 m0, %[m0w], 66560\n\t" \
  "s_add_u32 %[st], %[sofg], 917504\n\t" \
  "buffer_load_dwordx4 v251, %[srdu], %[st] offen lds\n\t" \
  "s_waitcnt lgkmcnt(6)\n\t" \
  "v_mfma_f32_32x32x16_bf16 a[160:175], v[244:247], v[32:35], a[160:175]\n\t" \
  "ds_read_b64_tr_b16 v[236:237], v248 offset:20992\n\t" \
  "s_add_u32 m0, %[m0w], 67584\n\t" \
  "s_add_u32 %[st], %[sofg], 946176\n\t" \
  "buffer_load_dwordx4 v251, %[srdg], %[st] offen lds\n\t" \
  "v_mfma_f32_32x32x16_bf16 a[176:191], v[244:247], v[48:51], a[176:191]\n\t" \
  "ds_read_b64_tr_b16 v[238:239], v248 offset:21248\n\t" \
  "s_add_u32 m0, %[m0w], 68608\n\t" \
  "s_add_u32 %[st], %[sofg], 946176\n\t" \
  "buffer_load_dwordx4 v251, %[srdu], %[st] offen lds\n\t" \
  "s_waitcnt lgkmcnt(6)\n\t" \
  "v_mfma_f32_32x32x16_bf16 a[192:207], v[224:227], v[32:35], a[192:207]\n\t" \
  "ds_read_b64_tr_b16 v[240:241], v248 offset:21504\n\t" \
  "s_mov_b32 %[st], 12288\n\t" \
  "buffer_load_dwordx4 v[96:99], v252, %[srdx], %[st] offen offset:0\n\t" \
  "v_mfma_f32_32x32x16_bf16 a[208:223], v[224:227], v[48:51], a[208:223]\n\t" \
  "ds_read_b64_tr_b16 v[242:243], v248 offset:21760\n\t" \
  "s_mov_b32 %[st], 12288\n\t" \
  "buffer_load_dwordx4 v[100:103], v252, %[srdx], %[st] offen offset:1024\n\t" \
  "s_waitcnt lgkmcnt(6)\n\t" \
  "v_mfma_f32_32x32x16_bf16 a[224:239], v[228:231], v[32:35], a[224:239]\n\t" \
  "ds_read_b64_tr_b16 v[244:245], v248 offset:22016\n\t" \
  "s_mov_b32 %[st], 12288\n\t" \
  "buffer_load_dwordx4 v[104:107], v252, %[srdx], %[st] offen offset:2048\n\t" \
  "v_mfma_f32_32x32x16_bf16 a[240:255], v[228:231], v[48:51], a[240:255]\n\t" \
  "ds_read_b64_tr_b16 v[246:247], v248 offset:22272\n\t" \
  "s_mov_b32 %[st], 12288\n\t" \
  "buffer_load_dwordx4 v[108:111], v252, %[srdx], %[st] offen offset:3072\n\t" \
  "s_waitcnt lgkmcnt(6)\n\t" \
  "v_mfma_f32_32x32x16_bf16 a[128:143], v[232:235], v[36:39], a[128:143]\n\t" \
  "ds_read_b64_tr_b16 v[224:225], v248 offset:24576\n\t" \
  "s_mov_b32 %[st], 45056\n\t" \
  "buffer_load_dwordx4 v[112:115], v252, %[srdx], %[st] offen offset:0\n\t" \
  "v_mfma_f32_32x32x16_bf16 a[144:159], v[232:235], v[52:55], a[144:159]\n\t" \
  "ds_read_b64_tr_b16 v[226:227], v248 offset:24832\n\t" \
  "s_mov_b32 %[st], 45056\n\t" \
  "buffer_load_dwordx4 v[116:119], v252, %[srdx], %[st] offen offset:1024\n\t" \
  "s_waitcnt lgkmcnt(6)\n\t" \
  "v_mfma_f32_32x32x16_bf16 a[160:175], v[236:239], v[36:39], a[160:175]\n\t" \
  "ds_read_b64_tr_b16 v[228:229], v248 offset:25088\n\t" \
  "s_mov_b32 %[st], 45056\n\t" \
  "buffer_load_dwordx4 v[120:123], v252, %[srdx], %[st] offen offset:2048\n\t" \
  "v_mfma_f32_32x32x16_bf16 a[176:191], v[236:239], v[52:55], a[176:191]\n\t" \
  "ds_read_b64_tr_b16 v[230:231], v248 offset:25344\n\t" \
  "s_mov_b32 %[st], 45056\n\t" \
  "buffer_load_dwordx4 v[124:127], v252, %[srdx], %[st] offen offset:3072\n\t" \
  "s_waitcnt lgkmcnt(6)\n\t" \
  "v_mfma_f32_32x32x16_bf16 a[192:207], v[240:243], v[36:39], a[192:207]\n\t" \
  "ds_read_b64_tr_b16 v[232:233], v248 offset:25600\n\t" \
  "v_mfma_f32_32x32x16_bf16 a[208:223], v[240:243], v[52:55], a[208:223]\n\t" \
  "ds_read_b64_tr_b16 v[234:235], v248 offset:25856\n\t" \
  "s_waitcnt lgkmcnt(6)\n\t" \
  "v_mfma_f32_32x32x16_bf16 a[224:239], v[244:247], v[36:39], a[224:239]\n\t" \
  "ds_read_b64_tr_b16 v[236:237], v248 offset:26112\n\t" \
  "v_mfma_f32_32x32x16_bf16 a[240:255], v[244:247], v[52:55], a[240:255]\n\t" \
  "ds_read_b64_tr_b16 v[238:239], v248 offset:26368\n\t" \
  "s_waitcnt lgkmcnt(6)\n\t" \
  "v_mfma_f32_32x32x16_bf16 a[128:143], v[224:227], v[40:43], a[128:143]\n\t" \
  "ds_read_b64_tr_b16 v[240:241], v248 offset:28672\n\t" \
  "s_waitcnt vmcnt(24)\n\t" \
  "s_barrier\n\t" \
  "v_mfma_f32_32x32x16_bf16 a[144:159], v[224:227], v[56:59], a[144:159]\n\t" \
  "ds_read_b64_tr_b16 v[242:243], v248 offset:28928\n\t" \
  "s_add_u32 m0, %[m0w], 81920\n\t" \
  "s_add_u32 %[st], %[sofg], 1146880\n\t" \
  "buffer_load_dwordx4 v251, %[srdg], %[st] offen lds\n\t" \
  "s_waitcnt lgkmcnt(6)\n\t" \
  "v_mfma_f32_32x32x16_bf16 a[160:175], v[228:231], v[40:43], a[160:175]\n\t" \
  "ds_read_b64_tr_b16 v[244:245], v248 offset:29184\n\t" \
  "v_mfma_f32_32x32x16_bf16 a[176:191], v[228:231], v[56:59], a[176:191]\n\t" \
  "ds_read_b64_tr_b16 v[246:247], v248 offset:29440\n\t" \
  "s_add_u32 m0, %[m0w], 82944\n\t" \
  "s_add_u32 %[st], %[sofg], 1146880\n\t" \
  "buffer_load_dwordx4 v251, %[srdu], %[st] offen lds\n\t" \
  "s_waitcnt lgkmcnt(6)\n\t" \
  "v_mfma_f32_32x32x16_bf16 a[192:207], v[232:235], v[40:43], a[192:207]\n\t" \
  "ds_read_b64_tr_b16 v[224:225], v248 offset:29696\n\t" \
  "v_mfma_f32_32x32x16_bf16 a[208:223], v[232:235], v[56:59], a[208:223]\n\t" \
  "ds_read_b64_tr_b16 v[226:227], v248 offset:29952\n\t" \
  "s_add_u32 m0, %[m0w], 83968\n\t" \
  "s_add_u32 %[st], %[sofg], 1175552\n\t" \
  "buffer_load_dwordx4 v251, %[srdg], %[st] offen lds\n\t" \
  "s_waitcnt lgkmcnt(6)\n\t" \
  "v_mfma_f32_32x32x16_bf16 a[224:239], v[236:239], v[40:43], a[224:239]\n\t" \
  "ds_read_b64_tr_b16 v[228:229], v248 offset:30208\n\t" \
  "v_mfma_f32_32x32x16_bf16 a[240:255], v[236:239], v[56:59], a[240:255]\n\t" \
  "ds_read_b64_tr_b16 v[230:231], v248 offset:30464\n\t" \
  "s_add_u32 m0, %[m0w], 84992\n\t" \
  "s_add_u32 %[st], %[sofg], 1175552\n\t" \
  "buffer_load_dwordx4 v251, %[srdu], %[st] offen lds\n\t" \
  "s_waitcnt lgkmcnt(6)\n\t" \
  "v_mfma_f32_32x32x16_bf16 a[128:143], v[240:243], v[44:47], a[128:143]\n\t" \
  "ds_read_b64_tr_b16 v[232:233], v248 offset:32768\n\t" \
  "v_mfma_f32_32x32x16_bf16 a[144:159], v[240:243], v[60:63], a[144:159]\n\t" \
  "ds_read_b64_tr_b16 v[234:235], v248 offset:33024\n\t" \
  "s_waitcnt lgkmcnt(6)\n\t" \
  "v_mfma_f32_32x32x16_bf16 a[160:175], v[244:247], v[44:47], a[160:175]\n\t" \
  "ds_read_b64_tr_b16 v[236:237], v248 offset:33280\n\t" \
  "v_mfma_f32_32x32x16_bf16 a[176:191], v[244:247], v[60:63], a[176:191]\n\t" \
  "ds_read_b64_tr_b16 v[238:239], v248 offset:33536\n\t" \
  "s_waitcnt lgkmcnt(6)\n\t" \
  "v_mfma_f32_32x32x16_bf16 a[192:207], v[224:227], v[44:47], a[192:207]\n\t" \
  "ds_read_b64_tr_b16 v[240:241], v248 offset:33792\n\t" \
  "v_mfma_f32_32x32x16_bf16 a[208:223], v[224:227], v[60:63], a[208:223]\n\t" \
  "ds_read_b64_tr_b16 v[242:243], v248 offset:34048\n\t" \
  "s_waitcnt lgkmcnt(6)\n\t" \
  "v_mfma_f32_32x32x16_bf16 a[224:239], v[228:231], v[44:47], a[224:239]\n\t" \
  "ds_read_b64_tr_b16 v[244:245], v248 offset:34304\n\t" \
  "v_mfma_f32_32x32x16_bf16 a[240:255], v[228:231], v[60:63], a[240:255]\n\t" \
  "ds_read_b64_tr_b16 v[246:247], v248 offset:34560\n\t" \
  "s_waitcnt vmcnt(16)\n\t" \
  "s_waitcnt lgkmcnt(6)\n\t" \
  "v_mfma_f32_32x32x16_bf16 a[128:143], v[232:235], v[64:67], a[128:143]\n\t" \
  "ds_read_b64_tr_b16 v[224:225], v248 offset:36864\n\t" \
  "v_mfma_f32_32x32x16_bf16 a[144:159], v[232:235], v[80:83], a[144:159]\n\t" \
  "ds_read_b64_tr_b16 v[226:227], v248 offset:37120\n\t" \
  "s_waitcnt lgkmcnt(6)\n\t" \
  "v_mfma_f32_32x32x16_bf16 a[160:175], v[236:239], v[64:67], a[160:175]\n\t" \
  "ds_read_b64_tr_b16 v[228:229], v248 offset:37376\n\t" \
  "v_mfma_f32_32x32x16_bf16 a[176:191], v[236:239], v[80:83], a[176:191]\n\t" \
  "ds_read_b64_tr_b16 v[230:231], v248 offset:37632\n\t" \
  "s_waitcnt lgkmcnt(6)\n\t" \
  "v_mfma_f32_32x32x16_bf16 a[192:207], v[240:243], v[64:67], a[192:207]\n\t" \
  "ds_read_b64_tr_b16 v[232:233], v248 offset:37888\n\t" \
  "s_mov_b32 %[st], 16384\n\t" \
  "buffer_load_dwordx4 v[128:131], v252, %[srdx], %[st] offen offset:0\n\t" \
  "v_mfma_f32_32x32x16_bf16 a[208:223], v[240:243], v[80:83], a[208:223]\n\t" \
  "ds_read_b64_tr_b16 v[234:235], v248 offset:38144\n\t" \
  "s_mov_b32 %[st], 16384\n\t" \
  "buffer_load_dwordx4 v[132:135], v252, %[srdx], %[st] offen offset:1024\n\t" \
  "s_waitcnt lgkmcnt(6)\n\t" \
  "v_mfma_f32_32x32x16_bf16 a[224:239], v[244:247], v[64:67], a[224:239]\n\t" \
  "ds_read_b64_tr_b16 v[236:237], v248 offset:38400\n\t" \
  "s_mov_b32 %[st], 16384\n\t" \
  "buffer_load_dwordx4 v[136:139], v252, %[srdx], %[st] offen offset:2048\n\t" \
  "v_mfma_f32_32x32x16_bf16 a[240:255], v[244:247], v[80:83], a[240:255]\n\t" \
  "ds_read_b64_tr_b16 v[238:239], v248 offset:38656\n\t" \
  "s_mov_b32 %[st], 16384\n\t" \
  "buffer_load_dwordx4 v[140:143], v252, %[srdx], %[st] offen offset:3072\n\t" \
  "s_waitcnt lgkmcnt(6)\n\t" \
  "v_mfma_f32_32x32x16_bf16 a[128:143], v[224:227], v[68:71], a[128:143]\n\t" \
  "ds_read_b64_tr_b16 v[240:241], v248 offset:40960\n\t" \
  "s_mov_b32 %[st], 49152\n\t" \
  "buffer_load_dwordx4 v[144:147], v252, %[srdx], %[st] offen offset:0\n\t" \
  "v_mfma_f32_32x32x16_bf16 a[144:159], v[224:227], v[84:87], a[144:159]\n\t" \
  "ds_read_b64_tr_b16 v[242:243], v248 offset:41216\n\t" \
  "s_mov_b32 %[st], 49152\n\t" \
  "buffer_load_dwordx4 v[148:151], v252, %[srdx], %[st] offen offset:1024\n\t" \
  "s_waitcnt lgkmcnt(6)\n\t" \
  "v_mfma_f32_32x32x16_bf16 a[160:175], v[228:231], v[68:71], a[160:175]\n\t" \
  "ds_read_b64_tr_b16 v[244:245], v248 offset:41472\n\t" \
  "s_mov_b32 %[st], 49152\n\t" \
  "buffer_load_dwordx4 v[152:155], v252, %[srdx], %[st] offen offset:2048\n\t" \
  "v_mfma_f32_32x32x16_bf16 a[176:191], v[228:231], v[84:87], a[176:191]\n\t" \
  "ds_read_b64_tr_b16 v[246:247], v248 offset:41728\n\t" \
  "s_mov_b32 %[st], 49152\n\t" \
  "buffer_load_dwordx4 v[156:159], v252, %[srdx], %[st] offen offset:3072\n\t" \
  "s_waitcnt lgkmcnt(6)\n\t" \
  "v_mfma_f32_32x32x16_bf16 a[192:207], v[232:235], v[68:71], a[192:207]\n\t" \
  "ds_read_b64_tr_b16 v[224:225], v248 offset:41984\n\t" \
  "v_mfma_f32_32x32x16_bf16 a[208:223], v[232:235], v[84:87], a[208:223]\n\t" \
  "ds_read_b64_tr_b16 v[226:227], v248 offset:42240\n\t" \
  "s_waitcnt lgkmcnt(6)\n\t" \
  "v_mfma_f32_32x32x16_bf16 a[224:239], v[236:239], v[68:71], a[224:239]\n\t" \
  "ds_read_b64_tr_b16 v[228:229], v248 offset:42496\n\t" \
  "v_mfma_f32_32x32x16_bf16 a[240:255], v[236:239], v[84:87], a[240:255]\n\t" \
  "ds_read_b64_tr_b16 v[230:231], v248 offset:42752\n\t" \
  "s_waitcnt lgkmcnt(6)\n\t" \
  "v_mfma_f32_32x32x16_bf16 a[128:143], v[240:243], v[72:75], a[128:143]\n\t" \
  "ds_read_b64_tr_b16 v[232:233], v248 offset:45056\n\t" \
  "s_waitcnt vmcnt(32)\n\t" \
  "s_barrier\n\t" \
  "v_mfma_f32_32x32x16_bf16 a[144:159], v[240:243], v[88:91], a[144:159]\n\t" \
  "ds_read_b64_tr_b16 v[234:235], v248 offset:45312\n\t" \
  "s_add_u32 m0, %[m0w], 0\n\t" \
  "s_add_u32 %[st], %[sofq], 0\n\t" \
  "buffer_load_dwordx4 v250, %[srdq], %[st] offen lds\n\t" \
  "s_waitcnt lgkmcnt(6)\n\t" \
  "v_mfma_f32_32x32x16_bf16 a[160:175], v[244:247], v[72:75], a[160:175]\n\t" \
  "ds_read_b64_tr_b16 v[236:237], v248 offset:45568\n\t" \
  "v_mfma_f32_32x32x16_bf16 a[176:191], v[244:247], v[88:91], a[176:191]\n\t" \
  "ds_read_b64_tr_b16 v[238:239], v248 offset:45824\n\t" \
  "s_add_u32 m0, %[m0w], 1024\n\t" \
  "s_add_u32 %[st], %[sofq], 128\n\t" \
  "buffer_load_dwordx4 v250, %[srdq], %[st] offen lds\n\t" \
  "s_waitcnt lgkmcnt(6)\n\t" \
  "v_mfma_f32_32x32x16_bf16 a[192:207], v[224:227], v[72:75], a[192:207]\n\t" \
  "ds_read_b64_tr_b16 v[240:241], v248 offset:46080\n\t" \
  "v_mfma_f32_32x32x16_bf16 a[208:223], v[224:227], v[88:91], a[208:223]\n\t" \
  "ds_read_b64_tr_b16 v[242:243], v248 offset:46336\n\t" \
  "s_add_u32 m0, %[m0w], 2048\n\t" \
  "s_add_u32 %[st], %[sofq], 65536\n\t" \
  "buffer_load_dwordx4 v250, %[srdq], %[st] offen lds\n\t" \
  "s_waitcnt lgkmcnt(6)\n\t" \
  "v_mfma_f32_32x32x16_bf16 a[224:239], v[228:231], v[72:75], a[224:239]\n\t" \
  "ds_read_b64_tr_b16 v[244:245], v248 offset:46592\n\t" \
  "v_mfma_f32_32x32x16_bf16 a[240:255], v[228:231], v[88:91], a[240:255]\n\t" \
  "ds_read_b64_tr_b16 v[246:247], v248 offset:46848\n\t" \
  "s_add_u32 m0, %[m0w], 3072\n\t" \
  "s_add_u32 %[st], %[sofq], 65664\n\t" \
  "buffer_load_dwordx4 v250, %[srdq], %[st] offen lds\n\t" \
  "s_waitcnt lgkmcnt(6)\n\t" \
  "v_mfma_f32_32x32x16_bf16 a[128:143], v[232:235], v[76:79], a[128:143]\n\t" \
  "ds_read_b64_tr_b16 v[224:225], v248 offset:49152\n\t" \
  "v_mfma_f32_32x32x16_bf16 a[144:159], v[232:235], v[92:95], a[144:159]\n\t" \
  "ds_read_b64_tr_b16 v[226:227], v248 offset:49408\n\t" \
  "s_waitcnt lgkmcnt(6)\n\t" \
  "v_mfma_f32_32x32x16_bf16 a[160:175], v[236:239], v[76:79], a[160:175]\n\t" \
  "ds_read_b64_tr_b16 v[228:229], v248 offset:49664\n\t" \
  "v_mfma_f32_32x32x16_bf16 a[176:191], v[236:239], v[92:95], a[176:191]\n\t" \
  "ds_read_b64_tr_b16 v[230:231], v248 offset:49920\n\t" \
  "s_waitcnt lgkmcnt(6)\n\t" \
  "v_mfma_f32_32x32x16_bf16 a[192:207], v[240:243], v[76:79], a[192:207]\n\t" \
  "ds_read_b64_tr_b16 v[232:233], v248 offset:50176\n\t" \
  "v_mfma_f32_32x32x16_bf16 a[208:223], v[240:243], v[92:95], a[208:223]\n\t" \
  "ds_read_b64_tr_b16 v[234:235], v248 offset:50432\n\t" \
  "s_waitcnt lgkmcnt(6)\n\t" \
  "v_mfma_f32_32x32x16_bf16 a[224:239], v[244:247], v[76:79], a[224:239]\n\t" \
  "ds_read_b64_tr_b16 v[236:237], v248 offset:50688\n\t" \
  "v_mfma_f32_32x32x16_bf16 a[240:255], v[244:247], v[92:95], a[240:255]\n\t" \
  "ds_read_b64_tr_b16 v[238:239], v248 offset:50944\n\t" \
  "s_waitcnt vmcnt(16)\n\t" \
  "s_waitcnt lgkmcnt(6)\n\t" \
  "v_mfma_f32_32x32x16_bf16 a[128:143], v[224:227], v[96:99], a[128:143]\n\t" \
  "ds_read_b64_tr_b16 v[240:241], v248 offset:53248\n\t" \
  "v_mfma_f32_32x32x16_bf16 a[144:159], v[224:227], v[112:115], a[144:159]\n\t" \
  "ds_read_b64_tr_b16 v[242:243], v248 offset:53504\n\t" \
  "s_waitcnt lgkmcnt(6)\n\t" \
  "v_mfma_f32_32x32x16_bf16 a[160:175], v[228:231], v[96:99], a[160:175]\n\t" \
  "ds_read_b64_tr_b16 v[244:245], v248 offset:53760\n\t" \
  "v_mfma_f32_32x32x16_bf16 a[176:191], v[228:231], v[112:115], a[176:191]\n\t" \
  "ds_read_b64_tr_b16 v[246:247], v248 offset:54016\n\t" \
  "s_waitcnt lgkmcnt(6)\n\t" \
  "v_mfma_f32_32x32x16_bf16 a[192:207], v[232:235], v[96:99], a[192:207]\n\t" \
  "ds_read_b64_tr_b16 v[224:225], v248 offset:54272\n\t" \
  "s_mov_b32 %[st], 20480\n\t" \
  "buffer_load_dwordx4 v[160:163], v252, %[srdx], %[st] offen offset:0\n\t" \
  "v_mfma_f32_32x32x16_bf16 a[208:223], v[232:235], v[112:115], a[208:223]\n\t" \
  "ds_read_b64_tr_b16 v[226:227], v248 offset:54528\n\t" \
  "s_mov_b32 %[st], 20480\n\t" \
  "buffer_load_dwordx4 v[164:167], v252, %[srdx], %[st] offen offset:1024\n\t" \
  "s_waitcnt lgkmcnt(6)\n\t" \
  "v_mfma_f32_32x32x16_bf16 a[224:239], v[236:239], v[96:99], a[224:239]\n\t" \
  "ds_read_b64_tr_b16 v[228:229], v248 offset:54784\n\t" \
  "s_mov_b32 %[st], 20480\n\t" \
  "buffer_load_dwordx4 v[168:171], v252, %[srdx], %[st] offen offset:2048\n\t" \
  "v_mfma_f32_32x32x16_bf16 a[240:255], v[236:239], v[112:115], a[240:255]\n\t" \
  "ds_read_b64_tr_b16 v[230:231], v248 offset:55040\n\t" \
  "s_mov_b32 %[st], 20480\n\t" \
  "buffer_load_dwordx4 v[172:175], v252, %[srdx], %[st] offen offset:3072\n\t" \
  "s_waitcnt lgkmcnt(6)\n\t" \
  "v_mfma_f32_32x32x16_bf16 a[128:143], v[240:243], v[100:103], a[128:143]\n\t" \
  "ds_read_b64_tr_b16 v[232:233], v248 offset:57344\n\t" \
  "s_mov_b32 %[st], 53248\n\t" \
  "buffer_load_dwordx4 v[176:179], v252, %[srdx], %[st] offen offset:0\n\t" \
  "v_mfma_f32_32x32x16_bf16 a[144:159], v[240:243], v[116:119], a[144:159]\n\t" \
  "ds_read_b64_tr_b16 v[234:235], v248 offset:57600\n\t" \
  "s_mov_b32 %[st], 53248\n\t" \
  "buffer_load_dwordx4 v[180:183], v252, %[srdx], %[st] offen offset:1024\n\t" \
  "s_waitcnt lgkmcnt(6)\n\t" \
  "v_mfma_f32_32x32x16_bf16 a[160:175], v[244:247], v[100:103], a[160:175]\n\t" \
  "ds_read_b64_tr_b16 v[236:237], v248 offset:57856\n\t" \
  "s_mov_b32 %[st], 53248\n\t" \
  "buffer_load_dwordx4 v[184:187], v252, %[srdx], %[st] offen offset:2048\n\t" \
  "v_mfma_f32_32x32x16_bf16 a[176:191], v[244:247], v[116:119], a[176:191]\n\t" \
  "ds_read_b64_tr_b16 v[238:239], v248 offset:58112\n\t" \
  "s_mov_b32 %[st], 53248\n\t" \
  "buffer_load_dwordx4 v[188:191], v252, %[srdx], %[st] offen offset:3072\n\t" \
  "s_waitcnt lgkmcnt(6)\n\t" \
  "v_mfma_f32_32x32x16_bf16 a[192:207], v[224:227], v[100:103], a[192:207]\n\t" \
  "ds_read_b64_tr_b16 v[240:241], v248 offset:58368\n\t" \
  "v_mfma_f32_32x32x16_bf16 a[208:223], v[224:227], v[116:119], a[208:223]\n\t" \
  "ds_read_b64_tr_b16 v[242:243], v248 offset:58624\n\t" \
  "s_waitcnt lgkmcnt(6)\n\t" \
  "v_mfma_f32_32x32x16_bf16 a[224:239], v[228:231], v[100:103], a[224:239]\n\t" \
  "ds_read_b64_tr_b16 v[244:245], v248 offset:58880\n\t" \
  "v_mfma_f32_32x32x16_bf16 a[240:255], v[228:231], v[116:119], a[240:255]\n\t" \
  "ds_read_b64_tr_b16 v[246:247], v248 offset:59136\n\t" \
  "s_waitcnt lgkmcnt(6)\n\t" \
  "v_mfma_f32_32x32x16_bf16 a[128:143], v[232:235], v[104:107], a[128:143]\n\t" \
  "ds_read_b64_tr_b16 v[224:225], v248 offset:61440\n\t" \
  "s_waitcnt vmcnt(32)\n\t" \
  "s_barrier\n\t" \
  "v_mfma_f32_32x32x16_bf16 a[144:159], v[232:235], v[120:123], a[144:159]\n\t" \
  "ds_read_b64_tr_b16 v[226:227], v248 offset:61696\n\t" \
  "s_add_u32 m0, %[m0w], 16384\n\t" \
  "s_add_u32 %[st], %[sofg], 1376256\n\t" \
  "buffer_load_dwordx4 v251, %[srdg], %[st] offen lds\n\t" \
  "s_waitcnt lgkmcnt(6)\n\t" \
  "v_mfma_f32_32x32x16_bf16 a[160:175], v[236:239], v[104:107], a[160:175]\n\t" \
  "ds_read_b64_tr_b16 v[228:229], v248 offset:61952\n\t" \
  "v_mfma_f32_32x32x16_bf16 a[176:191], v[236:239], v[120:123], a[176:191]\n\t" \
  "ds_read_b64_tr_b16 v[230:231], v248 offset:62208\n\t" \
  "s_add_u32 m0, %[m0w], 17408\n\t" \
  "s_add_u32 %[st], %[sofg], 1376256\n\t" \
  "buffer_load_dwordx4 v251, %[srdu], %[st] offen lds\n\t" \
  "s_waitcnt lgkmcnt(6)\n\t" \
  "v_mfma_f32_32x32x16_bf16 a[192:207], v[240:243], v[104:107], a[192:207]\n\t" \
  "ds_read_b64_tr_b16 v[232:233], v248 offset:62464\n\t" \
  "v_mfma_f32_32x32x16_bf16 a[208:223], v[240:243], v[120:123], a[208:223]\n\t" \
  "ds_read_b64_tr_b16 v[234:235], v248 offset:62720\n\t" \
  "s_add_u32 m0, %[m0w], 18432\n\t" \
  "s_add_u32 %[st], %[sofg], 1404928\n\t" \
  "buffer_load_dwordx4 v251, %[srdg], %[st] offen lds\n\t" \
  "s_waitcnt lgkmcnt(6)\n\t" \
  "v_mfma_f32_32x32x16_bf16 a[224:239], v[244:247], v[104:107], a[224:239]\n\t" \
  "ds_read_b64_tr_b16 v[236:237], v248 offset:62976\n\t" \
  "v_mfma_f32_32x32x16_bf16 a[240:255], v[244:247], v[120:123], a[240:255]\n\t" \
  "ds_read_b64_tr_b16 v[238:239], v248 offset:63232\n\t" \
  "s_add_u32 m0, %[m0w], 19456\n\t" \
  "s_add_u32 %[st], %[sofg], 1404928\n\t" \
  "buffer_load_dwordx4 v251, %[srdu], %[st] offen lds\n\t" \
  "s_waitcnt lgkmcnt(6)\n\t" \
  "v_mfma_f32_32x32x16_bf16 a[128:143], v[224:227], v[108:111], a[128:143]\n\t" \
  "ds_read_b64_tr_b16 v[240:241], v249 offset:0\n\t" \
  "v_mfma_f32_32x32x16_bf16 a[144:159], v[224:227], v[124:127], a[144:159]\n\t" \
  "ds_read_b64_tr_b16 v[242:243], v249 offset:256\n\t" \
  "s_waitcnt lgkmcnt(6)\n\t" \
  "v_mfma_f32_32x32x16_bf16 a[160:175], v[228:231], v[108:111], a[160:175]\n\t" \
  "ds_read_b64_tr_b16 v[244:245], v249 offset:512\n\t" \
  "v_mfma_f32_32x32x16_bf16 a[176:191], v[228:231], v[124:127], a[176:191]\n\t" \
  "ds_read_b64_tr_b16 v[246:247], v249 offset:768\n\t" \
  "s_waitcnt lgkmcnt(6)\n\t" \
  "v_mfma_f32_32x32x16_bf16 a[192:207], v[232:235], v[108:111], a[192:207]\n\t" \
  "ds_read_b64_tr_b16 v[224:225], v249 offset:1024\n\t" \
  "v_mfma_f32_32x32x16_bf16 a[208:223], v[232:235], v[124:127], a[208:223]\n\t" \
  "ds_read_b64_tr_b16 v[226:227], v249 offset:1280\n\t" \
  "s_waitcnt lgkmcnt(6)\n\t" \
  "v_mfma_f32_32x32x16_bf16 a[224:239], v[236:239], v[108:111], a[224:239]\n\t" \
  "ds_read_b64_tr_b16 v[228:229], v249 offset:1536\n\t" \
  "v_mfma_f32_32x32x16_bf16 a[240:255], v[236:239], v[124:127], a[240:255]\n\t" \
  "ds_read_b64_tr_b16 v[230:231], v249 offset:1792\n\t" \
  "s_waitcnt vmcnt(16)\n\t" \
  "s_waitcnt lgkmcnt(6)\n\t" \
  "v_mfma_f32_32x32x16_bf16 a[128:143], v[240:243], v[128:131], a[128:143]\n\t" \
  "ds_read_b64_tr_b16 v[232:233], v249 offset:4096\n\t" \
  "v_mfma_f32_32x32x16_bf16 a[144:159], v[240:243], v[144:147], a[144:159]\n\t" \
  "ds_read_b64_tr_b16 v[234:235], v249 offset:4352\n\t" \
  "s_waitcnt lgkmcnt(6)\n\t" \
  "v_mfma_f32_32x32x16_bf16 a[160:175], v[244:247], v[128:131], a[160:175]\n\t" \
  "ds_read_b64_tr_b16 v[236:237], v249 offset:4608\n\t" \
  "v_mfma_f32_32x32x16_bf16 a[176:191], v[244:247], v[144:147], a[176:191]\n\t" \
  "ds_read_b64_tr_b16 v[238:239], v249 offset:4864\n\t" \
  "s_waitcnt lgkmcnt(6)\n\t" \
  "v_mfma_f32_32x32x16_bf16 a[192:207], v[224:227], v[128:131], a[192:207]\n\t" \
  "ds_read_b64_tr_b16 v[240:241], v249 offset:5120\n\t" \
  "s_mov_b32 %[st], 24576\n\t" \
  "buffer_load_dwordx4 v[192:195], v252, %[srdx], %[st] offen offset:0\n\t" \
  "v_mfma_f32_32x32x16_bf16 a[208:223], v[224:227], v[144:147], a[208:223]\n\t" \
  "ds_read_b64_tr_b16 v[242:243], v249 offset:5376\n\t" \
  "s_mov_b32 %[st], 24576\n\t" \
  "buffer_load_dwordx4 v[196:199], v252, %[srdx], %[st] offen offset:1024\n\t" \
  "s_waitcnt lgkmcnt(6)\n\t" \
  "v_mfma_f32_32x32x16_bf16 a[224:239], v[228:231], v[128:131], a[224:239]\n\t" \
  "ds_read_b64_tr_b16 v[244:245], v249 offset:5632\n\t" \
  "s_mov_b32 %[st], 24576\n\t" \
  "buffer_load_dwordx4 v[200:203], v252, %[srdx], %[st] offen offset:2048\n\t" \
  "v_mfma_f32_32x32x16_bf16 a[240:255], v[228:231], v[144:147], a[240:255]\n\t" \
  "ds_read_b64_tr_b16 v[246:247], v249 offset:5888\n\t" \
  "s_mov_b32 %[st], 24576\n\t" \
  "buffer_load_dwordx4 v[204:207], v252, %[srdx], %[st] offen offset:3072\n\t" \
  "s_waitcnt lgkmcnt(6)\n\t" \
  "v_mfma_f32_32x32x16_bf16 a[128:143], v[232:235], v[132:135], a[128:143]\n\t" \
  "ds_read_b64_tr_b16 v[224:225], v249 offset:8192\n\t" \
  "s_mov_b32 %[st], 57344\n\t" \
  "buffer_load_dwordx4 v[208:211], v252, %[srdx], %[st] offen offset:0\n\t" \
  "v_mfma_f32_32x32x16_bf16 a[144:159], v[232:235], v[148:151], a[144:159]\n\t" \
  "ds_read_b64_tr_b16 v[226:227], v249 offset:8448\n\t" \
  "s_mov_b32 %[st], 57344\n\t" \
  "buffer_load_dwordx4 v[212:215], v252, %[srdx], %[st] offen offset:1024\n\t" \
  "s_waitcnt lgkmcnt(6)\n\t" \
  "v_mfma_f32_32x32x16_bf16 a[160:175], v[236:239], v[132:135], a[160:175]\n\t" \
  "ds_read_b64_tr_b16 v[228:229], v249 offset:8704\n\t" \
  "s_mov_b32 %[st], 57344\n\t" \
  "buffer_load_dwordx4 v[216:219], v252, %[srdx], %[st] offen offset:2048\n\t" \
  "v_mfma_f32_32x32x16_bf16 a[176:191], v[236:239], v[148:151], a[176:191]\n\t" \
  "ds_read_b64_tr_b16 v[230:231], v249 offset:8960\n\t" \
  "s_mov_b32 %[st], 57344\n\t" \
  "buffer_load_dwordx4 v[220:223], v252, %[srdx], %[st] offen offset:3072\n\t" \
  "s_waitcnt lgkmcnt(6)\n\t" \
  "v_mfma_f32_32x32x16_bf16 a[192:207], v[240:243], v[132:135], a[192:207]\n\t" \
  "ds_read_b64_tr_b16 v[232:233], v249 offset:9216\n\t" \
  "v_mfma_f32_32x32x16_bf16 a[208:223], v[240:243], v[148:151], a[208:223]\n\t" \
  "ds_read_b64_tr_b16 v[234:235], v249 offset:9472\n\t" \
  "s_waitcnt lgkmcnt(6)\n\t" \
  "v_mfma_f32_32x32x16_bf16 a[224:239], v[244:247], v[132:135], a[224:239]\n\t" \
  "ds_read_b64_tr_b16 v[236:237], v249 offset:9728\n\t" \
  "v_mfma_f32_32x32x16_bf16 a[240:255], v[244:247], v[148:151], a[240:255]\n\t" \
  "ds_read_b64_tr_b16 v[238:239], v249 offset:9984\n\t" \
  "s_waitcnt lgkmcnt(6)\n\t" \
  "v_mfma_f32_32x32x16_bf16 a[128:143], v[224:227], v[136:139], a[128:143]\n\t" \
  "ds_read_b64_tr_b16 v[240:241], v249 offset:12288\n\t" \
  "s_waitcnt vmcnt(32)\n\t" \
  "s_barrier\n\t" \
  "v_mfma_f32_32x32x16_bf16 a[144:159], v[224:227], v[152:155], a[144:159]\n\t" \
  "ds_read_b64_tr_b16 v[242:243], v249 offset:12544\n\t" \
  "s_add_u32 m0, %[m0w], 32768\n\t" \
  "s_add_u32 %[st], %[sofq], 524288\n\t" \
  "buffer_load_dwordx4 v250, %[srdq], %[st] offen lds\n\t" \
  "s_waitcnt lgkmcnt(6)\n\t" \
  "v_mfma_f32_32x32x16_bf16 a[160:175], v[228:231], v[136:139], a[160:175]\n\t" \
  "ds_read_b64_tr_b16 v[244:245], v249 offset:12800\n\t" \
  "v_mfma_f32_32x32x16_bf16 a[176:191], v[228:231], v[152:155], a[176:191]\n\t" \
  "ds_read_b64_tr_b16 v[246:247], v249 offset:13056\n\t" \
  "s_add_u32 m0, %[m0w], 33792\n\t" \
  "s_add_u32 %[st], %[sofq], 524416\n\t" \
  "buffer_load_dwordx4 v250, %[srdq], %[st] offen lds\n\t" \
  "s_waitcnt lgkmcnt(6)\n\t" \
  "v_mfma_f32_32x32x16_bf16 a[192:207], v[232:235], v[136:139], a[192:207]\n\t" \
  "ds_read_b64_tr_b16 v[224:225], v249 offset:13312\n\t" \
  "v_mfma_f32_32x32x16_bf16 a[208:223], v[232:235], v[152:155], a[208:223]\n\t" \
  "ds_read_b64_tr_b16 v[226:227], v249 offset:13568\n\t" \
  "s_add_u32 m0, %[m0w], 34816\n\t" \
  "s_add_u32 %[st], %[sofq], 589824\n\t" \
  "buffer_load_dwordx4 v250, %[srdq], %[st] offen lds\n\t" \
  "s_waitcnt lgkmcnt(6)\n\t" \
  "v_mfma_f32_32x32x16_bf16 a[224:239], v[236:239], v[136:139], a[224:239]\n\t" \
  "ds_read_b64_tr_b16 v[228:229], v249 offset:13824\n\t" \
  "v_mfma_f32_32x32x16_bf16 a[240:255], v[236:239], v[152:155], a[240:255]\n\t" \
  "ds_read_b64_tr_b16 v[230:231], v249 offset:14080\n\t" \
  "s_add_u32 m0, %[m0w], 35840\n\t" \
  "s_add_u32 %[st], %[sofq], 589952\n\t" \
  "buffer_load_dwordx4 v250, %[srdq], %[st] offen lds\n\t" \
  "s_waitcnt lgkmcnt(6)\n\t" \
  "v_mfma_f32_32x32x16_bf16 a[128:143], v[240:243], v[140:143], a[128:143]\n\t" \
  "ds_read_b64_tr_b16 v[232:233], v249 offset:16384\n\t" \
  "v_mfma_f32_32x32x16_bf16 a[144:159], v[240:243], v[156:159], a[144:159]\n\t" \
  "ds_read_b64_tr_b16 v[234:235], v249 offset:16640\n\t" \
  "s_waitcnt lgkmcnt(6)\n\t" \
  "v_mfma_f32_32x32x16_bf16 a[160:175], v[244:247], v[140:143], a[160:175]\n\t" \
  "ds_read_b64_tr_b16 v[236:237], v249 offset:16896\n\t" \
  "v_mfma_f32_32x32x16_bf16 a[176:191], v[244:247], v[156:159], a[176:191]\n\t" \
  "ds_read_b64_tr_b16 v[238:239], v249 offset:17152\n\t" \
  "s_waitcnt lgkmcnt(6)\n\t" \
  "v_mfma_f32_32x32x16_bf16 a[192:207], v[224:227], v[140:143], a[192:207]\n\t" \
  "ds_read_b64_tr_b16 v[240:241], v249 offset:17408\n\t" \
  "v_mfma_f32_32x32x16_bf16 a[208:223], v[224:227], v[156:159], a[208:223]\n\t" \
  "ds_read_b64_tr_b16 v[242:243], v249 offset:17664\n\t" \
  "s_waitcnt lgkmcnt(6)\n\t" \
  "v_mfma_f32_32x32x16_bf16 a[224:239], v[228:231], v[140:143], a[224:239]\n\t" \
  "ds_read_b64_tr_b16 v[244:245], v249 offset:17920\n\t" \
  "v_mfma_f32_32x32x16_bf16 a[240:255], v[228:231], v[156:159], a[240:255]\n\t" \
  "ds_read_b64_tr_b16 v[246:247], v249 offset:18176\n\t" \
  "s_waitcnt vmcnt(16)\n\t" \
  "s_waitcnt lgkmcnt(6)\n\t" \
  "v_mfma_f32_32x32x16_bf16 a[128:143], v[232:235], v[160:163], a[128:143]\n\t" \
  "ds_read_b64_tr_b16 v[224:225], v249 offset:20480\n\t" \
  "v_mfma_f32_32x32x16_bf16 a[144:159], v[232:235], v[176:179], a[144:159]\n\t" \
  "ds_read_b64_tr_b16 v[226:227], v249 offset:20736\n\t" \
  "s_waitcnt lgkmcnt(6)\n\t" \
  "v_mfma_f32_32x32x16_bf16 a[160:175], v[236:239], v[160:163], a[160:175]\n\t" \
  "ds_read_b64_tr_b16 v[228:229], v249 offset:20992\n\t" \
  "v_mfma_f32_32x32x16_bf16 a[176:191], v[236:239], v[176:179], a[176:191]\n\t" \
  "ds_read_b64_tr_b16 v[230:231], v249 offset:21248\n\t" \
  "s_waitcnt lgkmcnt(6)\n\t" \
  "v_mfma_f32_32x32x16_bf16 a[192:207], v[240:243], v[160:163], a[192:207]\n\t" \
  "ds_read_b64_tr_b16 v[232:233], v249 offset:21504\n\t" \
  "v_mfma_f32_32x32x16_bf16 a[208:223], v[240:243], v[176:179], a[208:223]\n\t" \
  "ds_read_b64_tr_b16 v[234:235], v249 offset:21760\n\t" \
  "s_waitcnt lgkmcnt(6)\n\t" \
  "v_mfma_f32_32x32x16_bf16 a[224:239], v[244:247], v[160:163], a[224:239]\n\t" \
  "ds_read_b64_tr_b16 v[236:237], v249 offset:22016\n\t" \
  "v_mfma_f32_32x32x16_bf16 a[240:255], v[244:247], v[176:179], a[240:255]\n\t" \
  "ds_read_b64_tr_b16 v[238:239], v249 offset:22272\n\t" \
  "s_waitcnt lgkmcnt(6)\n\t" \
  "v_mfma_f32_32x32x16_bf16 a[128:143], v[224:227], v[164:167], a[128:143]\n\t" \
  "ds_read_b64_tr_b16 v[240:241], v249 offset:24576\n\t" \
  "v_mfma_f32_32x32x16_bf16 a[144:159], v[224:227], v[180:183], a[144:159]\n\t" \
  "ds_read_b64_tr_b16 v[242:243], v249 offset:24832\n\t" \
  "s_waitcnt lgkmcnt(6)\n\t" \
  "v_mfma_f32_32x32x16_bf16 a[160:175], v[228:231], v[164:167], a[160:175]\n\t" \
  "ds_read_b64_tr_b16 v[244:245], v249 offset:25088\n\t" \
  "v_mfma_f32_32x32x16_bf16 a[176:191], v[228:231], v[180:183], a[176:191]\n\t" \
  "ds_read_b64_tr_b16 v[246:247], v249 offset:25344\n\t" \
  "s_waitcnt lgkmcnt(6)\n\t" \
  "v_mfma_f32_32x32x16_bf16 a[192:207], v[232:235], v[164:167], a[192:207]\n\t" \
  "ds_read_b64_tr_b16 v[224:225], v249 offset:25600\n\t" \
  "v_mfma_f32_32x32x16_bf16 a[208:223], v[232:235], v[180:183], a[208:223]\n\t" \
  "ds_read_b64_tr_b16 v[226:227], v249 offset:25856\n\t" \
  "s_waitcnt lgkmcnt(6)\n\t" \
  "v_mfma_f32_32x32x16_bf16 a[224:239], v[236:239], v[164:167], a[224:239]\n\t" \
  "ds_read_b64_tr_b16 v[228:229], v249 offset:26112\n\t" \
  "v_mfma_f32_32x32x16_bf16 a[240:255], v[236:239], v[180:183], a[240:255]\n\t" \
  "ds_read_b64_tr_b16 v[230:231], v249 offset:26368\n\t" \
  "s_waitcnt lgkmcnt(6)\n\t" \
  "v_mfma_f32_32x32x16_bf16 a[128:143], v[240:243], v[168:171], a[128:143]\n\t" \
  "ds_read_b64_tr_b16 v[232:233], v249 offset:28672\n\t" \
  "s_waitcnt vmcnt(24)\n\t" \
  "s_barrier\n\t" \
  "v_mfma_f32_32x32x16_bf16 a[144:159], v[240:243], v[184:187], a[144:159]\n\t" \
  "ds_read_b64_tr_b16 v[234:235], v249 offset:28928\n\t" \
  "s_add_u32 m0, %[m0w], 49152\n\t" \
  "s_add_u32 %[st], %[sofg], 1605632\n\t" \
  "buffer_load_dwordx4 v251, %[srdg], %[st] offen lds\n\t" \
  "s_waitcnt lgkmcnt(6)\n\t" \
  "v_mfma_f32_32x32x16_bf16 a[160:175], v[244:247], v[168:171], a[160:175]\n\t" \
  "ds_read_b64_tr_b16 v[236:237], v249 offset:29184\n\t" \
  "v_mfma_f32_32x32x16_bf16 a[176:191], v[244:247], v[184:187], a[176:191]\n\t" \
  "ds_read_b64_tr_b16 v[238:239], v249 offset:29440\n\t" \
  "s_add_u32 m0, %[m0w], 50176\n\t" \
  "s_add_u32 %[st], %[sofg], 1605632\n\t" \
  "buffer_load_dwordx4 v251, %[srdu], %[st] offen lds\n\t" \
  "s_waitcnt lgkmcnt(6)\n\t" \
  "v_mfma_f32_32x32x16_bf16 a[192:207], v[224:227], v[168:171], a[192:207]\n\t" \
  "ds_read_b64_tr_b16 v[240:241], v249 offset:29696\n\t" \
  "v_mfma_f32_32x32x16_bf16 a[208:223], v[224:227], v[184:187], a[208:223]\n\t" \
  "ds_read_b64_tr_b16 v[242:243], v249 offset:29952\n\t" \
  "s_add_u32 m0, %[m0w], 51200\n\t" \
  "s_add_u32 %[st], %[sofg], 1634304\n\t" \
  "buffer_load_dwordx4 v251, %[srdg], %[st] offen lds\n\t" \
  "s_waitcnt lgkmcnt(6)\n\t" \
  "v_mfma_f32_32x32x16_bf16 a[224:239], v[228:231], v[168:171], a[224:239]\n\t" \
  "ds_read_b64_tr_b16 v[244:245], v249 offset:30208\n\t" \
  "v_mfma_f32_32x32x16_bf16 a[240:255], v[228:231], v[184:187], a[240:255]\n\t" \
  "ds_read_b64_tr_b16 v[246:247], v249 offset:30464\n\t" \
  "s_add_u32 m0, %[m0w], 52224\n\t" \
  "s_add_u32 %[st], %[sofg], 1634304\n\t" \
  "buffer_load_dwordx4 v251, %[srdu], %[st] offen lds\n\t" \
  "s_waitcnt lgkmcnt(6)\n\t" \
  "v_mfma_f32_32x32x16_bf16 a[128:143], v[232:235], v[172:175], a[128:143]\n\t" \
  "ds_read_b64_tr_b16 v[224:225], v248 offset:0\n\t" \
  "v_mfma_f32_32x32x16_bf16 a[144:159], v[232:235], v[188:191], a[144:159]\n\t" \
  "ds_read_b64_tr_b16 v[226:227], v248 offset:256\n\t" \
  "s_waitcnt lgkmcnt(6)\n\t" \
  "v_mfma_f32_32x32x16_bf16 a[160:175], v[236:239], v[172:175], a[160:175]\n\t" \
  "ds_read_b64_tr_b16 v[228:229], v248 offset:512\n\t" \
  "v_mfma_f32_32x32x16_bf16 a[176:191], v[236:239], v[188:191], a[176:191]\n\t" \
  "ds_read_b64_tr_b16 v[230:231], v248 offset:768\n\t" \
  "s_waitcnt lgkmcnt(6)\n\t" \
  "v_mfma_f32_32x32x16_bf16 a[192:207], v[240:243], v[172:175], a[192:207]\n\t" \
  "ds_read_b64_tr_b16 v[232:233], v248 offset:1024\n\t" \
  "v_mfma_f32_32x32x16_bf16 a[208:223], v[240:243], v[188:191], a[208:223]\n\t" \
  "ds_read_b64_tr_b16 v[234:235], v248 offset:1280\n\t" \
  "s_waitcnt lgkmcnt(6)\n\t" \
  "v_mfma_f32_32x32x16_bf16 a[224:239], v[244:247], v[172:175], a[224:239]\n\t" \
  "ds_read_b64_tr_b16 v[236:237], v248 offset:1536\n\t" \
  "v_mfma_f32_32x32x16_bf16 a[240:255], v[244:247], v[188:191], a[240:255]\n\t" \
  "ds_read_b64_tr_b16 v[238:239], v248 offset:1792\n\t" \
  "s_waitcnt lgkmcnt(6)\n\t" \
  "v_mfma_f32_32x32x16_bf16 a[0:15], v[224:227], v[0:3], 0\n\t" \
  "ds_read_b64_tr_b16 v[240:241], v248 offset:4096\n\t" \
  "v_mfma_f32_32x32x16_bf16 a[16:31], v[224:227], v[16:19], 0\n\t" \
  "ds_read_b64_tr_b16 v[242:243], v248 offset:4352\n\t" \
  "s_waitcnt lgkmcnt(6)\n\t" \
  "v_mfma_f32_32x32x16_bf16 a[32:47], v[228:231], v[0:3], 0\n\t" \
  "ds_read_b64_tr_b16 v[244:245], v248 offset:4608\n\t" \
  "v_mfma_f32_32x32x16_bf16 a[48:63], v[228:231], v[16:19], 0\n\t" \
  "ds_read_b64_tr_b16 v[246:247], v248 offset:4864\n\t" \
  "s_waitcnt lgkmcnt(6)\n\t" \
  "v_mfma_f32_32x32x16_bf16 a[64:79], v[232:235], v[0:3], 0\n\t" \
  "ds_read_b64_tr_b16 v[224:225], v248 offset:5120\n\t" \
  "v_mfma_f32_32x32x16_bf16 a[80:95], v[232:235], v[16:19], 0\n\t" \
  "ds_read_b64_tr_b16 v[226:227], v248 offset:5376\n\t" \
  "s_waitcnt lgkmcnt(6)\n\t" \
  "v_mfma_f32_32x32x16_bf16 a[96:111], v[236:239], v[0:3], 0\n\t" \
  "ds_read_b64_tr_b16 v[228:229], v248 offset:5632\n\t" \
  "v_mfma_f32_32x32x16_bf16 a[112:127], v[236:239], v[16:19], 0\n\t" \
  "ds_read_b64_tr_b16 v[230:231], v248 offset:5888\n\t" \
  "s_waitcnt lgkmcnt(6)\n\t" \
  "v_mfma_f32_32x32x16_bf16 a[0:15], v[240:243], v[4:7], a[0:15]\n\t" \
  "ds_read_b64_tr_b16 v[232:233], v248 offset:8192\n\t" \
  "v_mfma_f32_32x32x16_bf16 a[16:31], v[240:243], v[20:23], a[16:31]\n\t" \
  "ds_read_b64_tr_b16 v[234:235], v248 offset:8448\n\t" \
  "s_waitcnt lgkmcnt(6)\n\t" \
  "v_mfma_f32_32x32x16_bf16 a[32:47], v[244:247], v[4:7], a[32:47]\n\t" \
  "ds_read_b64_tr_b16 v[236:237], v248 offset:8704\n\t" \
  "v_mfma_f32_32x32x16_bf16 a[48:63], v[244:247], v[20:23], a[48:63]\n\t" \
  "ds_read_b64_tr_b16 v[238:239], v248 offset:8960\n\t" \
  "s_waitcnt lgkmcnt(6)\n\t" \
  "v_mfma_f32_32x32x16_bf16 a[64:79], v[224:227], v[4:7], a[64:79]\n\t" \
  "ds_read_b64_tr_b16 v[240:241], v248 offset:9216\n\t" \
  "v_mfma_f32_32x32x16_bf16 a[80:95], v[224:227], v[20:23], a[80:95]\n\t" \
  "ds_read_b64_tr_b16 v[242:243], v248 offset:9472\n\t" \
  "s_waitcnt lgkmcnt(6)\n\t" \
  "v_mfma_f32_32x32x16_bf16 a[96:111], v[228:231], v[4:7], a[96:111]\n\t" \
  "ds_read_b64_tr_b16 v[244:245], v248 offset:9728\n\t" \
  "v_mfma_f32_32x32x16_bf16 a[112:127], v[228:231], v[20:23], a[112:127]\n\t" \
  "ds_read_b64_tr_b16 v[246:247], v248 offset:9984\n\t" \
  "s_waitcnt lgkmcnt(6)\n\t" \
  "v_mfma_f32_32x32x16_bf16 a[0:15], v[232:235], v[8:11], a[0:15]\n\t" \
  "ds_read_b64_tr_b16 v[224:225], v248 offset:12288\n\t" \
  "s_waitcnt vmcnt(16)\n\t" \
  "s_barrier\n\t" \
  "v_mfma_f32_32x32x16_bf16 a[16:31], v[232:235], v[24:27], a[16:31]\n\t" \
  "ds_read_b64_tr_b16 v[226:227], v248 offset:12544\n\t" \
  "s_add_u32 m0, %[m0w], 65536\n\t" \
  "s_add_u32 %[st], %[sofq], 1048576\n\t" \
  "buffer_load_dwordx4 v250, %[srdq], %[st] offen lds\n\t" \
  "s_waitcnt lgkmcnt(6)\n\t" \
  "v_mfma_f32_32x32x16_bf16 a[32:47], v[236:239], v[8:11], a[32:47]\n\t" \
  "ds_read_b64_tr_b16 v[228:229], v248 offset:12800\n\t" \
  "v_mfma_f32_32x32x16_bf16 a[48:63], v[236:239], v[24:27], a[48:63]\n\t" \
  "ds_read_b64_tr_b16 v[230:231], v248 offset:13056\n\t" \
  "s_add_u32 m0, %[m0w], 66560\n\t" \
  "s_add_u32 %[st], %[sofq], 1048704\n\t" \
  "buffer_load_dwordx4 v250, %[srdq], %[st] offen lds\n\t" \
  "s_waitcnt lgkmcnt(6)\n\t" \
  "v_mfma_f32_32x32x16_bf16 a[64:79], v[240:243], v[8:11], a[64:79]\n\t" \
  "ds_read_b64_tr_b16 v[232:233], v248 offset:13312\n\t" \
  "v_mfma_f32_32x32x16_bf16 a[80:95], v[240:243], v[24:27], a[80:95]\n\t" \
  "ds_read_b64_tr_b16 v[234:235], v248 offset:13568\n\t" \
  "s_add_u32 m0, %[m0w], 67584\n\t" \
  "s_add_u32 %[st], %[sofq], 1114112\n\t" \
  "buffer_load_dwordx4 v250, %[srdq], %[st] offen lds\n\t" \
  "s_waitcnt lgkmcnt(6)\n\t" \
  "v_mfma_f32_32x32x16_bf16 a[96:111], v[244:247], v[8:11], a[96:111]\n\t" \
  "ds_read_b64_tr_b16 v[236:237], v248 offset:13824\n\t" \
  "v_mfma_f32_32x32x16_bf16 a[112:127], v[244:247], v[24:27], a[112:127]\n\t" \
  "ds_read_b64_tr_b16 v[238:239], v248 offset:14080\n\t" \
  "s_add_u32 m0, %[m0w], 68608\n\t" \
  "s_add_u32 %[st], %[sofq], 1114240\n\t" \
  "buffer_load_dwordx4 v250, %[srdq], %[st] offen lds\n\t" \
  "s_waitcnt lgkmcnt(6)\n\t" \
  "v_mfma_f32_32x32x16_bf16 a[0:15], v[224:227], v[12:15], a[0:15]\n\t" \
  "ds_read_b64_tr_b16 v[240:241], v248 offset:16384\n\t" \
  "v_mfma_f32_32x32x16_bf16 a[16:31], v[224:227], v[28:31], a[16:31]\n\t" \
  "ds_read_b64_tr_b16 v[242:243], v248 offset:16640\n\t" \
  "s_waitcnt lgkmcnt(6)\n\t" \
  "v_mfma_f32_32x32x16_bf16 a[32:47], v[228:231], v[12:15], a[32:47]\n\t" \
  "ds_read_b64_tr_b16 v[244:245], v248 offset:16896\n\t" \
  "v_mfma_f32_32x32x16_bf16 a[48:63], v[228:231], v[28:31], a[48:63]\n\t" \
  "ds_read_b64_tr_b16 v[246:247], v248 offset:17152\n\t" \
  "s_waitcnt lgkmcnt(6)\n\t" \
  "v_mfma_f32_32x32x16_bf16 a[64:79], v[232:235], v[12:15], a[64:79]\n\t" \
  "ds_read_b64_tr_b16 v[224:225], v248 offset:17408\n\t" \
  "v_mfma_f32_32x32x16_bf16 a[80:95], v[232:235], v[28:31], a[80:95]\n\t" \
  "ds_read_b64_tr_b16 v[226:227], v248 offset:17664\n\t" \
  "s_waitcnt lgkmcnt(6)\n\t" \
  "v_mfma_f32_32x32x16_bf16 a[96:111], v[236:239], v[12:15], a[96:111]\n\t" \
  "ds_read_b64_tr_b16 v[228:229], v248 offset:17920\n\t" \
  "v_mfma_f32_32x32x16_bf16 a[112:127], v[236:239], v[28:31], a[112:127]\n\t" \
  "ds_read_b64_tr_b16 v[230:231], v248 offset:18176\n\t" \
  "s_waitcnt vmcnt(12)\n\t" \
  "s_waitcnt lgkmcnt(6)\n\t" \
  "v_mfma_f32_32x32x16_bf16 a[128:143], v[240:243], v[192:195], a[128:143]\n\t" \
  "ds_read_b64_tr_b16 v[232:233], v248 offset:20480\n\t" \
  "v_mfma_f32_32x32x16_bf16 a[144:159], v[240:243], v[208:211], a[144:159]\n\t" \
  "ds_read_b64_tr_b16 v[234:235], v248 offset:20736\n\t" \
  "s_waitcnt lgkmcnt(6)\n\t" \
  "v_mfma_f32_32x32x16_bf16 a[160:175], v[244:247], v[192:195], a[160:175]\n\t" \
  "ds_read_b64_tr_b16 v[236:237], v248 offset:20992\n\t" \
  "v_mfma_f32_32x32x16_bf16 a[176:191], v[244:247], v[208:211], a[176:191]\n\t" \
  "ds_read_b64_tr_b16 v[238:239], v248 offset:21248\n\t" \
  "s_waitcnt lgkmcnt(6)\n\t" \
  "v_mfma_f32_32x32x16_bf16 a[192:207], v[224:227], v[192:195], a[192:207]\n\t" \
  "ds_read_b64_tr_b16 v[240:241], v248 offset:21504\n\t" \
  "s_mov_b32 %[st], 28672\n\t" \
  "buffer_load_dwordx4 v[0:3], v252, %[srdx], %[st] offen offset:0\n\t" \
  "v_mfma_f32_32x32x16_bf16 a[208:223], v[224:227], v[208:211], a[208:223]\n\t" \
  "ds_read_b64_tr_b16 v[242:243], v248 offset:21760\n\t" \
  "s_mov_b32 %[st], 28672\n\t" \
  "buffer_load_dwordx4 v[4:7], v252, %[srdx], %[st] offen offset:1024\n\t" \
  "s_waitcnt lgkmcnt(6)\n\t" \
  "v_mfma_f32_32x32x16_bf16 a[224:239], v[228:231], v[192:195], a[224:239]\n\t" \
  "ds_read_b64_tr_b16 v[244:245], v248 offset:22016\n\t" \
  "s_mov_b32 %[st], 28672\n\t" \
  "buffer_load_dwordx4 v[8:11], v252, %[srdx], %[st] offen offset:2048\n\t" \
  "v_mfma_f32_32x32x16_bf16 a[240:255], v[228:231], v[208:211], a[240:255]\n\t" \
  "ds_read_b64_tr_b16 v[246:247], v248 offset:22272\n\t" \
  "s_mov_b32 %[st], 28672\n\t" \
  "buffer_load_dwordx4 v[12:15], v252, %[srdx], %[st] offen offset:3072\n\t" \
  "s_waitcnt lgkmcnt(6)\n\t" \
  "v_mfma_f32_32x32x16_bf16 a[128:143], v[232:235], v[196:199], a[128:143]\n\t" \
  "ds_read_b64_tr_b16 v[224:225], v248 offset:24576\n\t" \
  "s_mov_b32 %[st], 61440\n\t" \
  "buffer_load_dwordx4 v[16:19], v252, %[srdx], %[st] offen offset:0\n\t" \
  "v_mfma_f32_32x32x16_bf16 a[144:159], v[232:235], v[212:215], a[144:159]\n\t" \
  "ds_read_b64_tr_b16 v[226:227], v248 offset:24832\n\t" \
  "s_mov_b32 %[st], 61440\n\t" \
  "buffer_load_dwordx4 v[20:23], v252, %[srdx], %[st] offen offset:1024\n\t" \
  "s_waitcnt lgkmcnt(6)\n\t" \
  "v_mfma_f32_32x32x16_bf16 a[160:175], v[236:239], v[196:199], a[160:175]\n\t" \
  "ds_read_b64_tr_b16 v[228:229], v248 offset:25088\n\t" \
  "s_mov_b32 %[st], 61440\n\t" \
  "buffer_load_dwordx4 v[24:27], v252, %[srdx], %[st] offen offset:2048\n\t" \
  "v_mfma_f32_32x32x16_bf16 a[176:191], v[236:239], v[212:215], a[176:191]\n\t" \
  "ds_read_b64_tr_b16 v[230:231], v248 offset:25344\n\t" \
  "s_mov_b32 %[st], 61440\n\t" \
  "buffer_load_dwordx4 v[28:31], v252, %[srdx], %[st] offen offset:3072\n\t" \
  "s_waitcnt lgkmcnt(6)\n\t" \
  "v_mfma_f32_32x32x16_bf16 a[192:207], v[240:243], v[196:199], a[192:207]\n\t" \
  "ds_read_b64_tr_b16 v[232:233], v248 offset:25600\n\t" \
  "v_mfma_f32_32x32x16_bf16 a[208:223], v[240:243], v[212:215], a[208:223]\n\t" \
  "ds_read_b64_tr_b16 v[234:235], v248 offset:25856\n\t" \
  "s_waitcnt lgkmcnt(6)\n\t" \
  "v_mfma_f32_32x32x16_bf16 a[224:239], v[244:247], v[196:199], a[224:239]\n\t" \
  "ds_read_b64_tr_b16 v[236:237], v248 offset:26112\n\t" \
  "v_mfma_f32_32x32x16_bf16 a[240:255], v[244:247], v[212:215], a[240:255]\n\t" \
  "ds_read_b64_tr_b16 v[238:239], v248 offset:26368\n\t" \
  "s_waitcnt lgkmcnt(6)\n\t" \
  "v_mfma_f32_32x32x16_bf16 a[128:143], v[224:227], v[200:203], a[128:143]\n\t" \
  "ds_read_b64_tr_b16 v[240:241], v248 offset:28672\n\t" \
  "s_waitcnt vmcnt(16)\n\t" \
  "s_barrier\n\t" \
  "v_mfma_f32_32x32x16_bf16 a[144:159], v[224:227], v[216:219], a[144:159]\n\t" \
  "ds_read_b64_tr_b16 v[242:243], v248 offset:28928\n\t" \
  "s_add_u32 m0, %[m0w], 81920\n\t" \
  "s_add_u32 %[st], %[sofq], 1572864\n\t" \
  "buffer_load_dwordx4 v250, %[srdq], %[st] offen lds\n\t" \
  "s_waitcnt lgkmcnt(6)\n\t" \
  "v_mfma_f32_32x32x16_bf16 a[160:175], v[228:231], v[200:203], a[160:175]\n\t" \
  "ds_read_b64_tr_b16 v[244:245], v248 offset:29184\n\t" \
  "v_mfma_f32_32x32x16_bf16 a[176:191], v[228:231], v[216:219], a[176:191]\n\t" \
  "ds_read_b64_tr_b16 v[246:247], v248 offset:29440\n\t" \
  "s_add_u32 m0, %[m0w], 82944\n\t" \
  "s_add_u32 %[st], %[sofq], 1572992\n\t" \
  "buffer_load_dwordx4 v250, %[srdq], %[st] offen lds\n\t" \
  "s_waitcnt lgkmcnt(6)\n\t" \
  "v_mfma_f32_32x32x16_bf16 a[192:207], v[232:235], v[200:203], a[192:207]\n\t" \
  "ds_read_b64_tr_b16 v[224:225], v248 offset:29696\n\t" \
  "v_mfma_f32_32x32x16_bf16 a[208:223], v[232:235], v[216:219], a[208:223]\n\t" \
  "ds_read_b64_tr_b16 v[226:227], v248 offset:29952\n\t" \
  "s_add_u32 m0, %[m0w], 83968\n\t" \
  "s_add_u32 %[st], %[sofq], 1638400\n\t" \
  "buffer_load_dwordx4 v250, %[srdq], %[st] offen lds\n\t" \
  "s_waitcnt lgkmcnt(6)\n\t" \
  "v_mfma_f32_32x32x16_bf16 a[224:239], v[236:239], v[200:203], a[224:239]\n\t" \
  "ds_read_b64_tr_b16 v[228:229], v248 offset:30208\n\t" \
  "v_mfma_f32_32x32x16_bf16 a[240:255], v[236:239], v[216:219], a[240:255]\n\t" \
  "ds_read_b64_tr_b16 v[230:231], v248 offset:30464\n\t" \
  "s_add_u32 m0, %[m0w], 84992\n\t" \
  "s_add_u32 %[st], %[sofq], 1638528\n\t" \
  "buffer_load_dwordx4 v250, %[srdq], %[st] offen lds\n\t" \
  "s_waitcnt lgkmcnt(6)\n\t" \
  "v_mfma_f32_32x32x16_bf16 a[128:143], v[240:243], v[204:207], a[128:143]\n\t" \
  "ds_read_b64_tr_b16 v[232:233], v248 offset:32768\n\t" \
  "v_mfma_f32_32x32x16_bf16 a[144:159], v[240:243], v[220:223], a[144:159]\n\t" \
  "ds_read_b64_tr_b16 v[234:235], v248 offset:33024\n\t" \
  "s_waitcnt lgkmcnt(6)\n\t" \
  "v_mfma_f32_32x32x16_bf16 a[160:175], v[244:247], v[204:207], a[160:175]\n\t" \
  "ds_read_b64_tr_b16 v[236:237], v248 offset:33280\n\t" \
  "v_mfma_f32_32x32x16_bf16 a[176:191], v[244:247], v[220:223], a[176:191]\n\t" \
  "ds_read_b64_tr_b16 v[238:239], v248 offset:33536\n\t" \
  "s_waitcnt lgkmcnt(6)\n\t" \
  "v_mfma_f32_32x32x16_bf16 a[192:207], v[224:227], v[204:207], a[192:207]\n\t" \
  "ds_read_b64_tr_b16 v[240:241], v248 offset:33792\n\t" \
  "v_mfma_f32_32x32x16_bf16 a[208:223], v[224:227], v[220:223], a[208:223]\n\t" \
  "ds_read_b64_tr_b16 v[242:243], v248 offset:34048\n\t" \
  "s_waitcnt lgkmcnt(6)\n\t" \
  "v_mfma_f32_32x32x16_bf16 a[224:239], v[228:231], v[204:207], a[224:239]\n\t" \
  "ds_read_b64_tr_b16 v[244:245], v248 offset:34304\n\t" \
  "v_mfma_f32_32x32x16_bf16 a[240:255], v[228:231], v[220:223], a[240:255]\n\t" \
  "ds_read_b64_tr_b16 v[246:247], v248 offset:34560\n\t" \
  "s_waitcnt lgkmcnt(6)\n\t" \
  "v_mfma_f32_32x32x16_bf16 a[0:15], v[232:235], v[32:35], a[0:15]\n\t" \
  "ds_read_b64_tr_b16 v[224:225], v248 offset:36864\n\t" \
  "v_mfma_f32_32x32x16_bf16 a[16:31], v[232:235], v[48:51], a[16:31]\n\t" \
  "ds_read_b64_tr_b16 v[226:227], v248 offset:37120\n\t" \
  "s_waitcnt lgkmcnt(6)\n\t" \
  "v_mfma_f32_32x32x16_bf16 a[32:47], v[236:239], v[32:35], a[32:47]\n\t" \
  "ds_read_b64_tr_b16 v[228:229], v248 offset:37376\n\t" \
  "v_mfma_f32_32x32x16_bf16 a[48:63], v[236:239], v[48:51], a[48:63]\n\t" \
  "ds_read_b64_tr_b16 v[230:231], v248 offset:37632\n\t" \
  "s_waitcnt lgkmcnt(6)\n\t" \
  "v_mfma_f32_32x32x16_bf16 a[64:79], v[240:243], v[32:35], a[64:79]\n\t" \
  "ds_read_b64_tr_b16 v[232:233], v248 offset:37888\n\t" \
  "v_mfma_f32_32x32x16_bf16 a[80:95], v[240:243], v[48:51], a[80:95]\n\t" \
  "ds_read_b64_tr_b16 v[234:235], v248 offset:38144\n\t" \
  "s_waitcnt lgkmcnt(6)\n\t" \
  "v_mfma_f32_32x32x16_bf16 a[96:111], v[244:247], v[32:35], a[96:111]\n\t" \
  "ds_read_b64_tr_b16 v[236:237], v248 offset:38400\n\t" \
  "v_mfma_f32_32x32x16_bf16 a[112:127], v[244:247], v[48:51], a[112:127]\n\t" \
  "ds_read_b64_tr_b16 v[238:239], v248 offset:38656\n\t" \
  "s_waitcnt lgkmcnt(6)\n\t" \
  "v_mfma_f32_32x32x16_bf16 a[0:15], v[224:227], v[36:39], a[0:15]\n\t" \
  "ds_read_b64_tr_b16 v[240:241], v248 offset:40960\n\t" \
  "v_mfma_f32_32x32x16_bf16 a[16:31], v[224:227], v[52:55], a[16:31]\n\t" \
  "ds_read_b64_tr_b16 v[242:243], v248 offset:41216\n\t" \
  "s_waitcnt lgkmcnt(6)\n\t" \
  "v_mfma_f32_32x32x16_bf16 a[32:47], v[228:231], v[36:39], a[32:47]\n\t" \
  "ds_read_b64_tr_b16 v[244:245], v248 offset:41472\n\t" \
  "v_mfma_f32_32x32x16_bf16 a[48:63], v[228:231], v[52:55], a[48:63]\n\t" \
  "ds_read_b64_tr_b16 v[246:247], v248 offset:41728\n\t" \
  "s_waitcnt lgkmcnt(6)\n\t" \
  "v_mfma_f32_32x32x16_bf16 a[64:79], v[232:235], v[36:39], a[64:79]\n\t" \
  "ds_read_b64_tr_b16 v[224:225], v248 offset:41984\n\t" \
  "v_mfma_f32_32x32x16_bf16 a[80:95], v[232:235], v[52:55], a[80:95]\n\t" \
  "ds_read_b64_tr_b16 v[226:227], v248 offset:42240\n\t" \
  "s_waitcnt lgkmcnt(6)\n\t" \
  "v_mfma_f32_32x32x16_bf16 a[96:111], v[236:239], v[36:39], a[96:111]\n\t" \
  "ds_read_b64_tr_b16 v[228:229], v248 offset:42496\n\t" \
  "v_mfma_f32_32x32x16_bf16 a[112:127], v[236:239], v[52:55], a[112:127]\n\t" \
  "ds_read_b64_tr_b16 v[230:231], v248 offset:42752\n\t" \
  "s_waitcnt lgkmcnt(6)\n\t" \
  "v_mfma_f32_32x32x16_bf16 a[0:15], v[240:243], v[40:43], a[0:15]\n\t" \
  "ds_read_b64_tr_b16 v[232:233], v248 offset:45056\n\t" \
  "s_waitcnt vmcnt(16)\n\t" \
  "s_barrier\n\t" \
  "v_mfma_f32_32x32x16_bf16 a[16:31], v[240:243], v[56:59], a[16:31]\n\t" \
  "ds_read_b64_tr_b16 v[234:235], v248 offset:45312\n\t" \
  "s_add_u32 m0, %[m0w], 0\n\t" \
  "s_add_u32 %[st], %[sofq], 2097152\n\t" \
  "buffer_load_dwordx4 v250, %[srdq], %[st] offen lds\n\t" \
  "s_waitcnt lgkmcnt(6)\n\t" \
  "v_mfma_f32_32x32x16_bf16 a[32:47], v[244:247], v[40:43], a[32:47]\n\t" \
  "ds_read_b64_tr_b16 v[236:237], v248 offset:45568\n\t" \
  "v_mfma_f32_32x32x16_bf16 a[48:63], v[244:247], v[56:59], a[48:63]\n\t" \
  "ds_read_b64_tr_b16 v[238:239], v248 offset:45824\n\t" \
  "s_add_u32 m0, %[m0w], 1024\n\t" \
  "s_add_u32 %[st], %[sofq], 2097280\n\t" \
  "buffer_load_dwordx4 v250, %[srdq], %[st] offen lds\n\t" \
  "s_waitcnt lgkmcnt(6)\n\t" \
  "v_mfma_f32_32x32x16_bf16 a[64:79], v[224:227], v[40:43], a[64:79]\n\t" \
  "ds_read_b64_tr_b16 v[240:241], v248 offset:46080\n\t" \
  "v_mfma_f32_32x32x16_bf16 a[80:95], v[224:227], v[56:59], a[80:95]\n\t" \
  "ds_read_b64_tr_b16 v[242:243], v248 offset:46336\n\t" \
  "s_add_u32 m0, %[m0w], 2048\n\t" \
  "s_add_u32 %[st], %[sofq], 2162688\n\t" \
  "buffer_load_dwordx4 v250, %[srdq], %[st] offen lds\n\t" \
  "s_waitcnt lgkmcnt(6)\n\t" \
  "v_mfma_f32_32x32x16_bf16 a[96:111], v[228:231], v[40:43], a[96:111]\n\t" \
  "ds_read_b64_tr_b16 v[244:245], v248 offset:46592\n\t" \
  "v_mfma_f32_32x32x16_bf16 a[112:127], v[228:231], v[56:59], a[112:127]\n\t" \
  "ds_read_b64_tr_b16 v[246:247], v248 offset:46848\n\t" \
  "s_add_u32 m0, %[m0w], 3072\n\t" \
  "s_add_u32 %[st], %[sofq], 2162816\n\t" \
  "buffer_load_dwordx4 v250, %[srdq], %[st] offen lds\n\t" \
  "s_waitcnt lgkmcnt(6)\n\t" \
  "v_mfma_f32_32x32x16_bf16 a[0:15], v[232:235], v[44:47], a[0:15]\n\t" \
  "ds_read_b64_tr_b16 v[224:225], v248 offset:49152\n\t" \
  "v_mfma_f32_32x32x16_bf16 a[16:31], v[232:235], v[60:63], a[16:31]\n\t" \
  "ds_read_b64_tr_b16 v[226:227], v248 offset:49408\n\t" \
  "s_waitcnt lgkmcnt(6)\n\t" \
  "v_mfma_f32_32x32x16_bf16 a[32:47], v[236:239], v[44:47], a[32:47]\n\t" \
  "ds_read_b64_tr_b16 v[228:229], v248 offset:49664\n\t" \
  "v_mfma_f32_32x32x16_bf16 a[48:63], v[236:239], v[60:63], a[48:63]\n\t" \
  "ds_read_b64_tr_b16 v[230:231], v248 offset:49920\n\t" \
  "s_waitcnt lgkmcnt(6)\n\t" \
  "v_mfma_f32_32x32x16_bf16 a[64:79], v[240:243], v[44:47], a[64:79]\n\t" \
  "ds_read_b64_tr_b16 v[232:233], v248 offset:50176\n\t" \
  "v_mfma_f32_32x32x16_bf16 a[80:95], v[240:243], v[60:63], a[80:95]\n\t" \
  "ds_read_b64_tr_b16 v[234:235], v248 offset:50432\n\t" \
  "s_waitcnt lgkmcnt(6)\n\t" \
  "v_mfma_f32_32x32x16_bf16 a[96:111], v[244:247], v[44:47], a[96:111]\n\t" \
  "ds_read_b64_tr_b16 v[236:237], v248 offset:50688\n\t" \
  "v_mfma_f32_32x32x16_bf16 a[112:127], v[244:247], v[60:63], a[112:127]\n\t" \
  "ds_read_b64_tr_b16 v[238:239], v248 offset:50944\n\t" \
  "s_waitcnt vmcnt(8)\n\t" \
  "s_waitcnt lgkmcnt(6)\n\t" \
  "v_mfma_f32_32x32x16_bf16 a[128:143], v[224:227], v[0:3], a[128:143]\n\t" \
  "ds_read_b64_tr_b16 v[240:241], v248 offset:53248\n\t" \
  "v_mfma_f32_32x32x16_bf16 a[144:159], v[224:227], v[16:19], a[144:159]\n\t" \
  "ds_read_b64_tr_b16 v[242:243], v248 offset:53504\n\t" \
  "s_waitcnt lgkmcnt(6)\n\t" \
  "v_mfma_f32_32x32x16_bf16 a[160:175], v[228:231], v[0:3], a[160:175]\n\t" \
  "ds_read_b64_tr_b16 v[244:245], v248 offset:53760\n\t" \
  "v_mfma_f32_32x32x16_bf16 a[176:191], v[228:231], v[16:19], a[176:191]\n\t" \
  "ds_read_b64_tr_b16 v[246:247], v248 offset:54016\n\t" \
  "s_waitcnt lgkmcnt(6)\n\t" \
  "v_mfma_f32_32x32x16_bf16 a[192:207], v[232:235], v[0:3], a[192:207]\n\t" \
  "ds_read_b64_tr_b16 v[224:225], v248 offset:54272\n\t" \
  "v_mfma_f32_32x32x16_bf16 a[208:223], v[232:235], v[16:19], a[208:223]\n\t" \
  "ds_read_b64_tr_b16 v[226:227], v248 offset:54528\n\t" \
  "s_waitcnt lgkmcnt(6)\n\t" \
  "v_mfma_f32_32x32x16_bf16 a[224:239], v[236:239], v[0:3], a[224:239]\n\t" \
  "ds_read_b64_tr_b16 v[228:229], v248 offset:54784\n\t" \
  "v_mfma_f32_32x32x16_bf16 a[240:255], v[236:239], v[16:19], a[240:255]\n\t" \
  "ds_read_b64_tr_b16 v[230:231], v248 offset:55040\n\t" \
  "s_waitcnt lgkmcnt(6)\n\t" \
  "v_mfma_f32_32x32x16_bf16 a[128:143], v[240:243], v[4:7], a[128:143]\n\t" \
  "ds_read_b64_tr_b16 v[232:233], v248 offset:57344\n\t" \
  "v_mfma_f32_32x32x16_bf16 a[144:159], v[240:243], v[20:23], a[144:159]\n\t" \
  "ds_read_b64_tr_b16 v[234:235], v248 offset:57600\n\t" \
  "s_waitcnt lgkmcnt(6)\n\t" \
  "v_mfma_f32_32x32x16_bf16 a[160:175], v[244:247], v[4:7], a[160:175]\n\t" \
  "ds_read_b64_tr_b16 v[236:237], v248 offset:57856\n\t" \
  "v_mfma_f32_32x32x16_bf16 a[176:191], v[244:247], v[20:23], a[176:191]\n\t" \
  "ds_read_b64_tr_b16 v[238:239], v248 offset:58112\n\t" \
  "s_waitcnt lgkmcnt(6)\n\t" \
  "v_mfma_f32_32x32x16_bf16 a[192:207], v[224:227], v[4:7], a[192:207]\n\t" \
  "ds_read_b64_tr_b16 v[240:241], v248 offset:58368\n\t" \
  "v_mfma_f32_32x32x16_bf16 a[208:223], v[224:227], v[20:23], a[208:223]\n\t" \
  "ds_read_b64_tr_b16 v[242:243], v248 offset:58624\n\t" \
  "s_waitcnt lgkmcnt(6)\n\t" \
  "v_mfma_f32_32x32x16_bf16 a[224:239], v[228:231], v[4:7], a[224:239]\n\t" \
  "ds_read_b64_tr_b16 v[244:245], v248 offset:58880\n\t" \
  "v_mfma_f32_32x32x16_bf16 a[240:255], v[228:231], v[20:23], a[240:255]\n\t" \
  "ds_read_b64_tr_b16 v[246:247], v248 offset:59136\n\t" \
  "s_waitcnt lgkmcnt(6)\n\t" \
  "v_mfma_f32_32x32x16_bf16 a[128:143], v[232:235], v[8:11], a[128:143]\n\t" \
  "ds_read_b64_tr_b16 v[224:225], v248 offset:61440\n\t" \
  "s_waitcnt vmcnt(16)\n\t" \
  "s_barrier\n\t" \
  "v_mfma_f32_32x32x16_bf16 a[144:159], v[232:235], v[24:27], a[144:159]\n\t" \
  "ds_read_b64_tr_b16 v[226:227], v248 offset:61696\n\t" \
  "s_add_u32 m0, %[m0w], 16384\n\t" \
  "s_add_u32 %[st], %[sofq], 2621440\n\t" \
  "buffer_load_dwordx4 v250, %[srdq], %[st] offen lds\n\t" \
  "s_waitcnt lgkmcnt(6)\n\t" \
  "v_mfma_f32_32x32x16_bf16 a[160:175], v[236:239], v[8:11], a[160:175]\n\t" \
  "ds_read_b64_tr_b16 v[228:229], v248 offset:61952\n\t" \
  "v_mfma_f32_32x32x16_bf16 a[176:191], v[236:239], v[24:27], a[176:191]\n\t" \
  "ds_read_b64_tr_b16 v[230:231], v248 offset:62208\n\t" \
  "s_add_u32 m0, %[m0w], 17408\n\t" \
  "s_add_u32 %[st], %[sofq], 2621568\n\t" \
  "buffer_load_dwordx4 v250, %[srdq], %[st] offen lds\n\t" \
  "s_waitcnt lgkmcnt(6)\n\t" \
  "v_mfma_f32_32x32x16_bf16 a[192:207], v[240:243], v[8:11], a[192:207]\n\t" \
  "ds_read_b64_tr_b16 v[232:233], v248 offset:62464\n\t" \
  "v_mfma_f32_32x32x16_bf16 a[208:223], v[240:243], v[24:27], a[208:223]\n\t" \
  "ds_read_b64_tr_b16 v[234:235], v248 offset:62720\n\t" \
  "s_add_u32 m0, %[m0w], 18432\n\t" \
  "s_add_u32 %[st], %[sofq], 2686976\n\t" \
  "buffer_load_dwordx4 v250, %[srdq], %[st] offen lds\n\t" \
  "s_waitcnt lgkmcnt(6)\n\t" \
  "v_mfma_f32_32x32x16_bf16 a[224:239], v[244:247], v[8:11], a[224:239]\n\t" \
  "ds_read_b64_tr_b16 v[236:237], v248 offset:62976\n\t" \
  "v_mfma_f32_32x32x16_bf16 a[240:255], v[244:247], v[24:27], a[240:255]\n\t" \
  "ds_read_b64_tr_b16 v[238:239], v248 offset:63232\n\t" \
  "s_add_u32 m0, %[m0w], 19456\n\t" \
  "s_add_u32 %[st], %[sofq], 2687104\n\t" \
  "buffer_load_dwordx4 v250, %[srdq], %[st] offen lds\n\t" \
  "s_waitcnt lgkmcnt(6)\n\t" \
  "v_mfma_f32_32x32x16_bf16 a[128:143], v[224:227], v[12:15], a[128:143]\n\t" \
  "ds_read_b64_tr_b16 v[240:241], v249 offset:0\n\t" \
  "v_mfma_f32_32x32x16_bf16 a[144:159], v[224:227], v[28:31], a[144:159]\n\t" \
  "ds_read_b64_tr_b16 v[242:243], v249 offset:256\n\t" \
  "s_waitcnt lgkmcnt(6)\n\t" \
  "v_mfma_f32_32x32x16_bf16 a[160:175], v[228:231], v[12:15], a[160:175]\n\t" \
  "ds_read_b64_tr_b16 v[244:245], v249 offset:512\n\t" \
  "v_mfma_f32_32x32x16_bf16 a[176:191], v[228:231], v[28:31], a[176:191]\n\t" \
  "ds_read_b64_tr_b16 v[246:247], v249 offset:768\n\t" \
  "s_waitcnt lgkmcnt(6)\n\t" \
  "v_mfma_f32_32x32x16_bf16 a[192:207], v[232:235], v[12:15], a[192:207]\n\t" \
  "ds_read_b64_tr_b16 v[224:225], v249 offset:1024\n\t" \
  "v_mfma_f32_32x32x16_bf16 a[208:223], v[232:235], v[28:31], a[208:223]\n\t" \
  "ds_read_b64_tr_b16 v[226:227], v249 offset:1280\n\t" \
  "s_waitcnt lgkmcnt(6)\n\t" \
  "v_mfma_f32_32x32x16_bf16 a[224:239], v[236:239], v[12:15], a[224:239]\n\t" \
  "ds_read_b64_tr_b16 v[228:229], v249 offset:1536\n\t" \
  "v_mfma_f32_32x32x16_bf16 a[240:255], v[236:239], v[28:31], a[240:255]\n\t" \
  "ds_read_b64_tr_b16 v[230:231], v249 offset:1792\n\t" \
  "s_waitcnt lgkmcnt(6)\n\t" \
  "v_mfma_f32_32x32x16_bf16 a[0:15], v[240:243], v[64:67], a[0:15]\n\t" \
  "ds_read_b64_tr_b16 v[232:233], v249 offset:4096\n\t" \
  "v_mfma_f32_32x32x16_bf16 a[16:31], v[240:243], v[80:83], a[16:31]\n\t" \
  "ds_read_b64_tr_b16 v[234:235], v249 offset:4352\n\t" \
  "s_waitcnt lgkmcnt(6)\n\t" \
  "v_mfma_f32_32x32x16_bf16 a[32:47], v[244:247], v[64:67], a[32:47]\n\t" \
  "ds_read_b64_tr_b16 v[236:237], v249 offset:4608\n\t" \
  "v_mfma_f32_32x32x16_bf16 a[48:63], v[244:247], v[80:83], a[48:63]\n\t" \
  "ds_read_b64_tr_b16 v[238:239], v249 offset:4864\n\t" \
  "s_waitcnt lgkmcnt(6)\n\t" \
  "v_mfma_f32_32x32x16_bf16 a[64:79], v[224:227], v[64:67], a[64:79]\n\t" \
  "ds_read_b64_tr_b16 v[240:241], v249 offset:5120\n\t" \
  "v_mov_b32 v62, 0xbdd2d3e8\n\t" \
  "v_accvgpr_read_b32 v32, a128\n\t" \
  "v_accvgpr_read_b32 v33, a129\n\t" \
  "v_accvgpr_read_b32 v34, a130\n\t" \
  "v_accvgpr_read_b32 v35, a131\n\t" \
  "v_mfma_f32_32x32x16_bf16 a[80:95], v[224:227], v[80:83], a[80:95]\n\t" \
  "ds_read_b64_tr_b16 v[242:243], v249 offset:5376\n\t" \
  "v_accvgpr_read_b32 v36, a132\n\t" \
  "v_accvgpr_read_b32 v37, a133\n\t" \
  "v_accvgpr_read_b32 v38, a134\n\t" \
  "v_accvgpr_read_b32 v39, a135\n\t" \
  "v_accvgpr_read_b32 v40, a192\n\t" \
  "s_waitcnt lgkmcnt(6)\n\t" \
  "v_mfma_f32_32x32x16_bf16 a[96:111], v[228:231], v[64:67], a[96:111]\n\t" \
  "ds_read_b64_tr_b16 v[244:245], v249 offset:5632\n\t" \
  "v_accvgpr_read_b32 v41, a193\n\t" \
  "v_accvgpr_read_b32 v42, a194\n\t" \
  "v_accvgpr_read_b32 v43, a195\n\t" \
  "v_accvgpr_read_b32 v44, a196\n\t" \
  "v_accvgpr_read_b32 v45, a197\n\t" \
  "v_mfma_f32_32x32x16_bf16 a[112:127], v[228:231], v[80:83], a[112:127]\n\t" \
  "ds_read_b64_tr_b16 v[246:247], v249 offset:5888\n\t" \
  "v_accvgpr_read_b32 v46, a198\n\t" \
  "v_accvgpr_read_b32 v47, a199\n\t" \
  "v_mul_f32 v48, v32, v32\n\t" \
  "v_mul_f32 v49, v33, v33\n\t" \
  "v_mul_f32 v50, v34, v34\n\t" \
  "s_waitcnt lgkmcnt(6)\n\t" \
  "v_mfma_f32_32x32x16_bf16 a[0:15], v[232:235], v[68:71], a[0:15]\n\t" \
  "ds_read_b64_tr_b16 v[224:225], v249 offset:8192\n\t" \
  "v_mul_f32 v51, v35, v35\n\t" \
  "v_mul_f32 v52, v36, v36\n\t" \
  "v_mul_f32 v53, v37, v37\n\t" \
  "v_mul_f32 v54, v38, v38\n\t" \
  "v_mul_f32 v55, v39, v39\n\t" \
  "v_mfma_f32_32x32x16_bf16 a[16:31], v[232:235], v[84:87], a[16:31]\n\t" \
  "ds_read_b64_tr_b16 v[226:227], v249 offset:8448\n\t" \
  "v_fmaak_f32 v48, v62, v48, 0xc0135761\n\t" \
  "v_fmaak_f32 v49, v62, v49, 0xc0135761\n\t" \
  "v_fmaak_f32 v50, v62, v50, 0xc0135761\n\t" \
  "v_fmaak_f32 v51, v62, v51, 0xc0135761\n\t" \
  "v_fmaak_f32 v52, v62, v52, 0xc0135761\n\t" \
  "s_waitcnt lgkmcnt(6)\n\t" \
  "v_mfma_f32_32x32x16_bf16 a[32:47], v[236:239], v[68:71], a[32:47]\n\t" \
  "ds_read_b64_tr_b16 v[228:229], v249 offset:8704\n\t" \
  "v_fmaak_f32 v53, v62, v53, 0xc0135761\n\t" \
  "v_fmaak_f32 v54, v62, v54, 0xc0135761\n\t" \
  "v_fmaak_f32 v55, v62, v55, 0xc0135761\n\t" \
  "v_mul_f32 v48, v48, v32\n\t" \
  "v_mul_f32 v49, v49, v33\n\t" \
  "v_mfma_f32_32x32x16_bf16 a[48:63], v[236:239], v[84:87], a[48:63]\n\t" \
  "ds_read_b64_tr_b16 v[230:231], v249 offset:8960\n\t" \
  "v_mul_f32 v50, v50, v34\n\t" \
  "v_mul_f32 v51, v51, v35\n\t" \
  "v_mul_f32 v52, v52, v36\n\t" \
  "v_mul_f32 v53, v53, v37\n\t" \
  "v_mul_f32 v54, v54, v38\n\t" \
  "s_waitcnt lgkmcnt(6)\n\t" \
  "v_mfma_f32_32x32x16_bf16 a[64:79], v[240:243], v[68:71], a[64:79]\n\t" \
  "ds_read_b64_tr_b16 v[232:233], v249 offset:9216\n\t" \
  "v_mul_f32 v55, v55, v39\n\t" \
  "v_exp_f32 v48, v48\n\t" \
  "v_exp_f32 v49, v49\n\t" \
  "v_mfma_f32_32x32x16_bf16 a[80:95], v[240:243], v[84:87], a[80:95]\n\t" \
  "ds_read_b64_tr_b16 v[234:235], v249 offset:9472\n\t" \
  "v_exp_f32 v50, v50\n\t" \
  "v_exp_f32 v51, v51\n\t" \
  "s_waitcnt lgkmcnt(6)\n\t" \
  "v_mfma_f32_32x32x16_bf16 a[96:111], v[244:247], v[68:71], a[96:111]\n\t" \
  "ds_read_b64_tr_b16 v[236:237], v249 offset:9728\n\t" \
  "v_exp_f32 v52, v52\n\t" \
  "v_exp_f32 v53, v53\n\t" \
  "v_mfma_f32_32x32x16_bf16 a[112:127], v[244:247], v[84:87], a[112:127]\n\t" \
  "ds_read_b64_tr_b16 v[238:239], v249 offset:9984\n\t" \
  "v_exp_f32 v54, v54\n\t" \
  "v_exp_f32 v55, v55\n\t" \
  "s_waitcnt lgkmcnt(6)\n\t" \
  "v_mfma_f32_32x32x16_bf16 a[0:15], v[224:227], v[72:75], a[0:15]\n\t" \
  "ds_read_b64_tr_b16 v[240:241], v249 offset:12288\n\t" \
  "s_waitcnt vmcnt(8)\n\t" \
  "s_barrier\n\t" \
  "v_mul_f32 v32, v32, v40\n\t" \
  "v_mul_f32 v33, v33, v41\n\t" \
  "v_mul_f32 v34, v34, v42\n\t" \
  "v_mul_f32 v35, v35, v43\n\t" \
  "v_mul_f32 v36, v36, v44\n\t" \
  "v_mfma_f32_32x32x16_bf16 a[16:31], v[224:227], v[88:91], a[16:31]\n\t" \
  "ds_read_b64_tr_b16 v[242:243], v249 offset:12544\n\t" \
  "s_add_u32 m0, %[m0w], 32768\n\t" \
  "s_add_u32 %[st], %[sofq], 3145728\n\t" \
  "buffer_load_dwordx4 v250, %[srdq], %[st] offen lds\n\t" \
  "v_mul_f32 v37, v37, v45\n\t" \
  "v_mul_f32 v38, v38, v46\n\t" \
  "s_waitcnt lgkmcnt(6)\n\t" \
  "v_mfma_f32_32x32x16_bf16 a[32:47], v[228:231], v[72:75], a[32:47]\n\t" \
  "ds_read_b64_tr_b16 v[244:245], v249 offset:12800\n\t" \
  "v_mul_f32 v39, v39, v47\n\t" \
  "v_add_f32 v48, 1.0, v48\n\t" \
  "v_add_f32 v49, 1.0, v49\n\t" \
  "v_add_f32 v50, 1.0, v50\n\t" \
  "v_add_f32 v51, 1.0, v51\n\t" \
  "v_mfma_f32_32x32x16_bf16 a[48:63], v[228:231], v[88:91], a[48:63]\n\t" \
  "ds_read_b64_tr_b16 v[246:247], v249 offset:13056\n\t" \
  "s_add_u32 m0, %[m0w], 33792\n\t" \
  "s_add_u32 %[st], %[sofq], 3145856\n\t" \
  "buffer_load_dwordx4 v250, %[srdq], %[st] offen lds\n\t" \
  "v_add_f32 v52, 1.0, v52\n\t" \
  "v_add_f32 v53, 1.0, v53\n\t" \
  "s_waitcnt lgkmcnt(6)\n\t" \
  "v_mfma_f32_32x32x16_bf16 a[64:79], v[232:235], v[72:75], a[64:79]\n\t" \
  "ds_read_b64_tr_b16 v[224:225], v249 offset:13312\n\t" \
  "v_add_f32 v54, 1.0, v54\n\t" \
  "v_add_f32 v55, 1.0, v55\n\t" \
  "v_rcp_f32 v48, v48\n\t" \
  "v_rcp_f32 v49, v49\n\t" \
  "v_mfma_f32_32x32x16_bf16 a[80:95], v[232:235], v[88:91], a[80:95]\n\t" \
  "ds_read_b64_tr_b16 v[226:227], v249 offset:13568\n\t" \
  "s_add_u32 m0, %[m0w], 34816\n\t" \
  "s_add_u32 %[st], %[sofq], 3211264\n\t" \
  "buffer_load_dwordx4 v250, %[srdq], %[st] offen lds\n\t" \
  "v_rcp_f32 v50, v50\n\t" \
  "s_waitcnt lgkmcnt(6)\n\t" \
  "v_mfma_f32_32x32x16_bf16 a[96:111], v[236:239], v[72:75], a[96:111]\n\t" \
  "ds_read_b64_tr_b16 v[228:229], v249 offset:13824\n\t" \
  "v_rcp_f32 v51, v51\n\t" \
  "v_rcp_f32 v52, v52\n\t" \
  "v_mfma_f32_32x32x16_bf16 a[112:127], v[236:239], v[88:91], a[112:127]\n\t" \
  "ds_read_b64_tr_b16 v[230:231], v249 offset:14080\n\t" \
  "s_add_u32 m0, %[m0w], 35840\n\t" \
  "s_add_u32 %[st], %[sofq], 3211392\n\t" \
  "buffer_load_dwordx4 v250, %[srdq], %[st] offen lds\n\t" \
  "v_rcp_f32 v53, v53\n\t" \
  "s_waitcnt lgkmcnt(6)\n\t" \
  "v_mfma_f32_32x32x16_bf16 a[0:15], v[240:243], v[76:79], a[0:15]\n\t" \
  "ds_read_b64_tr_b16 v[232:233], v249 offset:16384\n\t" \
  "v_rcp_f32 v54, v54\n\t" \
  "v_rcp_f32 v55, v55\n\t" \
  "v_mfma_f32_32x32x16_bf16 a[16:31], v[240:243], v[92:95], a[16:31]\n\t" \
  "ds_read_b64_tr_b16 v[234:235], v249 offset:16640\n\t" \
  "v_mul_f32 v32, v32, v48\n\t" \
  "v_mul_f32 v33, v33, v49\n\t" \
  "v_mul_f32 v34, v34, v50\n\t" \
  "v_mul_f32 v35, v35, v51\n\t" \
  "v_mul_f32 v36, v36, v52\n\t" \
  "s_waitcnt lgkmcnt(6)\n\t" \
  "v_mfma_f32_32x32x16_bf16 a[32:47], v[244:247], v[76:79], a[32:47]\n\t" \
  "ds_read_b64_tr_b16 v[236:237], v249 offset:16896\n\t" \
  "v_mul_f32 v37, v37, v53\n\t" \
  "v_mul_f32 v38, v38, v54\n\t" \
  "v_mul_f32 v39, v39, v55\n\t" \
  "v_cvt_pk_bf16_f32 v56, v32, v33\n\t" \
  "v_cvt_pk_bf16_f32 v57, v34, v35\n\t" \
  "v_mfma_f32_32x32x16_bf16 a[48:63], v[244:247], v[92:95], a[48:63]\n\t" \
  "ds_read_b64_tr_b16 v[238:239], v249 offset:17152\n\t" \
  "v_cvt_pk_bf16_f32 v58, v36, v37\n\t" \
  "v_cvt_pk_bf16_f32 v59, v38, v39\n\t" \
  "s_mov_b32 %[st], 0\n\t" \
  "buffer_store_dwordx4 v[56:59], v252, %[srdh], %[st] offen offset:0\n\t" \
  "v_accvgpr_read_b32 v32, a136\n\t" \
  "s_waitcnt lgkmcnt(6)\n\t" \
  "v_mfma_f32_32x32x16_bf16 a[64:79], v[224:227], v[76:79], a[64:79]\n\t" \
  "ds_read_b64_tr_b16 v[240:241], v249 offset:17408\n\t" \
  "v_accvgpr_read_b32 v33, a137\n\t" \
  "v_accvgpr_read_b32 v34, a138\n\t" \
  "v_accvgpr_read_b32 v35, a139\n\t" \
  "v_accvgpr_read_b32 v36, a140\n\t" \
  "v_accvgpr_read_b32 v37, a141\n\t" \
  "v_mfma_f32_32x32x16_bf16 a[80:95], v[224:227], v[92:95], a[80:95]\n\t" \
  "ds_read_b64_tr_b16 v[242:243], v249 offset:17664\n\t" \
  "v_accvgpr_read_b32 v38, a142\n\t" \
  "v_accvgpr_read_b32 v39, a143\n\t" \
  "v_accvgpr_read_b32 v40, a200\n\t" \
  "v_accvgpr_read_b32 v41, a201\n\t" \
  "v_accvgpr_read_b32 v42, a202\n\t" \
  "s_waitcnt lgkmcnt(6)\n\t" \
  "v_mfma_f32_32x32x16_bf16 a[96:111], v[228:231], v[76:79], a[96:111]\n\t" \
  "ds_read_b64_tr_b16 v[244:245], v249 offset:17920\n\t" \
  "v_accvgpr_read_b32 v43, a203\n\t" \
  "v_accvgpr_read_b32 v44, a204\n\t" \
  "v_accvgpr_read_b32 v45, a205\n\t" \
  "v_accvgpr_read_b32 v46, a206\n\t" \
  "v_accvgpr_read_b32 v47, a207\n\t" \
  "v_mfma_f32_32x32x16_bf16 a[112:127], v[228:231], v[92:95], a[112:127]\n\t" \
  "ds_read_b64_tr_b16 v[246:247], v249 offset:18176\n\t" \
  "v_mul_f32 v48, v32, v32\n\t" \
  "v_mul_f32 v49, v33, v33\n\t" \
  "v_mul_f32 v50, v34, v34\n\t" \
  "v_mul_f32 v51, v35, v35\n\t" \
  "v_mul_f32 v52, v36, v36\n\t" \
  "s_waitcnt lgkmcnt(6)\n\t" \
  "v_mfma_f32_32x32x16_bf16 a[0:15], v[232:235], v[96:99], a[0:15]\n\t" \
  "ds_read_b64_tr_b16 v[224:225], v249 offset:20480\n\t" \
  "v_mul_f32 v53, v37, v37\n\t" \
  "v_mul_f32 v54, v38, v38\n\t" \
  "v_mul_f32 v55, v39, v39\n\t" \
  "v_fmaak_f32 v48, v62, v48, 0xc0135761\n\t" \
  "v_fmaak_f32 v49, v62, v49, 0xc0135761\n\t" \
  "v_mfma_f32_32x32x16_bf16 a[16:31], v[232:235], v[112:115], a[16:31]\n\t" \
  "ds_read_b64_tr_b16 v[226:227], v249 offset:20736\n\t" \
  "v_fmaak_f32 v50, v62, v50, 0xc0135761\n\t" \
  "v_fmaak_f32 v51, v62, v51, 0xc0135761\n\t" \
  "v_fmaak_f32 v52, v62, v52, 0xc0135761\n\t" \
  "v_fmaak_f32 v53, v62, v53, 0xc0135761\n\t" \
  "v_fmaak_f32 v54, v62, v54, 0xc0135761\n\t" \
  "s_waitcnt lgkmcnt(6)\n\t" \
  "v_mfma_f32_32x32x16_bf16 a[32:47], v[236:239], v[96:99], a[32:47]\n\t" \
  "ds_read_b64_tr_b16 v[228:229], v249 offset:20992\n\t" \
  "v_fmaak_f32 v55, v62, v55, 0xc0135761\n\t" \
  "v_mul_f32 v48, v48, v32\n\t" \
  "v_mul_f32 v49, v49, v33\n\t" \
  "v_mul_f32 v50, v50, v34\n\t" \
  "v_mul_f32 v51, v51, v35\n\t" \
  "v_mfma_f32_32x32x16_bf16 a[48:63], v[236:239], v[112:115], a[48:63]\n\t" \
  "ds_read_b64_tr_b16 v[230:231], v249 offset:21248\n\t" \
  "v_mul_f32 v52, v52, v36\n\t" \
  "v_mul_f32 v53, v53, v37\n\t" \
  "v_mul_f32 v54, v54, v38\n\t" \
  "v_mul_f32 v55, v55, v39\n\t" \
  "v_exp_f32 v48, v48\n\t" \
  "s_waitcnt lgkmcnt(6)\n\t" \
  "v_mfma_f32_32x32x16_bf16 a[64:79], v[240:243], v[96:99], a[64:79]\n\t" \
  "ds_read_b64_tr_b16 v[232:233], v249 offset:21504\n\t" \
  "v_exp_f32 v49, v49\n\t" \
  "v_exp_f32 v50, v50\n\t" \
  "v_mfma_f32_32x32x16_bf16 a[80:95], v[240:243], v[112:115], a[80:95]\n\t" \
  "ds_read_b64_tr_b16 v[234:235], v249 offset:21760\n\t" \
  "v_exp_f32 v51, v51\n\t" \
  "v_exp_f32 v52, v52\n\t" \
  "s_waitcnt lgkmcnt(6)\n\t" \
  "v_mfma_f32_32x32x16_bf16 a[96:111], v[244:247], v[96:99], a[96:111]\n\t" \
  "ds_read_b64_tr_b16 v[236:237], v249 offset:22016\n\t" \
  "v_exp_f32 v53, v53\n\t" \
  "v_exp_f32 v54, v54\n\t" \
  "v_mfma_f32_32x32x16_bf16 a[112:127], v[244:247], v[112:115], a[112:127]\n\t" \
  "ds_read_b64_tr_b16 v[238:239], v249 offset:22272\n\t" \
  "v_exp_f32 v55, v55\n\t" \
  "v_mul_f32 v32, v32, v40\n\t" \
  "v_mul_f32 v33, v33, v41\n\t" \
  "v_mul_f32 v34, v34, v42\n\t" \
  "s_waitcnt lgkmcnt(6)\n\t" \
  "v_mfma_f32_32x32x16_bf16 a[0:15], v[224:227], v[100:103], a[0:15]\n\t" \
  "ds_read_b64_tr_b16 v[240:241], v249 offset:24576\n\t" \
  "v_mul_f32 v35, v35, v43\n\t" \
  "v_mul_f32 v36, v36, v44\n\t" \
  "v_mul_f32 v37, v37, v45\n\t" \
  "v_mul_f32 v38, v38, v46\n\t" \
  "v_mul_f32 v39, v39, v47\n\t" \
  "v_mfma_f32_32x32x16_bf16 a[16:31], v[224:227], v[116:119], a[16:31]\n\t" \
  "ds_read_b64_tr_b16 v[242:243], v249 offset:24832\n\t" \
  "v_add_f32 v48, 1.0, v48\n\t" \
  "v_add_f32 v49, 1.0, v49\n\t" \
  "v_add_f32 v50, 1.0, v50\n\t" \
  "v_add_f32 v51, 1.0, v51\n\t" \
  "v_add_f32 v52, 1.0, v52\n\t" \
  "s_waitcnt lgkmcnt(6)\n\t" \
  "v_mfma_f32_32x32x16_bf16 a[32:47], v[228:231], v[100:103], a[32:47]\n\t" \
  "ds_read_b64_tr_b16 v[244:245], v249 offset:25088\n\t" \
  "v_add_f32 v53, 1.0, v53\n\t" \
  "v_add_f32 v54, 1.0, v54\n\t" \
  "v_add_f32 v55, 1.0, v55\n\t" \
  "v_rcp_f32 v48, v48\n\t" \
  "v_mfma_f32_32x32x16_bf16 a[48:63], v[228:231], v[116:119], a[48:63]\n\t" \
  "ds_read_b64_tr_b16 v[246:247], v249 offset:25344\n\t" \
  "v_rcp_f32 v49, v49\n\t" \
  "v_rcp_f32 v50, v50\n\t" \
  "s_waitcnt lgkmcnt(6)\n\t" \
  "v_mfma_f32_32x32x16_bf16 a[64:79], v[232:235], v[100:103], a[64:79]\n\t" \
  "ds_read_b64_tr_b16 v[224:225], v249 offset:25600\n\t" \
  "v_rcp_f32 v51, v51\n\t" \
  "v_rcp_f32 v52, v52\n\t" \
  "v_mfma_f32_32x32x16_bf16 a[80:95], v[232:235], v[116:119], a[80:95]\n\t" \
  "ds_read_b64_tr_b16 v[226:227], v249 offset:25856\n\t" \
  "v_rcp_f32 v53, v53\n\t" \
  "v_rcp_f32 v54, v54\n\t" \
  "s_waitcnt lgkmcnt(6)\n\t" \
  "v_mfma_f32_32x32x16_bf16 a[96:111], v[236:239], v[100:103], a[96:111]\n\t" \
  "ds_read_b64_tr_b16 v[228:229], v249 offset:26112\n\t" \
  "v_rcp_f32 v55, v55\n\t" \
  "v_mul_f32 v32, v32, v48\n\t" \
  "v_mul_f32 v33, v33, v49\n\t" \
  "v_mul_f32 v34, v34, v50\n\t" \
  "v_mfma_f32_32x32x16_bf16 a[112:127], v[236:239], v[116:119], a[112:127]\n\t" \
  "ds_read_b64_tr_b16 v[230:231], v249 offset:26368\n\t" \
  "v_mul_f32 v35, v35, v51\n\t" \
  "v_mul_f32 v36, v36, v52\n\t" \
  "v_mul_f32 v37, v37, v53\n\t" \
  "v_mul_f32 v38, v38, v54\n\t" \
  "v_mul_f32 v39, v39, v55\n\t" \
  "s_waitcnt lgkmcnt(6)\n\t" \
  "v_mfma_f32_32x32x16_bf16 a[0:15], v[240:243], v[104:107], a[0:15]\n\t" \
  "ds_read_b64_tr_b16 v[232:233], v249 offset:28672\n\t" \
  "s_waitcnt vmcnt(9)\n\t" \
  "s_barrier\n\t" \
  "v_cvt_pk_bf16_f32 v64, v32, v33\n\t" \
  "v_cvt_pk_bf16_f32 v65, v34, v35\n\t" \
  "v_cvt_pk_bf16_f32 v66, v36, v37\n\t" \
  "v_cvt_pk_bf16_f32 v67, v38, v39\n\t" \
  "s_mov_b32 %[st], 0\n\t" \
  "buffer_store_dwordx4 v[64:67], v252, %[srdh], %[st] offen offset:1024\n\t" \
  "v_mfma_f32_32x32x16_bf16 a[16:31], v[240:243], v[120:123], a[16:31]\n\t" \
  "ds_read_b64_tr_b16 v[234:235], v249 offset:28928\n\t" \
  "s_add_u32 m0, %[m0w], 49152\n\t" \
  "s_add_u32 %[st], %[sofq], 3670016\n\t" \
  "buffer_load_dwordx4 v250, %[srdq], %[st] offen lds\n\t" \
  "v_accvgpr_read_b32 v32, a160\n\t" \
  "v_accvgpr_read_b32 v33, a161\n\t" \
  "s_waitcnt lgkmcnt(6)\n\t" \
  "v_mfma_f32_32x32x16_bf16 a[32:47], v[244:247], v[104:107], a[32:47]\n\t" \
  "ds_read_b64_tr_b16 v[236:237], v249 offset:29184\n\t" \
  "v_accvgpr_read_b32 v34, a162\n\t" \
  "v_accvgpr_read_b32 v35, a163\n\t" \
  "v_accvgpr_read_b32 v36, a164\n\t" \
  "v_accvgpr_read_b32 v37, a165\n\t" \
  "v_accvgpr_read_b32 v38, a166\n\t" \
  "v_mfma_f32_32x32x16_bf16 a[48:63], v[244:247], v[120:123], a[48:63]\n\t" \
  "ds_read_b64_tr_b16 v[238:239], v249 offset:29440\n\t" \
  "s_add_u32 m0, %[m0w], 50176\n\t" \
  "s_add_u32 %[st], %[sofq], 3670144\n\t" \
  "buffer_load_dwordx4 v250, %[srdq], %[st] offen lds\n\t" \
  "v_accvgpr_read_b32 v39, a167\n\t" \
  "v_accvgpr_read_b32 v40, a224\n\t" \
  "s_waitcnt lgkmcnt(6)\n\t" \
  "v_mfma_f32_32x32x16_bf16 a[64:79], v[224:227], v[104:107], a[64:79]\n\t" \
  "ds_read_b64_tr_b16 v[240:241], v249 offset:29696\n\t" \
  "v_accvgpr_read_b32 v41, a225\n\t" \
  "v_accvgpr_read_b32 v42, a226\n\t" \
  "v_accvgpr_read_b32 v43, a227\n\t" \
  "v_accvgpr_read_b32 v44, a228\n\t" \
  "v_accvgpr_read_b32 v45, a229\n\t" \
  "v_mfma_f32_32x32x16_bf16 a[80:95], v[224:227], v[120:123], a[80:95]\n\t" \
  "ds_read_b64_tr_b16 v[242:243], v249 offset:29952\n\t" \
  "s_add_u32 m0, %[m0w], 51200\n\t" \
  "s_add_u32 %[st], %[sofq], 3735552\n\t" \
  "buffer_load_dwordx4 v250, %[srdq], %[st] offen lds\n\t" \
  "v_accvgpr_read_b32 v46, a230\n\t" \
  "v_accvgpr_read_b32 v47, a231\n\t" \
  "s_waitcnt lgkmcnt(6)\n\t" \
  "v_mfma_f32_32x32x16_bf16 a[96:111], v[228:231], v[104:107], a[96:111]\n\t" \
  "ds_read_b64_tr_b16 v[244:245], v249 offset:30208\n\t" \
  "v_mul_f32 v48, v32, v32\n\t" \
  "v_mul_f32 v49, v33, v33\n\t" \
  "v_mul_f32 v50, v34, v34\n\t" \
  "v_mul_f32 v51, v35, v35\n\t" \
  "v_mul_f32 v52, v36, v36\n\t" \
  "v_mfma_f32_32x32x16_bf16 a[112:127], v[228:231], v[120:123], a[112:127]\n\t" \
  "ds_read_b64_tr_b16 v[246:247], v249 offset:30464\n\t" \
  "s_add_u32 m0, %[m0w], 52224\n\t" \
  "s_add_u32 %[st], %[sofq], 3735680\n\t" \
  "buffer_load_dwordx4 v250, %[srdq], %[st] offen lds\n\t" \
  "v_mul_f32 v53, v37, v37\n\t" \
  "v_mul_f32 v54, v38, v38\n\t" \
  "s_waitcnt lgkmcnt(6)\n\t" \
  "v_mfma_f32_32x32x16_bf16 a[0:15], v[232:235], v[108:111], a[0:15]\n\t" \
  "ds_read_b64_tr_b16 v[224:225], v248 offset:0\n\t" \
  "v_mul_f32 v55, v39, v39\n\t" \
  "v_fmaak_f32 v48, v62, v48, 0xc0135761\n\t" \
  "v_fmaak_f32 v49, v62, v49, 0xc0135761\n\t" \
  "v_fmaak_f32 v50, v62, v50, 0xc0135761\n\t" \
  "v_fmaak_f32 v51, v62, v51, 0xc0135761\n\t" \
  "v_mfma_f32_32x32x16_bf16 a[16:31], v[232:235], v[124:127], a[16:31]\n\t" \
  "ds_read_b64_tr_b16 v[226:227], v248 offset:256\n\t" \
  "v_fmaak_f32 v52, v62, v52, 0xc0135761\n\t" \
  "v_fmaak_f32 v53, v62, v53, 0xc0135761\n\t" \
  "v_fmaak_f32 v54, v62, v54, 0xc0135761\n\t" \
  "v_fmaak_f32 v55, v62, v55, 0xc0135761\n\t" \
  "v_mul_f32 v48, v48, v32\n\t" \
  "s_waitcnt lgkmcnt(6)\n\t" \
  "v_mfma_f32_32x32x16_bf16 a[32:47], v[236:239], v[108:111], a[32:47]\n\t" \
  "ds_read_b64_tr_b16 v[228:229], v248 offset:512\n\t" \
  "v_mul_f32 v49, v49, v33\n\t" \
  "v_mul_f32 v50, v50, v34\n\t" \
  "v_mul_f32 v51, v51, v35\n\t" \
  "v_mul_f32 v52, v52, v36\n\t" \
  "v_mul_f32 v53, v53, v37\n\t" \
  "v_mfma_f32_32x32x16_bf16 a[48:63], v[236:239], v[124:127], a[48:63]\n\t" \
  "ds_read_b64_tr_b16 v[230:231], v248 offset:768\n\t" \
  "v_mul_f32 v54, v54, v38\n\t" \
  "v_mul_f32 v55, v55, v39\n\t" \
  "v_exp_f32 v48, v48\n\t" \
  "v_exp_f32 v49, v49\n\t" \
  "s_waitcnt lgkmcnt(6)\n\t" \
  "v_mfma_f32_32x32x16_bf16 a[64:79], v[240:243], v[108:111], a[64:79]\n\t" \
  "ds_read_b64_tr_b16 v[232:233], v248 offset:1024\n\t" \
  "v_exp_f32 v50, v50\n\t" \
  "v_exp_f32 v51, v51\n\t" \
  "v_mfma_f32_32x32x16_bf16 a[80:95], v[240:243], v[124:127], a[80:95]\n\t" \
  "ds_read_b64_tr_b16 v[234:235], v248 offset:1280\n\t" \
  "v_exp_f32 v52, v52\n\t" \
  "v_exp_f32 v53, v53\n\t" \
  "s_waitcnt lgkmcnt(6)\n\t" \
  "v_mfma_f32_32x32x16_bf16 a[96:111], v[244:247], v[108:111], a[96:111]\n\t" \
  "ds_read_b64_tr_b16 v[236:237], v248 offset:1536\n\t" \
  "v_exp_f32 v54, v54\n\t" \
  "v_exp_f32 v55, v55\n\t" \
  "v_mfma_f32_32x32x16_bf16 a[112:127], v[244:247], v[124:127], a[112:127]\n\t" \
  "ds_read_b64_tr_b16 v[238:239], v248 offset:1792\n\t" \
  "v_mul_f32 v32, v32, v40\n\t" \
  "v_mul_f32 v33, v33, v41\n\t" \
  "v_mul_f32 v34, v34, v42\n\t" \
  "v_mul_f32 v35, v35, v43\n\t" \
  "v_mul_f32 v36, v36, v44\n\t" \
  "s_waitcnt lgkmcnt(6)\n\t" \
  "v_mfma_f32_32x32x16_bf16 a[0:15], v[224:227], v[128:131], a[0:15]\n\t" \
  "ds_read_b64_tr_b16 v[240:241], v248 offset:4096\n\t" \
  "v_and_b32 v96, 0x7f, v250\n\t" \
  "v_lshrrev_b32 v97, 13, v250\n\t" \
  "v_lshl_add_u32 v96, v97, 10, v96\n\t" \
  "v_mul_f32 v37, v37, v45\n\t" \
  "v_mul_f32 v38, v38, v46\n\t" \
  "v_mul_f32 v39, v39, v47\n\t" \
  "v_add_f32 v48, 1.0, v48\n\t" \
  "v_add_f32 v49, 1.0, v49\n\t" \
  "v_mfma_f32_32x32x16_bf16 a[16:31], v[224:227], v[144:147], a[16:31]\n\t" \
  "ds_read_b64_tr_b16 v[242:243], v248 offset:4352\n\t" \
  "v_add_f32 v50, 1.0, v50\n\t" \
  "v_add_f32 v51, 1.0, v51\n\t" \
  "v_add_f32 v52, 1.0, v52\n\t" \
  "v_add_f32 v53, 1.0, v53\n\t" \
  "v_add_f32 v54, 1.0, v54\n\t" \
  "s_waitcnt lgkmcnt(6)\n\t" \
  "v_mfma_f32_32x32x16_bf16 a[32:47], v[228:231], v[128:131], a[32:47]\n\t" \
  "ds_read_b64_tr_b16 v[244:245], v248 offset:4608\n\t" \
  "v_add_f32 v55, 1.0, v55\n\t" \
  "v_rcp_f32 v48, v48\n\t" \
  "v_rcp_f32 v49, v49\n\t" \
  "v_mfma_f32_32x32x16_bf16 a[48:63], v[228:231], v[144:147], a[48:63]\n\t" \
  "ds_read_b64_tr_b16 v[246:247], v248 offset:4864\n\t" \
  "v_rcp_f32 v50, v50\n\t" \
  "v_rcp_f32 v51, v51\n\t" \
  "s_waitcnt lgkmcnt(6)\n\t" \
  "v_mfma_f32_32x32x16_bf16 a[64:79], v[232:235], v[128:131], a[64:79]\n\t" \
  "ds_read_b64_tr_b16 v[224:225], v248 offset:5120\n\t" \
  "v_rcp_f32 v52, v52\n\t" \
  "v_rcp_f32 v53, v53\n\t" \
  "v_mfma_f32_32x32x16_bf16 a[80:95], v[232:235], v[144:147], a[80:95]\n\t" \
  "ds_read_b64_tr_b16 v[226:227], v248 offset:5376\n\t" \
  "v_rcp_f32 v54, v54\n\t" \
  "v_rcp_f32 v55, v55\n\t" \
  "s_waitcnt lgkmcnt(6)\n\t" \
  "v_mfma_f32_32x32x16_bf16 a[96:111], v[236:239], v[128:131], a[96:111]\n\t" \
  "ds_read_b64_tr_b16 v[228:229], v248 offset:5632\n\t" \
  "v_mul_f32 v32, v32, v48\n\t" \
  "v_mul_f32 v33, v33, v49\n\t" \
  "v_mul_f32 v34, v34, v50\n\t" \
  "v_mul_f32 v35, v35, v51\n\t" \
  "v_mul_f32 v36, v36, v52\n\t" \
  "v_mfma_f32_32x32x16_bf16 a[112:127], v[236:239], v[144:147], a[112:127]\n\t" \
  "ds_read_b64_tr_b16 v[230:231], v248 offset:5888\n\t" \
  "v_mul_f32 v37, v37, v53\n\t" \
  "v_mul_f32 v38, v38, v54\n\t" \
  "v_mul_f32 v39, v39, v55\n\t" \
  "v_cvt_pk_bf16_f32 v56, v32, v33\n\t" \
  "v_cvt_pk_bf16_f32 v57, v34, v35\n\t" \
  "s_waitcnt lgkmcnt(6)\n\t" \
  "v_mfma_f32_32x32x16_bf16 a[0:15], v[240:243], v[132:135], a[0:15]\n\t" \
  "ds_read_b64_tr_b16 v[232:233], v248 offset:8192\n\t" \
  "v_cvt_pk_bf16_f32 v58, v36, v37\n\t" \
  "v_cvt_pk_bf16_f32 v59, v38, v39\n\t" \
  "s_mov_b32 %[st], 0\n\t" \
  "buffer_store_dwordx4 v[56:59], v252, %[srdh], %[st] offen offset:2048\n\t" \
  "v_accvgpr_read_b32 v32, a168\n\t" \
  "v_mfma_f32_32x32x16_bf16 a[16:31], v[240:243], v[148:151], a[16:31]\n\t" \
  "ds_read_b64_tr_b16 v[234:235], v248 offset:8448\n\t" \
  "v_accvgpr_read_b32 v33, a169\n\t" \
  "v_accvgpr_read_b32 v34, a170\n\t" \
  "v_accvgpr_read_b32 v35, a171\n\t" \
  "v_accvgpr_read_b32 v36, a172\n\t" \
  "v_accvgpr_read_b32 v37, a173\n\t" \
  "s_waitcnt lgkmcnt(6)\n\t" \
  "v_mfma_f32_32x32x16_bf16 a[32:47], v[244:247], v[132:135], a[32:47]\n\t" \
  "ds_read_b64_tr_b16 v[236:237], v248 offset:8704\n\t" \
  "v_accvgpr_read_b32 v38, a174\n\t" \
  "v_accvgpr_read_b32 v39, a175\n\t" \
  "v_accvgpr_read_b32 v40, a232\n\t" \
  "v_accvgpr_read_b32 v41, a233\n\t" \
  "v_accvgpr_read_b32 v42, a234\n\t" \
  "v_mfma_f32_32x32x16_bf16 a[48:63], v[244:247], v[148:151], a[48:63]\n\t" \
  "ds_read_b64_tr_b16 v[238:239], v248 offset:8960\n\t" \
  "v_accvgpr_read_b32 v43, a235\n\t" \
  "v_accvgpr_read_b32 v44, a236\n\t" \
  "v_accvgpr_read_b32 v45, a237\n\t" \
  "v_accvgpr_read_b32 v46, a238\n\t" \
  "v_accvgpr_read_b32 v47, a239\n\t" \
  "s_waitcnt lgkmcnt(6)\n\t" \
  "v_mfma_f32_32x32x16_bf16 a[64:79], v[224:227], v[132:135], a[64:79]\n\t" \
  "ds_read_b64_tr_b16 v[240:241], v248 offset:9216\n\t" \
  "v_mul_f32 v48, v32, v32\n\t" \
  "v_mul_f32 v49, v33, v33\n\t" \
  "v_mul_f32 v50, v34, v34\n\t" \
  "v_mul_f32 v51, v35, v35\n\t" \
  "v_mul_f32 v52, v36, v36\n\t" \
  "v_mfma_f32_32x32x16_bf16 a[80:95], v[224:227], v[148:151], a[80:95]\n\t" \
  "ds_read_b64_tr_b16 v[242:243], v248 offset:9472\n\t" \
  "v_mul_f32 v53, v37, v37\n\t" \
  "v_mul_f32 v54, v38, v38\n\t" \
  "v_mul_f32 v55, v39, v39\n\t" \
  "v_fmaak_f32 v48, v62, v48, 0xc0135761\n\t" \
  "v_fmaak_f32 v49, v62, v49, 0xc0135761\n\t" \
  "s_waitcnt lgkmcnt(6)\n\t" \
  "v_mfma_f32_32x32x16_bf16 a[96:111], v[228:231], v[132:135], a[96:111]\n\t" \
  "ds_read_b64_tr_b16 v[244:245], v248 offset:9728\n\t" \
  "v_fmaak_f32 v50, v62, v50, 0xc0135761\n\t" \
  "v_fmaak_f32 v51, v62, v51, 0xc0135761\n\t" \
  "v_fmaak_f32 v52, v62, v52, 0xc0135761\n\t" \
  "v_fmaak_f32 v53, v62, v53, 0xc0135761\n\t" \
  "v_fmaak_f32 v54, v62, v54, 0xc0135761\n\t" \
  "v_mfma_f32_32x32x16_bf16 a[112:127], v[228:231], v[148:151], a[112:127]\n\t" \
  "ds_read_b64_tr_b16 v[246:247], v248 offset:9984\n\t" \
  "v_fmaak_f32 v55, v62, v55, 0xc0135761\n\t" \
  "v_mul_f32 v48, v48, v32\n\t" \
  "v_mul_f32 v49, v49, v33\n\t" \
  "v_mul_f32 v50, v50, v34\n\t" \
  "v_mul_f32 v51, v51, v35\n\t" \
  "s_waitcnt lgkmcnt(6)\n\t" \
  "v_mfma_f32_32x32x16_bf16 a[0:15], v[232:235], v[136:139], a[0:15]\n\t" \
  "ds_read_b64_tr_b16 v[224:225], v248 offset:12288\n\t" \
  "s_waitcnt vmcnt(11)\n\t" \
  "s_barrier\n\t" \
  "v_mul_f32 v52, v52, v36\n\t" \
  "v_mul_f32 v53, v53, v37\n\t" \
  "v_mul_f32 v54, v54, v38\n\t" \
  "v_mul_f32 v55, v55, v39\n\t" \
  "v_exp_f32 v48, v48\n\t" \
  "v_mfma_f32_32x32x16_bf16 a[16:31], v[232:235], v[152:155], a[16:31]\n\t" \
  "ds_read_b64_tr_b16 v[226:227], v248 offset:12544\n\t" \
  "s_add_u32 m0, %[m0k], 0\n\t" \
  "s_add_u32 %[st], %[sofk], 0\n\t" \
  "buffer_load_dwordx4 v253, %[srdk], %[st] offen lds\n\t" \
  "v_exp_f32 v49, v49\n\t" \
  "s_waitcnt lgkmcnt(6)\n\t" \
  "v_mfma_f32_32x32x16_bf16 a[32:47], v[236:239], v[136:139], a[32:47]\n\t" \
  "ds_read_b64_tr_b16 v[228:229], v248 offset:12800\n\t" \
  "v_exp_f32 v50, v50\n\t" \
  "v_exp_f32 v51, v51\n\t" \
  "v_mfma_f32_32x32x16_bf16 a[48:63], v[236:239], v[152:155], a[48:63]\n\t" \
  "ds_read_b64_tr_b16 v[230:231], v248 offset:13056\n\t" \
  "s_add_u32 m0, %[m0k], 1024\n\t" \
  "s_add_u32 %[st], %[sofk], 1024\n\t" \
  "buffer_load_dwordx4 v254, %[srdk], %[st] offen lds\n\t" \
  "v_exp_f32 v52, v52\n\t" \
  "s_waitcnt lgkmcnt(6)\n\t" \
  "v_mfma_f32_32x32x16_bf16 a[64:79], v[240:243], v[136:139], a[64:79]\n\t" \
  "ds_read_b64_tr_b16 v[232:233], v248 offset:13312\n\t" \
  "v_exp_f32 v53, v53\n\t" \
  "v_exp_f32 v54, v54\n\t" \
  "v_mfma_f32_32x32x16_bf16 a[80:95], v[240:243], v[152:155], a[80:95]\n\t" \
  "ds_read_b64_tr_b16 v[234:235], v248 offset:13568\n\t" \
  "s_add_u32 m0, %[m0k], 2048\n\t" \
  "s_add_u32 %[st], %[sofk], 2048\n\t" \
  "buffer_load_dwordx4 v253, %[srdk], %[st] offen lds\n\t" \
  "v_exp_f32 v55, v55\n\t" \
  "s_waitcnt lgkmcnt(6)\n\t" \
  "v_mfma_f32_32x32x16_bf16 a[96:111], v[244:247], v[136:139], a[96:111]\n\t" \
  "ds_read_b64_tr_b16 v[236:237], v248 offset:13824\n\t" \
  "v_mul_f32 v32, v32, v40\n\t" \
  "v_mul_f32 v33, v33, v41\n\t" \
  "v_mul_f32 v34, v34, v42\n\t" \
  "v_mul_f32 v35, v35, v43\n\t" \
  "v_mul_f32 v36, v36, v44\n\t" \
  "v_mfma_f32_32x32x16_bf16 a[112:127], v[244:247], v[152:155], a[112:127]\n\t" \
  "ds_read_b64_tr_b16 v[238:239], v248 offset:14080\n\t" \
  "s_add_u32 m0, %[m0k], 3072\n\t" \
  "s_add_u32 %[st], %[sofk], 3072\n\t" \
  "buffer_load_dwordx4 v254, %[srdk], %[st] offen lds\n\t" \
  "v_mul_f32 v37, v37, v45\n\t" \
  "v_mul_f32 v38, v38, v46\n\t" \
  "s_waitcnt lgkmcnt(6)\n\t" \
  "v_mfma_f32_32x32x16_bf16 a[0:15], v[224:227], v[140:143], a[0:15]\n\t" \
  "ds_read_b64_tr_b16 v[240:241], v248 offset:16384\n\t" \
  "v_mul_f32 v39, v39, v47\n\t" \
  "v_add_f32 v48, 1.0, v48\n\t" \
  "v_add_f32 v49, 1.0, v49\n\t" \
  "v_add_f32 v50, 1.0, v50\n\t" \
  "v_add_f32 v51, 1.0, v51\n\t" \
  "v_mfma_f32_32x32x16_bf16 a[16:31], v[224:227], v[156:159], a[16:31]\n\t" \
  "ds_read_b64_tr_b16 v[242:243], v248 offset:16640\n\t" \
  "s_add_u32 m0, %[m0v], 0\n\t" \
  "s_add_u32 %[st], %[sofk], 0\n\t" \
  "buffer_load_dwordx4 v255, %[srdv], %[st] offen lds\n\t" \
  "v_add_f32 v52, 1.0, v52\n\t" \
  "v_add_f32 v53, 1.0, v53\n\t" \
  "s_waitcnt lgkmcnt(6)\n\t" \
  "v_mfma_f32_32x32x16_bf16 a[32:47], v[228:231], v[140:143], a[32:47]\n\t" \
  "ds_read_b64_tr_b16 v[244:245], v248 offset:16896\n\t" \
  "v_add_f32 v54, 1.0, v54\n\t" \
  "v_add_f32 v55, 1.0, v55\n\t" \
  "v_rcp_f32 v48, v48\n\t" \
  "v_rcp_f32 v49, v49\n\t" \
  "v_mfma_f32_32x32x16_bf16 a[48:63], v[228:231], v[156:159], a[48:63]\n\t" \
  "ds_read_b64_tr_b16 v[246:247], v248 offset:17152\n\t" \
  "s_add_u32 m0, %[m0v], 1024\n\t" \
  "s_add_u32 %[st], %[sofk], 128\n\t" \
  "buffer_load_dwordx4 v255, %[srdv], %[st] offen lds\n\t" \
  "v_rcp_f32 v50, v50\n\t" \
  "s_waitcnt lgkmcnt(6)\n\t" \
  "v_mfma_f32_32x32x16_bf16 a[64:79], v[232:235], v[140:143], a[64:79]\n\t" \
  "ds_read_b64_tr_b16 v[224:225], v248 offset:17408\n\t" \
  "v_rcp_f32 v51, v51\n\t" \
  "v_rcp_f32 v52, v52\n\t" \
  "v_mfma_f32_32x32x16_bf16 a[80:95], v[232:235], v[156:159], a[80:95]\n\t" \
  "ds_read_b64_tr_b16 v[226:227], v248 offset:17664\n\t" \
  "s_add_u32 m0, %[m0v], 2048\n\t" \
  "s_add_u32 %[st], %[sofk], 2048\n\t" \
  "buffer_load_dwordx4 v255, %[srdv], %[st] offen lds\n\t" \
  "v_rcp_f32 v53, v53\n\t" \
  "s_waitcnt lgkmcnt(6)\n\t" \
  "v_mfma_f32_32x32x16_bf16 a[96:111], v[236:239], v[140:143], a[96:111]\n\t" \
  "ds_read_b64_tr_b16 v[228:229], v248 offset:17920\n\t" \
  "v_rcp_f32 v54, v54\n\t" \
  "v_rcp_f32 v55, v55\n\t" \
  "v_mfma_f32_32x32x16_bf16 a[112:127], v[236:239], v[156:159], a[112:127]\n\t" \
  "ds_read_b64_tr_b16 v[230:231], v248 offset:18176\n\t" \
  "s_add_u32 m0, %[m0v], 3072\n\t" \
  "s_add_u32 %[st], %[sofk], 2176\n\t" \
  "buffer_load_dwordx4 v255, %[srdv], %[st] offen lds\n\t" \
  "v_mul_f32 v32, v32, v48\n\t" \
  "v_mul_f32 v33, v33, v49\n\t" \
  "s_waitcnt lgkmcnt(6)\n\t" \
  "v_mfma_f32_32x32x16_bf16 a[0:15], v[240:243], v[160:163], a[0:15]\n\t" \
  "ds_read_b64_tr_b16 v[232:233], v248 offset:20480\n\t" \
  "v_mul_f32 v34, v34, v50\n\t" \
  "v_mul_f32 v35, v35, v51\n\t" \
  "v_mul_f32 v36, v36, v52\n\t" \
  "v_mul_f32 v37, v37, v53\n\t" \
  "v_mul_f32 v38, v38, v54\n\t" \
  "v_mfma_f32_32x32x16_bf16 a[16:31], v[240:243], v[176:179], a[16:31]\n\t" \
  "ds_read_b64_tr_b16 v[234:235], v248 offset:20736\n\t" \
  "s_add_u32 m0, %[m0w], 65536\n\t" \
  "s_add_u32 %[st], %[sort], 0\n\t" \
  "buffer_load_dwordx4 v96, %[srdwo], %[st] offen lds\n\t" \
  "v_mul_f32 v39, v39, v55\n\t" \
  "v_cvt_pk_bf16_f32 v64, v32, v33\n\t" \
  "s_waitcnt lgkmcnt(6)\n\t" \
  "v_mfma_f32_32x32x16_bf16 a[32:47], v[244:247], v[160:163], a[32:47]\n\t" \
  "ds_read_b64_tr_b16 v[236:237], v248 offset:20992\n\t" \
  "v_cvt_pk_bf16_f32 v65, v34, v35\n\t" \
  "v_cvt_pk_bf16_f32 v66, v36, v37\n\t" \
  "v_cvt_pk_bf16_f32 v67, v38, v39\n\t" \
  "s_mov_b32 %[st], 0\n\t" \
  "buffer_store_dwordx4 v[64:67], v252, %[srdh], %[st] offen offset:3072\n\t" \
  "v_mfma_f32_32x32x16_bf16 a[48:63], v[244:247], v[176:179], a[48:63]\n\t" \
  "ds_read_b64_tr_b16 v[238:239], v248 offset:21248\n\t" \
  "s_add_u32 m0, %[m0w], 66560\n\t" \
  "s_add_u32 %[st], %[sort], 8192\n\t" \
  "buffer_load_dwordx4 v96, %[srdwo], %[st] offen lds\n\t" \
  "v_accvgpr_read_b32 v32, a144\n\t" \
  "v_accvgpr_read_b32 v33, a145\n\t" \
  "s_waitcnt lgkmcnt(6)\n\t" \
  "v_mfma_f32_32x32x16_bf16 a[64:79], v[224:227], v[160:163], a[64:79]\n\t" \
  "ds_read_b64_tr_b16 v[240:241], v248 offset:21504\n\t" \
  "v_accvgpr_read_b32 v34, a146\n\t" \
  "v_accvgpr_read_b32 v35, a147\n\t" \
  "v_accvgpr_read_b32 v36, a148\n\t" \
  "v_accvgpr_read_b32 v37, a149\n\t" \
  "v_accvgpr_read_b32 v38, a150\n\t" \
  "v_mfma_f32_32x32x16_bf16 a[80:95], v[224:227], v[176:179], a[80:95]\n\t" \
  "ds_read_b64_tr_b16 v[242:243], v248 offset:21760\n\t" \
  "s_add_u32 m0, %[m0w], 67584\n\t" \
  "s_add_u32 %[st], %[sort], 16384\n\t" \
  "buffer_load_dwordx4 v96, %[srdwo], %[st] offen lds\n\t" \
  "v_accvgpr_read_b32 v39, a151\n\t" \
  "v_accvgpr_read_b32 v40, a208\n\t" \
  "s_waitcnt lgkmcnt(6)\n\t" \
  "v_mfma_f32_32x32x16_bf16 a[96:111], v[228:231], v[160:163], a[96:111]\n\t" \
  "ds_read_b64_tr_b16 v[244:245], v248 offset:22016\n\t" \
  "v_accvgpr_read_b32 v41, a209\n\t" \
  "v_accvgpr_read_b32 v42, a210\n\t" \
  "v_accvgpr_read_b32 v43, a211\n\t" \
  "v_accvgpr_read_b32 v44, a212\n\t" \
  "v_accvgpr_read_b32 v45, a213\n\t" \
  "v_mfma_f32_32x32x16_bf16 a[112:127], v[228:231], v[176:179], a[112:127]\n\t" \
  "ds_read_b64_tr_b16 v[246:247], v248 offset:22272\n\t" \
  "s_add_u32 m0, %[m0w], 68608\n\t" \
  "s_add_u32 %[st], %[sort], 24576\n\t" \
  "buffer_load_dwordx4 v96, %[srdwo], %[st] offen lds\n\t" \
  "v_accvgpr_read_b32 v46, a214\n\t" \
  "v_accvgpr_read_b32 v47, a215\n\t" \
  "s_waitcnt lgkmcnt(6)\n\t" \
  "v_mfma_f32_32x32x16_bf16 a[0:15], v[232:235], v[164:167], a[0:15]\n\t" \
  "ds_read_b64_tr_b16 v[224:225], v248 offset:24576\n\t" \
  "v_mul_f32 v48, v32, v32\n\t" \
  "v_mul_f32 v49, v33, v33\n\t" \
  "v_mul_f32 v50, v34, v34\n\t" \
  "v_mul_f32 v51, v35, v35\n\t" \
  "v_mul_f32 v52, v36, v36\n\t" \
  "v_mfma_f32_32x32x16_bf16 a[16:31], v[232:235], v[180:183], a[16:31]\n\t" \
  "ds_read_b64_tr_b16 v[226:227], v248 offset:24832\n\t" \
  "s_add_u32 m0, %[m0w], 81920\n\t" \
  "s_add_u32 %[st], %[sort], 0\n\t" \
  "buffer_load_dwordx4 v96, %[srdwd], %[st] offen lds\n\t" \
  "v_mul_f32 v53, v37, v37\n\t" \
  "v_mul_f32 v54, v38, v38\n\t" \
  "s_waitcnt lgkmcnt(6)\n\t" \
  "v_mfma_f32_32x32x16_bf16 a[32:47], v[236:239], v[164:167], a[32:47]\n\t" \
  "ds_read_b64_tr_b16 v[228:229], v248 offset:25088\n\t" \
  "v_mul_f32 v55, v39, v39\n\t" \
  "v_fmaak_f32 v48, v62, v48, 0xc0135761\n\t" \
  "v_fmaak_f32 v49, v62, v49, 0xc0135761\n\t" \
  "v_fmaak_f32 v50, v62, v50, 0xc0135761\n\t" \
  "v_fmaak_f32 v51, v62, v51, 0xc0135761\n\t" \
  "v_mfma_f32_32x32x16_bf16 a[48:63], v[236:239], v[180:183], a[48:63]\n\t" \
  "ds_read_b64_tr_b16 v[230:231], v248 offset:25344\n\t" \
  "s_add_u32 m0, %[m0w], 82944\n\t" \
  "s_add_u32 %[st], %[sort], 8192\n\t" \
  "buffer_load_dwordx4 v96, %[srdwd], %[st] offen lds\n\t" \
  "v_fmaak_f32 v52, v62, v52, 0xc0135761\n\t" \
  "v_fmaak_f32 v53, v62, v53, 0xc0135761\n\t" \
  "s_waitcnt lgkmcnt(6)\n\t" \
  "v_mfma_f32_32x32x16_bf16 a[64:79], v[240:243], v[164:167], a[64:79]\n\t" \
  "ds_read_b64_tr_b16 v[232:233], v248 offset:25600\n\t" \
  "v_fmaak_f32 v54, v62, v54, 0xc0135761\n\t" \
  "v_fmaak_f32 v55, v62, v55, 0xc0135761\n\t" \
  "v_mul_f32 v48, v48, v32\n\t" \
  "v_mul_f32 v49, v49, v33\n\t" \
  "v_mul_f32 v50, v50, v34\n\t" \
  "v_mfma_f32_32x32x16_bf16 a[80:95], v[240:243], v[180:183], a[80:95]\n\t" \
  "ds_read_b64_tr_b16 v[234:235], v248 offset:25856\n\t" \
  "s_add_u32 m0, %[m0w], 83968\n\t" \
  "s_add_u32 %[st], %[sort], 16384\n\t" \
  "buffer_load_dwordx4 v96, %[srdwd], %[st] offen lds\n\t" \
  "v_mul_f32 v51, v51, v35\n\t" \
  "v_mul_f32 v52, v52, v36\n\t" \
  "s_waitcnt lgkmcnt(6)\n\t" \
  "v_mfma_f32_32x32x16_bf16 a[96:111], v[244:247], v[164:167], a[96:111]\n\t" \
  "ds_read_b64_tr_b16 v[236:237], v248 offset:26112\n\t" \
  "v_mul_f32 v53, v53, v37\n\t" \
  "v_mul_f32 v54, v54, v38\n\t" \
  "v_mul_f32 v55, v55, v39\n\t" \
  "v_exp_f32 v48, v48\n\t" \
  "v_mfma_f32_32x32x16_bf16 a[112:127], v[244:247], v[180:183], a[112:127]\n\t" \
  "ds_read_b64_tr_b16 v[238:239], v248 offset:26368\n\t" \
  "s_add_u32 m0, %[m0w], 84992\n\t" \
  "s_add_u32 %[st], %[sort], 24576\n\t" \
  "buffer_load_dwordx4 v96, %[srdwd], %[st] offen lds\n\t" \
  "v_exp_f32 v49, v49\n\t" \
  "s_waitcnt lgkmcnt(6)\n\t" \
  "v_mfma_f32_32x32x16_bf16 a[0:15], v[224:227], v[168:171], a[0:15]\n\t" \
  "ds_read_b64_tr_b16 v[240:241], v248 offset:28672\n\t" \
  "s_waitcnt vmcnt(24)\n\t" \
  "s_barrier\n\t" \
  "v_exp_f32 v50, v50\n\t" \
  "v_exp_f32 v51, v51\n\t" \
  "v_mfma_f32_32x32x16_bf16 a[16:31], v[224:227], v[184:187], a[16:31]\n\t" \
  "ds_read_b64_tr_b16 v[242:243], v248 offset:28928\n\t" \
  "s_add_u32 m0, %[m0k], 16384\n\t" \
  "s_add_u32 %[st], %[sofk], 16384\n\t" \
  "buffer_load_dwordx4 v253, %[srdk], %[st] offen lds\n\t" \
  "v_exp_f32 v52, v52\n\t" \
  "s_waitcnt lgkmcnt(6)\n\t" \
  "v_mfma_f32_32x32x16_bf16 a[32:47], v[228:231], v[168:171], a[32:47]\n\t" \
  "ds_read_b64_tr_b16 v[244:245], v248 offset:29184\n\t" \
  "v_exp_f32 v53, v53\n\t" \
  "v_exp_f32 v54, v54\n\t" \
  "v_mfma_f32_32x32x16_bf16 a[48:63], v[228:231], v[184:187], a[48:63]\n\t" \
  "ds_read_b64_tr_b16 v[246:247], v248 offset:29440\n\t" \
  "s_add_u32 m0, %[m0k], 17408\n\t" \
  "s_add_u32 %[st], %[sofk], 17408\n\t" \
  "buffer_load_dwordx4 v254, %[srdk], %[st] offen lds\n\t" \
  "v_exp_f32 v55, v55\n\t" \
  "s_waitcnt lgkmcnt(6)\n\t" \
  "v_mfma_f32_32x32x16_bf16 a[64:79], v[232:235], v[168:171], a[64:79]\n\t" \
  "ds_read_b64_tr_b16 v[224:225], v248 offset:29696\n\t" \
  "v_mul_f32 v32, v32, v40\n\t" \
  "v_mul_f32 v33, v33, v41\n\t" \
  "v_mul_f32 v34, v34, v42\n\t" \
  "v_mul_f32 v35, v35, v43\n\t" \
  "v_mul_f32 v36, v36, v44\n\t" \
  "v_mfma_f32_32x32x16_bf16 a[80:95], v[232:235], v[184:187], a[80:95]\n\t" \
  "ds_read_b64_tr_b16 v[226:227], v248 offset:29952\n\t" \
  "s_add_u32 m0, %[m0k], 18432\n\t" \
  "s_add_u32 %[st], %[sofk], 18432\n\t" \
  "buffer_load_dwordx4 v253, %[srdk], %[st] offen lds\n\t" \
  "v_mul_f32 v37, v37, v45\n\t" \
  "v_mul_f32 v38, v38, v46\n\t" \
  "s_waitcnt lgkmcnt(6)\n\t" \
  "v_mfma_f32_32x32x16_bf16 a[96:111], v[236:239], v[168:171], a[96:111]\n\t" \
  "ds_read_b64_tr_b16 v[228:229], v248 offset:30208\n\t" \
  "v_mul_f32 v39, v39, v47\n\t" \
  "v_add_f32 v48, 1.0, v48\n\t" \
  "v_add_f32 v49, 1.0, v49\n\t" \
  "v_add_f32 v50, 1.0, v50\n\t" \
  "v_add_f32 v51, 1.0, v51\n\t" \
  "v_mfma_f32_32x32x16_bf16 a[112:127], v[236:239], v[184:187], a[112:127]\n\t" \
  "ds_read_b64_tr_b16 v[230:231], v248 offset:30464\n\t" \
  "s_add_u32 m0, %[m0k], 19456\n\t" \
  "s_add_u32 %[st], %[sofk], 19456\n\t" \
  "buffer_load_dwordx4 v254, %[srdk], %[st] offen lds\n\t" \
  "v_add_f32 v52, 1.0, v52\n\t" \
  "v_add_f32 v53, 1.0, v53\n\t" \
  "s_waitcnt lgkmcnt(6)\n\t" \
  "v_mfma_f32_32x32x16_bf16 a[0:15], v[240:243], v[172:175], a[0:15]\n\t" \
  "ds_read_b64_tr_b16 v[232:233], v248 offset:32768\n\t" \
  "v_add_f32 v54, 1.0, v54\n\t" \
  "v_add_f32 v55, 1.0, v55\n\t" \
  "v_rcp_f32 v48, v48\n\t" \
  "v_rcp_f32 v49, v49\n\t" \
  "v_mfma_f32_32x32x16_bf16 a[16:31], v[240:243], v[188:191], a[16:31]\n\t" \
  "ds_read_b64_tr_b16 v[234:235], v248 offset:33024\n\t" \
  "v_rcp_f32 v50, v50\n\t" \
  "v_rcp_f32 v51, v51\n\t" \
  "s_waitcnt lgkmcnt(6)\n\t" \
  "v_mfma_f32_32x32x16_bf16 a[32:47], v[244:247], v[172:175], a[32:47]\n\t" \
  "ds_read_b64_tr_b16 v[236:237], v248 offset:33280\n\t" \
  "v_rcp_f32 v52, v52\n\t" \
  "v_rcp_f32 v53, v53\n\t" \
  "v_mfma_f32_32x32x16_bf16 a[48:63], v[244:247], v[188:191], a[48:63]\n\t" \
  "ds_read_b64_tr_b16 v[238:239], v248 offset:33536\n\t" \
  "v_rcp_f32 v54, v54\n\t" \
  "v_rcp_f32 v55, v55\n\t" \
  "s_waitcnt lgkmcnt(6)\n\t" \
  "v_mfma_f32_32x32x16_bf16 a[64:79], v[224:227], v[172:175], a[64:79]\n\t" \
  "ds_read_b64_tr_b16 v[240:241], v248 offset:33792\n\t" \
  "v_mul_f32 v32, v32, v48\n\t" \
  "v_mul_f32 v33, v33, v49\n\t" \
  "v_mul_f32 v34, v34, v50\n\t" \
  "v_mul_f32 v35, v35, v51\n\t" \
  "v_mul_f32 v36, v36, v52\n\t" \
  "v_mfma_f32_32x32x16_bf16 a[80:95], v[224:227], v[188:191], a[80:95]\n\t" \
  "ds_read_b64_tr_b16 v[242:243], v248 offset:34048\n\t" \
  "v_mul_f32 v37, v37, v53\n\t" \
  "v_mul_f32 v38, v38, v54\n\t" \
  "v_mul_f32 v39, v39, v55\n\t" \
  "v_cvt_pk_bf16_f32 v56, v32, v33\n\t" \
  "v_cvt_pk_bf16_f32 v57, v34, v35\n\t" \
  "s_waitcnt lgkmcnt(6)\n\t" \
  "v_mfma_f32_32x32x16_bf16 a[96:111], v[228:231], v[172:175], a[96:111]\n\t" \
  "ds_read_b64_tr_b16 v[244:245], v248 offset:34304\n\t" \
  "v_cvt_pk_bf16_f32 v58, v36, v37\n\t" \
  "v_cvt_pk_bf16_f32 v59, v38, v39\n\t" \
  "s_mov_b32 %[st], 114688\n\t" \
  "buffer_store_dwordx4 v[56:59], v252, %[srdh], %[st] offen offset:0\n\t" \
  "v_accvgpr_read_b32 v32, a152\n\t" \
  "v_mfma_f32_32x32x16_bf16 a[112:127], v[228:231], v[188:191], a[112:127]\n\t" \
  "ds_read_b64_tr_b16 v[246:247], v248 offset:34560\n\t" \
  "v_accvgpr_read_b32 v33, a153\n\t" \
  "v_accvgpr_read_b32 v34, a154\n\t" \
  "v_accvgpr_read_b32 v35, a155\n\t" \
  "v_accvgpr_read_b32 v36, a156\n\t" \
  "v_accvgpr_read_b32 v37, a157\n\t" \
  "s_waitcnt lgkmcnt(6)\n\t" \
  "v_mfma_f32_32x32x16_bf16 a[0:15], v[232:235], v[192:195], a[0:15]\n\t" \
  "ds_read_b64_tr_b16 v[224:225], v248 offset:36864\n\t" \
  "v_accvgpr_read_b32 v38, a158\n\t" \
  "v_accvgpr_read_b32 v39, a159\n\t" \
  "v_accvgpr_read_b32 v40, a216\n\t" \
  "v_accvgpr_read_b32 v41, a217\n\t" \
  "v_accvgpr_read_b32 v42, a218\n\t" \
  "v_mfma_f32_32x32x16_bf16 a[16:31], v[232:235], v[208:211], a[16:31]\n\t" \
  "ds_read_b64_tr_b16 v[226:227], v248 offset:37120\n\t" \
  "v_accvgpr_read_b32 v43, a219\n\t" \
  "v_accvgpr_read_b32 v44, a220\n\t" \
  "v_accvgpr_read_b32 v45, a221\n\t" \
  "v_accvgpr_read_b32 v46, a222\n\t" \
  "v_accvgpr_read_b32 v47, a223\n\t" \
  "s_waitcnt lgkmcnt(6)\n\t" \
  "v_mfma_f32_32x32x16_bf16 a[32:47], v[236:239], v[192:195], a[32:47]\n\t" \
  "ds_read_b64_tr_b16 v[228:229], v248 offset:37376\n\t" \
  "v_mul_f32 v48, v32, v32\n\t" \
  "v_mul_f32 v49, v33, v33\n\t" \
  "v_mul_f32 v50, v34, v34\n\t" \
  "v_mul_f32 v51, v35, v35\n\t" \
  "v_mul_f32 v52, v36, v36\n\t" \
  "v_mfma_f32_32x32x16_bf16 a[48:63], v[236:239], v[208:211], a[48:63]\n\t" \
  "ds_read_b64_tr_b16 v[230:231], v248 offset:37632\n\t" \
  "v_mul_f32 v53, v37, v37\n\t" \
  "v_mul_f32 v54, v38, v38\n\t" \
  "v_mul_f32 v55, v39, v39\n\t" \
  "v_fmaak_f32 v48, v62, v48, 0xc0135761\n\t" \
  "v_fmaak_f32 v49, v62, v49, 0xc0135761\n\t" \
  "s_waitcnt lgkmcnt(6)\n\t" \
  "v_mfma_f32_32x32x16_bf16 a[64:79], v[240:243], v[192:195], a[64:79]\n\t" \
  "ds_read_b64_tr_b16 v[232:233], v248 offset:37888\n\t" \
  "v_fmaak_f32 v50, v62, v50, 0xc0135761\n\t" \
  "v_fmaak_f32 v51, v62, v51, 0xc0135761\n\t" \
  "v_fmaak_f32 v52, v62, v52, 0xc0135761\n\t" \
  "v_fmaak_f32 v53, v62, v53, 0xc0135761\n\t" \
  "v_fmaak_f32 v54, v62, v54, 0xc0135761\n\t" \
  "v_mfma_f32_32x32x16_bf16 a[80:95], v[240:243], v[208:211], a[80:95]\n\t" \
  "ds_read_b64_tr_b16 v[234:235], v248 offset:38144\n\t" \
  "v_fmaak_f32 v55, v62, v55, 0xc0135761\n\t" \
  "v_mul_f32 v48, v48, v32\n\t" \
  "v_mul_f32 v49, v49, v33\n\t" \
  "v_mul_f32 v50, v50, v34\n\t" \
  "v_mul_f32 v51, v51, v35\n\t" \
  "s_waitcnt lgkmcnt(6)\n\t" \
  "v_mfma_f32_32x32x16_bf16 a[96:111], v[244:247], v[192:195], a[96:111]\n\t" \
  "ds_read_b64_tr_b16 v[236:237], v248 offset:38400\n\t" \
  "v_mul_f32 v52, v52, v36\n\t" \
  "v_mul_f32 v53, v53, v37\n\t" \
  "v_mul_f32 v54, v54, v38\n\t" \
  "v_mul_f32 v55, v55, v39\n\t" \
  "v_exp_f32 v48, v48\n\t" \
  "v_mfma_f32_32x32x16_bf16 a[112:127], v[244:247], v[208:211], a[112:127]\n\t" \
  "ds_read_b64_tr_b16 v[238:239], v248 offset:38656\n\t" \
  "v_exp_f32 v49, v49\n\t" \
  "v_exp_f32 v50, v50\n\t" \
  "s_waitcnt lgkmcnt(6)\n\t" \
  "v_mfma_f32_32x32x16_bf16 a[0:15], v[224:227], v[196:199], a[0:15]\n\t" \
  "ds_read_b64_tr_b16 v[240:241], v248 offset:40960\n\t" \
  "v_exp_f32 v51, v51\n\t" \
  "v_exp_f32 v52, v52\n\t" \
  "v_mfma_f32_32x32x16_bf16 a[16:31], v[224:227], v[212:215], a[16:31]\n\t" \
  "ds_read_b64_tr_b16 v[242:243], v248 offset:41216\n\t" \
  "v_exp_f32 v53, v53\n\t" \
  "v_exp_f32 v54, v54\n\t" \
  "s_waitcnt lgkmcnt(6)\n\t" \
  "v_mfma_f32_32x32x16_bf16 a[32:47], v[228:231], v[196:199], a[32:47]\n\t" \
  "ds_read_b64_tr_b16 v[244:245], v248 offset:41472\n\t" \
  "v_exp_f32 v55, v55\n\t" \
  "v_mul_f32 v32, v32, v40\n\t" \
  "v_mul_f32 v33, v33, v41\n\t" \
  "v_mul_f32 v34, v34, v42\n\t" \
  "v_mfma_f32_32x32x16_bf16 a[48:63], v[228:231], v[212:215], a[48:63]\n\t" \
  "ds_read_b64_tr_b16 v[246:247], v248 offset:41728\n\t" \
  "v_mul_f32 v35, v35, v43\n\t" \
  "v_mul_f32 v36, v36, v44\n\t" \
  "v_mul_f32 v37, v37, v45\n\t" \
  "v_mul_f32 v38, v38, v46\n\t" \
  "v_mul_f32 v39, v39, v47\n\t" \
  "s_waitcnt lgkmcnt(6)\n\t" \
  "v_mfma_f32_32x32x16_bf16 a[64:79], v[232:235], v[196:199], a[64:79]\n\t" \
  "ds_read_b64_tr_b16 v[224:225], v248 offset:41984\n\t" \
  "v_add_f32 v48, 1.0, v48\n\t" \
  "v_add_f32 v49, 1.0, v49\n\t" \
  "v_add_f32 v50, 1.0, v50\n\t" \
  "v_add_f32 v51, 1.0, v51\n\t" \
  "v_add_f32 v52, 1.0, v52\n\t" \
  "v_mfma_f32_32x32x16_bf16 a[80:95], v[232:235], v[212:215], a[80:95]\n\t" \
  "ds_read_b64_tr_b16 v[226:227], v248 offset:42240\n\t" \
  "v_add_f32 v53, 1.0, v53\n\t" \
  "v_add_f32 v54, 1.0, v54\n\t" \
  "v_add_f32 v55, 1.0, v55\n\t" \
  "v_rcp_f32 v48, v48\n\t" \
  "s_waitcnt lgkmcnt(6)\n\t" \
  "v_mfma_f32_32x32x16_bf16 a[96:111], v[236:239], v[196:199], a[96:111]\n\t" \
  "ds_read_b64_tr_b16 v[228:229], v248 offset:42496\n\t" \
  "v_rcp_f32 v49, v49\n\t" \
  "v_rcp_f32 v50, v50\n\t" \
  "v_mfma_f32_32x32x16_bf16 a[112:127], v[236:239], v[212:215], a[112:127]\n\t" \
  "ds_read_b64_tr_b16 v[230:231], v248 offset:42752\n\t" \
  "v_rcp_f32 v51, v51\n\t" \
  "v_rcp_f32 v52, v52\n\t" \
  "s_waitcnt lgkmcnt(6)\n\t" \
  "v_mfma_f32_32x32x16_bf16 a[0:15], v[240:243], v[200:203], a[0:15]\n\t" \
  "ds_read_b64_tr_b16 v[232:233], v248 offset:45056\n\t" \
  "s_waitcnt vmcnt(23)\n\t" \
  "s_barrier\n\t" \
  "v_rcp_f32 v53, v53\n\t" \
  "v_rcp_f32 v54, v54\n\t" \
  "v_mfma_f32_32x32x16_bf16 a[16:31], v[240:243], v[216:219], a[16:31]\n\t" \
  "ds_read_b64_tr_b16 v[234:235], v248 offset:45312\n\t" \
  "v_rcp_f32 v55, v55\n\t" \
  "v_mul_f32 v32, v32, v48\n\t" \
  "v_mul_f32 v33, v33, v49\n\t" \
  "v_mul_f32 v34, v34, v50\n\t" \
  "s_waitcnt lgkmcnt(6)\n\t" \
  "v_mfma_f32_32x32x16_bf16 a[32:47], v[244:247], v[200:203], a[32:47]\n\t" \
  "ds_read_b64_tr_b16 v[236:237], v248 offset:45568\n\t" \
  "v_mul_f32 v35, v35, v51\n\t" \
  "v_mul_f32 v36, v36, v52\n\t" \
  "v_mul_f32 v37, v37, v53\n\t" \
  "v_mul_f32 v38, v38, v54\n\t" \
  "v_mul_f32 v39, v39, v55\n\t" \
  "v_mfma_f32_32x32x16_bf16 a[48:63], v[244:247], v[216:219], a[48:63]\n\t" \
  "ds_read_b64_tr_b16 v[238:239], v248 offset:45824\n\t" \
  "v_cvt_pk_bf16_f32 v64, v32, v33\n\t" \
  "v_cvt_pk_bf16_f32 v65, v34, v35\n\t" \
  "v_cvt_pk_bf16_f32 v66, v36, v37\n\t" \
  "v_cvt_pk_bf16_f32 v67, v38, v39\n\t" \
  "s_mov_b32 %[st], 114688\n\t" \
  "buffer_store_dwordx4 v[64:67], v252, %[srdh], %[st] offen offset:1024\n\t" \
  "s_waitcnt lgkmcnt(6)\n\t" \
  "v_mfma_f32_32x32x16_bf16 a[64:79], v[224:227], v[200:203], a[64:79]\n\t" \
  "ds_read_b64_tr_b16 v[240:241], v248 offset:46080\n\t" \
  "v_accvgpr_read_b32 v32, a176\n\t" \
  "v_accvgpr_read_b32 v33, a177\n\t" \
  "v_accvgpr_read_b32 v34, a178\n\t" \
  "v_accvgpr_read_b32 v35, a179\n\t" \
  "v_accvgpr_read_b32 v36, a180\n\t" \
  "v_mfma_f32_32x32x16_bf16 a[80:95], v[224:227], v[216:219], a[80:95]\n\t" \
  "ds_read_b64_tr_b16 v[242:243], v248 offset:46336\n\t" \
  "v_accvgpr_read_b32 v37, a181\n\t" \
  "v_accvgpr_read_b32 v38, a182\n\t" \
  "v_accvgpr_read_b32 v39, a183\n\t" \
  "v_accvgpr_read_b32 v40, a240\n\t" \
  "v_accvgpr_read_b32 v41, a241\n\t" \
  "s_waitcnt lgkmcnt(6)\n\t" \
  "v_mfma_f32_32x32x16_bf16 a[96:111], v[228:231], v[200:203], a[96:111]\n\t" \
  "ds_read_b64_tr_b16 v[244:245], v248 offset:46592\n\t" \
  "v_accvgpr_read_b32 v42, a242\n\t" \
  "v_accvgpr_read_b32 v43, a243\n\t" \
  "v_accvgpr_read_b32 v44, a244\n\t" \
  "v_accvgpr_read_b32 v45, a245\n\t" \
  "v_accvgpr_read_b32 v46, a246\n\t" \
  "v_mfma_f32_32x32x16_bf16 a[112:127], v[228:231], v[216:219], a[112:127]\n\t" \
  "ds_read_b64_tr_b16 v[246:247], v248 offset:46848\n\t" \
  "v_accvgpr_read_b32 v47, a247\n\t" \
  "v_mul_f32 v48, v32, v32\n\t" \
  "v_mul_f32 v49, v33, v33\n\t" \
  "v_mul_f32 v50, v34, v34\n\t" \
  "v_mul_f32 v51, v35, v35\n\t" \
  "s_waitcnt lgkmcnt(6)\n\t" \
  "v_mfma_f32_32x32x16_bf16 a[0:15], v[232:235], v[204:207], a[0:15]\n\t" \
  "ds_read_b64_tr_b16 v[224:225], v248 offset:49152\n\t" \
  "v_mul_f32 v52, v36, v36\n\t" \
  "v_mul_f32 v53, v37, v37\n\t" \
  "v_mul_f32 v54, v38, v38\n\t" \
  "v_mul_f32 v55, v39, v39\n\t" \
  "v_fmaak_f32 v48, v62, v48, 0xc0135761\n\t" \
  "v_mfma_f32_32x32x16_bf16 a[16:31], v[232:235], v[220:223], a[16:31]\n\t" \
  "ds_read_b64_tr_b16 v[226:227], v248 offset:49408\n\t" \
  "v_fmaak_f32 v49, v62, v49, 0xc0135761\n\t" \
  "v_fmaak_f32 v50, v62, v50, 0xc0135761\n\t" \
  "v_fmaak_f32 v51, v62, v51, 0xc0135761\n\t" \
  "v_fmaak_f32 v52, v62, v52, 0xc0135761\n\t" \
  "v_fmaak_f32 v53, v62, v53, 0xc0135761\n\t" \
  "s_waitcnt lgkmcnt(6)\n\t" \
  "v_mfma_f32_32x32x16_bf16 a[32:47], v[236:239], v[204:207], a[32:47]\n\t" \
  "ds_read_b64_tr_b16 v[228:229], v248 offset:49664\n\t" \
  "v_fmaak_f32 v54, v62, v54, 0xc0135761\n\t" \
  "v_fmaak_f32 v55, v62, v55, 0xc0135761\n\t" \
  "v_mul_f32 v48, v48, v32\n\t" \
  "v_mul_f32 v49, v49, v33\n\t" \
  "v_mul_f32 v50, v50, v34\n\t" \
  "v_mfma_f32_32x32x16_bf16 a[48:63], v[236:239], v[220:223], a[48:63]\n\t" \
  "ds_read_b64_tr_b16 v[230:231], v248 offset:49920\n\t" \
  "v_mul_f32 v51, v51, v35\n\t" \
  "v_mul_f32 v52, v52, v36\n\t" \
  "v_mul_f32 v53, v53, v37\n\t" \
  "v_mul_f32 v54, v54, v38\n\t" \
  "v_mul_f32 v55, v55, v39\n\t" \
  "s_waitcnt lgkmcnt(6)\n\t" \
  "v_mfma_f32_32x32x16_bf16 a[64:79], v[240:243], v[204:207], a[64:79]\n\t" \
  "ds_read_b64_tr_b16 v[232:233], v248 offset:50176\n\t" \
  "v_exp_f32 v48, v48\n\t" \
  "v_exp_f32 v49, v49\n\t" \
  "v_mfma_f32_32x32x16_bf16 a[80:95], v[240:243], v[220:223], a[80:95]\n\t" \
  "ds_read_b64_tr_b16 v[234:235], v248 offset:50432\n\t" \
  "v_exp_f32 v50, v50\n\t" \
  "v_exp_f32 v51, v51\n\t" \
  "s_waitcnt lgkmcnt(6)\n\t" \
  "v_mfma_f32_32x32x16_bf16 a[96:111], v[244:247], v[204:207], a[96:111]\n\t" \
  "ds_read_b64_tr_b16 v[236:237], v248 offset:50688\n\t" \
  "v_exp_f32 v52, v52\n\t" \
  "v_exp_f32 v53, v53\n\t" \
  "v_mfma_f32_32x32x16_bf16 a[112:127], v[244:247], v[220:223], a[112:127]\n\t" \
  "ds_read_b64_tr_b16 v[238:239], v248 offset:50944\n\t" \
  "v_exp_f32 v54, v54\n\t" \
  "v_exp_f32 v55, v55\n\t" \
  "s_waitcnt lgkmcnt(6)\n\t" \
  "v_mfma_f32_32x32x16_bf16 a[0:15], v[224:227], v[0:3], a[0:15]\n\t" \
  "ds_read_b64_tr_b16 v[240:241], v248 offset:53248\n\t" \
  "v_mul_f32 v32, v32, v40\n\t" \
  "v_mul_f32 v33, v33, v41\n\t" \
  "v_mul_f32 v34, v34, v42\n\t" \
  "v_mul_f32 v35, v35, v43\n\t" \
  "v_mul_f32 v36, v36, v44\n\t" \
  "v_mfma_f32_32x32x16_bf16 a[16:31], v[224:227], v[16:19], a[16:31]\n\t" \
  "ds_read_b64_tr_b16 v[242:243], v248 offset:53504\n\t" \
  "v_mul_f32 v37, v37, v45\n\t" \
  "v_mul_f32 v38, v38, v46\n\t" \
  "v_mul_f32 v39, v39, v47\n\t" \
  "v_add_f32 v48, 1.0, v48\n\t" \
  "v_add_f32 v49, 1.0, v49\n\t" \
  "s_waitcnt lgkmcnt(6)\n\t" \
  "v_mfma_f32_32x32x16_bf16 a[32:47], v[228:231], v[0:3], a[32:47]\n\t" \
  "ds_read_b64_tr_b16 v[244:245], v248 offset:53760\n\t" \
  "v_add_f32 v50, 1.0, v50\n\t" \
  "v_add_f32 v51, 1.0, v51\n\t" \
  "v_add_f32 v52, 1.0, v52\n\t" \
  "v_add_f32 v53, 1.0, v53\n\t" \
  "v_add_f32 v54, 1.0, v54\n\t" \
  "v_mfma_f32_32x32x16_bf16 a[48:63], v[228:231], v[16:19], a[48:63]\n\t" \
  "ds_read_b64_tr_b16 v[246:247], v248 offset:54016\n\t" \
  "v_add_f32 v55, 1.0, v55\n\t" \
  "v_rcp_f32 v48, v48\n\t" \
  "v_rcp_f32 v49, v49\n\t" \
  "s_waitcnt lgkmcnt(6)\n\t" \
  "v_mfma_f32_32x32x16_bf16 a[64:79], v[232:235], v[0:3], a[64:79]\n\t" \
  "ds_read_b64_tr_b16 v[224:225], v248 offset:54272\n\t" \
  "v_rcp_f32 v50, v50\n\t" \
  "v_rcp_f32 v51, v51\n\t" \
  "v_mfma_f32_32x32x16_bf16 a[80:95], v[232:235], v[16:19], a[80:95]\n\t" \
  "ds_read_b64_tr_b16 v[226:227], v248 offset:54528\n\t" \
  "v_rcp_f32 v52, v52\n\t" \
  "v_rcp_f32 v53, v53\n\t" \
  "s_waitcnt lgkmcnt(6)\n\t" \
  "v_mfma_f32_32x32x16_bf16 a[96:111], v[236:239], v[0:3], a[96:111]\n\t" \
  "ds_read_b64_tr_b16 v[228:229], v248 offset:54784\n\t" \
  "v_rcp_f32 v54, v54\n\t" \
  "v_rcp_f32 v55, v55\n\t" \
  "v_mfma_f32_32x32x16_bf16 a[112:127], v[236:239], v[16:19], a[112:127]\n\t" \
  "ds_read_b64_tr_b16 v[230:231], v248 offset:55040\n\t" \
  "v_mul_f32 v32, v32, v48\n\t" \
  "v_mul_f32 v33, v33, v49\n\t" \
  "v_mul_f32 v34, v34, v50\n\t" \
  "v_mul_f32 v35, v35, v51\n\t" \
  "v_mul_f32 v36, v36, v52\n\t" \
  "s_waitcnt lgkmcnt(6)\n\t" \
  "v_mfma_f32_32x32x16_bf16 a[0:15], v[240:243], v[4:7], a[0:15]\n\t" \
  "ds_read_b64_tr_b16 v[232:233], v248 offset:57344\n\t" \
  "v_mul_f32 v37, v37, v53\n\t" \
  "v_mul_f32 v38, v38, v54\n\t" \
  "v_mul_f32 v39, v39, v55\n\t" \
  "v_cvt_pk_bf16_f32 v56, v32, v33\n\t" \
  "v_cvt_pk_bf16_f32 v57, v34, v35\n\t" \
  "v_mfma_f32_32x32x16_bf16 a[16:31], v[240:243], v[20:23], a[16:31]\n\t" \
  "ds_read_b64_tr_b16 v[234:235], v248 offset:57600\n\t" \
  "v_cvt_pk_bf16_f32 v58, v36, v37\n\t" \
  "v_cvt_pk_bf16_f32 v59, v38, v39\n\t" \
  "s_mov_b32 %[st], 114688\n\t" \
  "buffer_store_dwordx4 v[56:59], v252, %[srdh], %[st] offen offset:2048\n\t" \
  "v_accvgpr_read_b32 v32, a184\n\t" \
  "s_waitcnt lgkmcnt(6)\n\t" \
  "v_mfma_f32_32x32x16_bf16 a[32:47], v[244:247], v[4:7], a[32:47]\n\t" \
  "ds_read_b64_tr_b16 v[236:237], v248 offset:57856\n\t" \
  "v_accvgpr_read_b32 v33, a185\n\t" \
  "v_accvgpr_read_b32 v34, a186\n\t" \
  "v_accvgpr_read_b32 v35, a187\n\t" \
  "v_accvgpr_read_b32 v36, a188\n\t" \
  "v_accvgpr_read_b32 v37, a189\n\t" \
  "v_mfma_f32_32x32x16_bf16 a[48:63], v[244:247], v[20:23], a[48:63]\n\t" \
  "ds_read_b64_tr_b16 v[238:239], v248 offset:58112\n\t" \
  "v_accvgpr_read_b32 v38, a190\n\t" \
  "v_accvgpr_read_b32 v39, a191\n\t" \
  "v_accvgpr_read_b32 v40, a248\n\t" \
  "v_accvgpr_read_b32 v41, a249\n\t" \
  "v_accvgpr_read_b32 v42, a250\n\t" \
  "s_waitcnt lgkmcnt(6)\n\t" \
  "v_mfma_f32_32x32x16_bf16 a[64:79], v[224:227], v[4:7], a[64:79]\n\t" \
  "ds_read_b64_tr_b16 v[240:241], v248 offset:58368\n\t" \
  "v_accvgpr_read_b32 v43, a251\n\t" \
  "v_accvgpr_read_b32 v44, a252\n\t" \
  "v_accvgpr_read_b32 v45, a253\n\t" \
  "v_accvgpr_read_b32 v46, a254\n\t" \
  "v_accvgpr_read_b32 v47, a255\n\t" \
  "v_mfma_f32_32x32x16_bf16 a[80:95], v[224:227], v[20:23], a[80:95]\n\t" \
  "ds_read_b64_tr_b16 v[242:243], v248 offset:58624\n\t" \
  "v_mul_f32 v48, v32, v32\n\t" \
  "v_mul_f32 v49, v33, v33\n\t" \
  "v_mul_f32 v50, v34, v34\n\t" \
  "v_mul_f32 v51, v35, v35\n\t" \
  "v_mul_f32 v52, v36, v36\n\t" \
  "s_waitcnt lgkmcnt(6)\n\t" \
  "v_mfma_f32_32x32x16_bf16 a[96:111], v[228:231], v[4:7], a[96:111]\n\t" \
  "ds_read_b64_tr_b16 v[244:245], v248 offset:58880\n\t" \
  "v_mul_f32 v53, v37, v37\n\t" \
  "v_mul_f32 v54, v38, v38\n\t" \
  "v_mul_f32 v55, v39, v39\n\t" \
  "v_fmaak_f32 v48, v62, v48, 0xc0135761\n\t" \
  "v_fmaak_f32 v49, v62, v49, 0xc0135761\n\t" \
  "v_mfma_f32_32x32x16_bf16 a[112:127], v[228:231], v[20:23], a[112:127]\n\t" \
  "ds_read_b64_tr_b16 v[246:247], v248 offset:59136\n\t" \
  "v_fmaak_f32 v50, v62, v50, 0xc0135761\n\t" \
  "v_fmaak_f32 v51, v62, v51, 0xc0135761\n\t" \
  "v_fmaak_f32 v52, v62, v52, 0xc0135761\n\t" \
  "v_fmaak_f32 v53, v62, v53, 0xc0135761\n\t" \
  "v_fmaak_f32 v54, v62, v54, 0xc0135761\n\t" \
  "s_waitcnt lgkmcnt(6)\n\t" \
  "v_mfma_f32_32x32x16_bf16 a[0:15], v[232:235], v[8:11], a[0:15]\n\t" \
  "ds_read_b64_tr_b16 v[224:225], v248 offset:61440\n\t" \
  "v_fmaak_f32 v55, v62, v55, 0xc0135761\n\t" \
  "v_mul_f32 v48, v48, v32\n\t" \
  "v_mul_f32 v49, v49, v33\n\t" \
  "v_mul_f32 v50, v50, v34\n\t" \
  "v_mul_f32 v51, v51, v35\n\t" \
  "v_mfma_f32_32x32x16_bf16 a[16:31], v[232:235], v[24:27], a[16:31]\n\t" \
  "ds_read_b64_tr_b16 v[226:227], v248 offset:61696\n\t" \
  "v_mul_f32 v52, v52, v36\n\t" \
  "v_mul_f32 v53, v53, v37\n\t" \
  "v_mul_f32 v54, v54, v38\n\t" \
  "v_mul_f32 v55, v55, v39\n\t" \
  "v_exp_f32 v48, v48\n\t" \
  "s_waitcnt lgkmcnt(6)\n\t" \
  "v_mfma_f32_32x32x16_bf16 a[32:47], v[236:239], v[8:11], a[32:47]\n\t" \
  "ds_read_b64_tr_b16 v[228:229], v248 offset:61952\n\t" \
  "v_exp_f32 v49, v49\n\t" \
  "v_exp_f32 v50, v50\n\t" \
  "v_mfma_f32_32x32x16_bf16 a[48:63], v[236:239], v[24:27], a[48:63]\n\t" \
  "ds_read_b64_tr_b16 v[230:231], v248 offset:62208\n\t" \
  "v_exp_f32 v51, v51\n\t" \
  "v_exp_f32 v52, v52\n\t" \
  "s_waitcnt lgkmcnt(6)\n\t" \
  "v_mfma_f32_32x32x16_bf16 a[64:79], v[240:243], v[8:11], a[64:79]\n\t" \
  "ds_read_b64_tr_b16 v[232:233], v248 offset:62464\n\t" \
  "v_exp_f32 v53, v53\n\t" \
  "v_exp_f32 v54, v54\n\t" \
  "v_mfma_f32_32x32x16_bf16 a[80:95], v[240:243], v[24:27], a[80:95]\n\t" \
  "ds_read_b64_tr_b16 v[234:235], v248 offset:62720\n\t" \
  "v_exp_f32 v55, v55\n\t" \
  "v_mul_f32 v32, v32, v40\n\t" \
  "v_mul_f32 v33, v33, v41\n\t" \
  "v_mul_f32 v34, v34, v42\n\t" \
  "s_waitcnt lgkmcnt(6)\n\t" \
  "v_mfma_f32_32x32x16_bf16 a[96:111], v[244:247], v[8:11], a[96:111]\n\t" \
  "ds_read_b64_tr_b16 v[236:237], v248 offset:62976\n\t" \
  "v_mul_f32 v35, v35, v43\n\t" \
  "v_mul_f32 v36, v36, v44\n\t" \
  "v_mul_f32 v37, v37, v45\n\t" \
  "v_mul_f32 v38, v38, v46\n\t" \
  "v_mul_f32 v39, v39, v47\n\t" \
  "v_mfma_f32_32x32x16_bf16 a[112:127], v[244:247], v[24:27], a[112:127]\n\t" \
  "ds_read_b64_tr_b16 v[238:239], v248 offset:63232\n\t" \
  "v_add_f32 v48, 1.0, v48\n\t" \
  "v_add_f32 v49, 1.0, v49\n\t" \
  "v_add_f32 v50, 1.0, v50\n\t" \
  "v_add_f32 v51, 1.0, v51\n\t" \
  "v_add_f32 v52, 1.0, v52\n\t" \
  "s_waitcnt lgkmcnt(6)\n\t" \
  "v_mfma_f32_32x32x16_bf16 a[0:15], v[224:227], v[12:15], a[0:15]\n\t" \
  "v_add_f32 v53, 1.0, v53\n\t" \
  "v_add_f32 v54, 1.0, v54\n\t" \
  "v_add_f32 v55, 1.0, v55\n\t" \
  "v_rcp_f32 v48, v48\n\t" \
  "v_rcp_f32 v49, v49\n\t" \
  "v_mfma_f32_32x32x16_bf16 a[16:31], v[224:227], v[28:31], a[16:31]\n\t" \
  "v_rcp_f32 v50, v50\n\t" \
  "v_rcp_f32 v51, v51\n\t" \
  "v_rcp_f32 v52, v52\n\t" \
  "s_waitcnt lgkmcnt(4)\n\t" \
  "v_mfma_f32_32x32x16_bf16 a[32:47], v[228:231], v[12:15], a[32:47]\n\t" \
  "v_rcp_f32 v53, v53\n\t" \
  "v_rcp_f32 v54, v54\n\t" \
  "v_rcp_f32 v55, v55\n\t" \
  "v_mfma_f32_32x32x16_bf16 a[48:63], v[228:231], v[28:31], a[48:63]\n\t" \
  "v_mul_f32 v32, v32, v48\n\t" \
  "v_mul_f32 v33, v33, v49\n\t" \
  "v_mul_f32 v34, v34, v50\n\t" \
  "v_mul_f32 v35, v35, v51\n\t" \
  "v_mul_f32 v36, v36, v52\n\t" \
  "v_mul_f32 v37, v37, v53\n\t" \
  "s_waitcnt lgkmcnt(2)\n\t" \
  "v_mfma_f32_32x32x16_bf16 a[64:79], v[232:235], v[12:15], a[64:79]\n\t" \
  "v_mul_f32 v38, v38, v54\n\t" \
  "v_mul_f32 v39, v39, v55\n\t" \
  "v_cvt_pk_bf16_f32 v64, v32, v33\n\t" \
  "v_cvt_pk_bf16_f32 v65, v34, v35\n\t" \
  "v_cvt_pk_bf16_f32 v66, v36, v37\n\t" \
  "v_cvt_pk_bf16_f32 v67, v38, v39\n\t" \
  "v_mfma_f32_32x32x16_bf16 a[80:95], v[232:235], v[28:31], a[80:95]\n\t" \
  "s_mov_b32 %[st], 114688\n\t" \
  "buffer_store_dwordx4 v[64:67], v252, %[srdh], %[st] offen offset:3072\n\t" \
  "s_waitcnt lgkmcnt(0)\n\t" \
  "v_mfma_f32_32x32x16_bf16 a[96:111], v[236:239], v[12:15], a[96:111]\n\t" \
  "v_mfma_f32_32x32x16_bf16 a[112:127], v[236:239], v[28:31], a[112:127]\n\t" \
  "s_nop 7\n\t" \
  "s_nop 7\n\t" \
  "v_accvgpr_read_b32 v128, a0\n\t" \
  "v_accvgpr_read_b32 v129, a1\n\t" \
  "v_accvgpr_read_b32 v130, a2\n\t" \
  "v_accvgpr_read_b32 v131, a3\n\t" \
  "v_accvgpr_read_b32 v132, a4\n\t" \
  "v_accvgpr_read_b32 v133, a5\n\t" \
  "v_accvgpr_read_b32 v134, a6\n\t" \
  "v_accvgpr_read_b32 v135, a7\n\t" \
  "v_mul_f32 v128, %[sc], v128\n\t" \
  "v_mul_f32 v129, %[sc], v129\n\t" \
  "v_mul_f32 v130, %[sc], v130\n\t" \
  "v_mul_f32 v131, %[sc], v131\n\t" \
  "v_mul_f32 v132, %[sc], v132\n\t" \
  "v_mul_f32 v133, %[sc], v133\n\t" \
  "v_mul_f32 v134, %[sc], v134\n\t" \
  "v_mul_f32 v135, %[sc], v135\n\t" \
  "v_cvt_pk_bf16_f32 v136, v128, v129\n\t" \
  "v_cvt_pk_bf16_f32 v137, v130, v131\n\t" \
  "v_cvt_pk_bf16_f32 v138, v132, v133\n\t" \
  "v_cvt_pk_bf16_f32 v139, v134, v135\n\t" \
  "v_accvgpr_write_b32 a128, v136\n\t" \
  "v_accvgpr_write_b32 a129, v137\n\t" \
  "v_accvgpr_write_b32 a130, v138\n\t" \
  "v_accvgpr_write_b32 a131, v139\n\t" \
  "v_accvgpr_read_b32 v144, a8\n\t" \
  "v_accvgpr_read_b32 v145, a9\n\t" \
  "v_accvgpr_read_b32 v146, a10\n\t" \
  "v_accvgpr_read_b32 v147, a11\n\t" \
  "v_accvgpr_read_b32 v148, a12\n\t" \
  "v_accvgpr_read_b32 v149, a13\n\t" \
  "v_accvgpr_read_b32 v150, a14\n\t" \
  "v_accvgpr_read_b32 v151, a15\n\t" \
  "v_mul_f32 v144, %[sc], v144\n\t" \
  "v_mul_f32 v145, %[sc], v145\n\t" \
  "v_mul_f32 v146, %[sc], v146\n\t" \
  "v_mul_f32 v147, %[sc], v147\n\t" \
  "v_mul_f32 v148, %[sc], v148\n\t" \
  "v_mul_f32 v149, %[sc], v149\n\t" \
  "v_mul_f32 v150, %[sc], v150\n\t" \
  "v_mul_f32 v151, %[sc], v151\n\t" \
  "v_cvt_pk_bf16_f32 v152, v144, v145\n\t" \
  "v_cvt_pk_bf16_f32 v153, v146, v147\n\t" \
  "v_cvt_pk_bf16_f32 v154, v148, v149\n\t" \
  "v_cvt_pk_bf16_f32 v155, v150, v151\n\t" \
  "v_accvgpr_write_b32 a132, v152\n\t" \
  "v_accvgpr_write_b32 a133, v153\n\t" \
  "v_accvgpr_write_b32 a134, v154\n\t" \
  "v_accvgpr_write_b32 a135, v155\n\t" \
  "v_accvgpr_read_b32 v128, a32\n\t" \
  "v_accvgpr_read_b32 v129, a33\n\t" \
  "v_accvgpr_read_b32 v130, a34\n\t" \
  "v_accvgpr_read_b32 v131, a35\n\t" \
  "v_accvgpr_read_b32 v132, a36\n\t" \
  "v_accvgpr_read_b32 v133, a37\n\t" \
  "v_accvgpr_read_b32 v134, a38\n\t" \
  "v_accvgpr_read_b32 v135, a39\n\t" \
  "v_mul_f32 v128, %[sc], v128\n\t" \
  "v_mul_f32 v129, %[sc], v129\n\t" \
  "v_mul_f32 v130, %[sc], v130\n\t" \
  "v_mul_f32 v131, %[sc], v131\n\t" \
  "v_mul_f32 v132, %[sc], v132\n\t" \
  "v_mul_f32 v133, %[sc], v133\n\t" \
  "v_mul_f32 v134, %[sc], v134\n\t" \
  "v_mul_f32 v135, %[sc], v135\n\t" \
  "v_cvt_pk_bf16_f32 v136, v128, v129\n\t" \
  "v_cvt_pk_bf16_f32 v137, v130, v131\n\t" \
  "v_cvt_pk_bf16_f32 v138, v132, v133\n\t" \
  "v_cvt_pk_bf16_f32 v139, v134, v135\n\t" \
  "v_accvgpr_write_b32 a136, v136\n\t" \
  "v_accvgpr_write_b32 a137, v137\n\t" \
  "v_accvgpr_write_b32 a138, v138\n\t" \
  "v_accvgpr_write_b32 a139, v139\n\t" \
  "v_accvgpr_read_b32 v144, a40\n\t" \
  "v_accvgpr_read_b32 v145, a41\n\t" \
  "v_accvgpr_read_b32 v146, a42\n\t" \
  "v_accvgpr_read_b32 v147, a43\n\t" \
  "v_accvgpr_read_b32 v148, a44\n\t" \
  "v_accvgpr_read_b32 v149, a45\n\t" \
  "v_accvgpr_read_b32 v150, a46\n\t" \
  "v_accvgpr_read_b32 v151, a47\n\t" \
  "v_mul_f32 v144, %[sc], v144\n\t" \
  "v_mul_f32 v145, %[sc], v145\n\t" \
  "v_mul_f32 v146, %[sc], v146\n\t" \
  "v_mul_f32 v147, %[sc], v147\n\t" \
  "v_mul_f32 v148, %[sc], v148\n\t" \
  "v_mul_f32 v149, %[sc], v149\n\t" \
  "v_mul_f32 v150, %[sc], v150\n\t" \
  "v_mul_f32 v151, %[sc], v151\n\t" \
  "v_cvt_pk_bf16_f32 v152, v144, v145\n\t" \
  "v_cvt_pk_bf16_f32 v153, v146, v147\n\t" \
  "v_cvt_pk_bf16_f32 v154, v148, v149\n\t" \
  "v_cvt_pk_bf16_f32 v155, v150, v151\n\t" \
  "v_accvgpr_write_b32 a140, v152\n\t" \
  "v_accvgpr_write_b32 a141, v153\n\t" \
  "v_accvgpr_write_b32 a142, v154\n\t" \
  "v_accvgpr_write_b32 a143, v155\n\t" \
  "v_accvgpr_read_b32 v128, a64\n\t" \
  "v_accvgpr_read_b32 v129, a65\n\t" \
  "v_accvgpr_read_b32 v130, a66\n\t" \
  "v_accvgpr_read_b32 v131, a67\n\t" \
  "v_accvgpr_read_b32 v132, a68\n\t" \
  "v_accvgpr_read_b32 v133, a69\n\t" \
  "v_accvgpr_read_b32 v134, a70\n\t" \
  "v_accvgpr_read_b32 v135, a71\n\t" \
  "v_mul_f32 v128, %[sc], v128\n\t" \
  "v_mul_f32 v129, %[sc], v129\n\t" \
  "v_mul_f32 v130, %[sc], v130\n\t" \
  "v_mul_f32 v131, %[sc], v131\n\t" \
  "v_mul_f32 v132, %[sc], v132\n\t" \
  "v_mul_f32 v133, %[sc], v133\n\t" \
  "v_mul_f32 v134, %[sc], v134\n\t" \
  "v_mul_f32 v135, %[sc], v135\n\t" \
  "v_cvt_pk_bf16_f32 v136, v128, v129\n\t" \
  "v_cvt_pk_bf16_f32 v137, v130, v131\n\t" \
  "v_cvt_pk_bf16_f32 v138, v132, v133\n\t" \
  "v_cvt_pk_bf16_f32 v139, v134, v135\n\t" \
  "v_accvgpr_write_b32 a144, v136\n\t" \
  "v_accvgpr_write_b32 a145, v137\n\t" \
  "v_accvgpr_write_b32 a146, v138\n\t" \
  "v_accvgpr_write_b32 a147, v139\n\t" \
  "v_accvgpr_read_b32 v144, a72\n\t" \
  "v_accvgpr_read_b32 v145, a73\n\t" \
  "v_accvgpr_read_b32 v146, a74\n\t" \
  "v_accvgpr_read_b32 v147, a75\n\t" \
  "v_accvgpr_read_b32 v148, a76\n\t" \
  "v_accvgpr_read_b32 v149, a77\n\t" \
  "v_accvgpr_read_b32 v150, a78\n\t" \
  "v_accvgpr_read_b32 v151, a79\n\t" \
  "v_mul_f32 v144, %[sc], v144\n\t" \
  "v_mul_f32 v145, %[sc], v145\n\t" \
  "v_mul_f32 v146, %[sc], v146\n\t" \
  "v_mul_f32 v147, %[sc], v147\n\t" \
  "v_mul_f32 v148, %[sc], v148\n\t" \
  "v_mul_f32 v149, %[sc], v149\n\t" \
  "v_mul_f32 v150, %[sc], v150\n\t" \
  "v_mul_f32 v151, %[sc], v151\n\t" \
  "v_cvt_pk_bf16_f32 v152, v144, v145\n\t" \
  "v_cvt_pk_bf16_f32 v153, v146, v147\n\t" \
  "v_cvt_pk_bf16_f32 v154, v148, v149\n\t" \
  "v_cvt_pk_bf16_f32 v155, v150, v151\n\t" \
  "v_accvgpr_write_b32 a148, v152\n\t" \
  "v_accvgpr_write_b32 a149, v153\n\t" \
  "v_accvgpr_write_b32 a150, v154\n\t" \
  "v_accvgpr_write_b32 a151, v155\n\t" \
  "v_accvgpr_read_b32 v128, a96\n\t" \
  "v_accvgpr_read_b32 v129, a97\n\t" \
  "v_accvgpr_read_b32 v130, a98\n\t" \
  "v_accvgpr_read_b32 v131, a99\n\t" \
  "v_accvgpr_read_b32 v132, a100\n\t" \
  "v_accvgpr_read_b32 v133, a101\n\t" \
  "v_accvgpr_read_b32 v134, a102\n\t" \
  "v_accvgpr_read_b32 v135, a103\n\t" \
  "v_mul_f32 v128, %[sc], v128\n\t" \
  "v_mul_f32 v129, %[sc], v129\n\t" \
  "v_mul_f32 v130, %[sc], v130\n\t" \
  "v_mul_f32 v131, %[sc], v131\n\t" \
  "v_mul_f32 v132, %[sc], v132\n\t" \
  "v_mul_f32 v133, %[sc], v133\n\t" \
  "v_mul_f32 v134, %[sc], v134\n\t" \
  "v_mul_f32 v135, %[sc], v135\n\t" \
  "v_cvt_pk_bf16_f32 v136, v128, v129\n\t" \
  "v_cvt_pk_bf16_f32 v137, v130, v131\n\t" \
  "v_cvt_pk_bf16_f32 v138, v132, v133\n\t" \
  "v_cvt_pk_bf16_f32 v139, v134, v135\n\t" \
  "v_accvgpr_write_b32 a152, v136\n\t" \
  "v_accvgpr_write_b32 a153, v137\n\t" \
  "v_accvgpr_write_b32 a154, v138\n\t" \
  "v_accvgpr_write_b32 a155, v139\n\t" \
  "v_accvgpr_read_b32 v144, a104\n\t" \
  "v_accvgpr_read_b32 v145, a105\n\t" \
  "v_accvgpr_read_b32 v146, a106\n\t" \
  "v_accvgpr_read_b32 v147, a107\n\t" \
  "v_accvgpr_read_b32 v148, a108\n\t" \
  "v_accvgpr_read_b32 v149, a109\n\t" \
  "v_accvgpr_read_b32 v150, a110\n\t" \
  "v_accvgpr_read_b32 v151, a111\n\t" \
  "v_mul_f32 v144, %[sc], v144\n\t" \
  "v_mul_f32 v145, %[sc], v145\n\t" \
  "v_mul_f32 v146, %[sc], v146\n\t" \
  "v_mul_f32 v147, %[sc], v147\n\t" \
  "v_mul_f32 v148, %[sc], v148\n\t" \
  "v_mul_f32 v149, %[sc], v149\n\t" \
  "v_mul_f32 v150, %[sc], v150\n\t" \
  "v_mul_f32 v151, %[sc], v151\n\t" \
  "v_cvt_pk_bf16_f32 v152, v144, v145\n\t" \
  "v_cvt_pk_bf16_f32 v153, v146, v147\n\t" \
  "v_cvt_pk_bf16_f32 v154, v148, v149\n\t" \
  "v_cvt_pk_bf16_f32 v155, v150, v151\n\t" \
  "v_accvgpr_write_b32 a156, v152\n\t" \
  "v_accvgpr_write_b32 a157, v153\n\t" \
  "v_accvgpr_write_b32 a158, v154\n\t" \
  "v_accvgpr_write_b32 a159, v155\n\t" \
  "v_accvgpr_read_b32 v128, a16\n\t" \
  "v_accvgpr_read_b32 v129, a17\n\t" \
  "v_accvgpr_read_b32 v130, a18\n\t" \
  "v_accvgpr_read_b32 v131, a19\n\t" \
  "v_accvgpr_read_b32 v132, a20\n\t" \
  "v_accvgpr_read_b32 v133, a21\n\t" \
  "v_accvgpr_read_b32 v134, a22\n\t" \
  "v_accvgpr_read_b32 v135, a23\n\t" \
  "v_mul_f32 v128, %[sc], v128\n\t" \
  "v_mul_f32 v129, %[sc], v129\n\t" \
  "v_mul_f32 v130, %[sc], v130\n\t" \
  "v_mul_f32 v131, %[sc], v131\n\t" \
  "v_mul_f32 v132, %[sc], v132\n\t" \
  "v_mul_f32 v133, %[sc], v133\n\t" \
  "v_mul_f32 v134, %[sc], v134\n\t" \
  "v_mul_f32 v135, %[sc], v135\n\t" \
  "v_cvt_pk_bf16_f32 v136, v128, v129\n\t" \
  "v_cvt_pk_bf16_f32 v137, v130, v131\n\t" \
  "v_cvt_pk_bf16_f32 v138, v132, v133\n\t" \
  "v_cvt_pk_bf16_f32 v139, v134, v135\n\t" \
  "v_accvgpr_write_b32 a160, v136\n\t" \
  "v_accvgpr_write_b32 a161, v137\n\t" \
  "v_accvgpr_write_b32 a162, v138\n\t" \
  "v_accvgpr_write_b32 a163, v139\n\t" \
  "v_accvgpr_read_b32 v144, a24\n\t" \
  "v_accvgpr_read_b32 v145, a25\n\t" \
  "v_accvgpr_read_b32 v146, a26\n\t" \
  "v_accvgpr_read_b32 v147, a27\n\t" \
  "v_accvgpr_read_b32 v148, a28\n\t" \
  "v_accvgpr_read_b32 v149, a29\n\t" \
  "v_accvgpr_read_b32 v150, a30\n\t" \
  "v_accvgpr_read_b32 v151, a31\n\t" \
  "v_mul_f32 v144, %[sc], v144\n\t" \
  "v_mul_f32 v145, %[sc], v145\n\t" \
  "v_mul_f32 v146, %[sc], v146\n\t" \
  "v_mul_f32 v147, %[sc], v147\n\t" \
  "v_mul_f32 v148, %[sc], v148\n\t" \
  "v_mul_f32 v149, %[sc], v149\n\t" \
  "v_mul_f32 v150, %[sc], v150\n\t" \
  "v_mul_f32 v151, %[sc], v151\n\t" \
  "v_cvt_pk_bf16_f32 v152, v144, v145\n\t" \
  "v_cvt_pk_bf16_f32 v153, v146, v147\n\t" \
  "v_cvt_pk_bf16_f32 v154, v148, v149\n\t" \
  "v_cvt_pk_bf16_f32 v155, v150, v151\n\t" \
  "v_accvgpr_write_b32 a164, v152\n\t" \
  "v_accvgpr_write_b32 a165, v153\n\t" \
  "v_accvgpr_write_b32 a166, v154\n\t" \
  "v_accvgpr_write_b32 a167, v155\n\t" \
  "v_accvgpr_read_b32 v128, a48\n\t" \
  "v_accvgpr_read_b32 v129, a49\n\t" \
  "v_accvgpr_read_b32 v130, a50\n\t" \
  "v_accvgpr_read_b32 v131, a51\n\t" \
  "v_accvgpr_read_b32 v132, a52\n\t" \
  "v_accvgpr_read_b32 v133, a53\n\t" \
  "v_accvgpr_read_b32 v134, a54\n\t" \
  "v_accvgpr_read_b32 v135, a55\n\t" \
  "v_mul_f32 v128, %[sc], v128\n\t" \
  "v_mul_f32 v129, %[sc], v129\n\t" \
  "v_mul_f32 v130, %[sc], v130\n\t" \
  "v_mul_f32 v131, %[sc], v131\n\t" \
  "v_mul_f32 v132, %[sc], v132\n\t" \
  "v_mul_f32 v133, %[sc], v133\n\t" \
  "v_mul_f32 v134, %[sc], v134\n\t" \
  "v_mul_f32 v135, %[sc], v135\n\t" \
  "v_cvt_pk_bf16_f32 v136, v128, v129\n\t" \
  "v_cvt_pk_bf16_f32 v137, v130, v131\n\t" \
  "v_cvt_pk_bf16_f32 v138, v132, v133\n\t" \
  "v_cvt_pk_bf16_f32 v139, v134, v135\n\t" \
  "v_accvgpr_write_b32 a168, v136\n\t" \
  "v_accvgpr_write_b32 a169, v137\n\t" \
  "v_accvgpr_write_b32 a170, v138\n\t" \
  "v_accvgpr_write_b32 a171, v139\n\t" \
  "v_accvgpr_read_b32 v144, a56\n\t" \
  "v_accvgpr_read_b32 v145, a57\n\t" \
  "v_accvgpr_read_b32 v146, a58\n\t" \
  "v_accvgpr_read_b32 v147, a59\n\t" \
  "v_accvgpr_read_b32 v148, a60\n\t" \
  "v_accvgpr_read_b32 v149, a61\n\t" \
  "v_accvgpr_read_b32 v150, a62\n\t" \
  "v_accvgpr_read_b32 v151, a63\n\t" \
  "v_mul_f32 v144, %[sc], v144\n\t" \
  "v_mul_f32 v145, %[sc], v145\n\t" \
  "v_mul_f32 v146, %[sc], v146\n\t" \
  "v_mul_f32 v147, %[sc], v147\n\t" \
  "v_mul_f32 v148, %[sc], v148\n\t" \
  "v_mul_f32 v149, %[sc], v149\n\t" \
  "v_mul_f32 v150, %[sc], v150\n\t" \
  "v_mul_f32 v151, %[sc], v151\n\t" \
  "v_cvt_pk_bf16_f32 v152, v144, v145\n\t" \
  "v_cvt_pk_bf16_f32 v153, v146, v147\n\t" \
  "v_cvt_pk_bf16_f32 v154, v148, v149\n\t" \
  "v_cvt_pk_bf16_f32 v155, v150, v151\n\t" \
  "v_accvgpr_write_b32 a172, v152\n\t" \
  "v_accvgpr_write_b32 a173, v153\n\t" \
  "v_accvgpr_write_b32 a174, v154\n\t" \
  "v_accvgpr_write_b32 a175, v155\n\t" \
  "v_accvgpr_read_b32 v128, a80\n\t" \
  "v_accvgpr_read_b32 v129, a81\n\t" \
  "v_accvgpr_read_b32 v130, a82\n\t" \
  "v_accvgpr_read_b32 v131, a83\n\t" \
  "v_accvgpr_read_b32 v132, a84\n\t" \
  "v_accvgpr_read_b32 v133, a85\n\t" \
  "v_accvgpr_read_b32 v134, a86\n\t" \
  "v_accvgpr_read_b32 v135, a87\n\t" \
  "v_mul_f32 v128, %[sc], v128\n\t" \
  "v_mul_f32 v129, %[sc], v129\n\t" \
  "v_mul_f32 v130, %[sc], v130\n\t" \
  "v_mul_f32 v131, %[sc], v131\n\t" \
  "v_mul_f32 v132, %[sc], v132\n\t" \
  "v_mul_f32 v133, %[sc], v133\n\t" \
  "v_mul_f32 v134, %[sc], v134\n\t" \
  "v_mul_f32 v135, %[sc], v135\n\t" \
  "v_cvt_pk_bf16_f32 v136, v128, v129\n\t" \
  "v_cvt_pk_bf16_f32 v137, v130, v131\n\t" \
  "v_cvt_pk_bf16_f32 v138, v132, v133\n\t" \
  "v_cvt_pk_bf16_f32 v139, v134, v135\n\t" \
  "v_accvgpr_write_b32 a176, v136\n\t" \
  "v_accvgpr_write_b32 a177, v137\n\t" \
  "v_accvgpr_write_b32 a178, v138\n\t" \
  "v_accvgpr_write_b32 a179, v139\n\t" \
  "v_accvgpr_read_b32 v144, a88\n\t" \
  "v_accvgpr_read_b32 v145, a89\n\t" \
  "v_accvgpr_read_b32 v146, a90\n\t" \
  "v_accvgpr_read_b32 v147, a91\n\t" \
  "v_accvgpr_read_b32 v148, a92\n\t" \
  "v_accvgpr_read_b32 v149, a93\n\t" \
  "v_accvgpr_read_b32 v150, a94\n\t" \
  "v_accvgpr_read_b32 v151, a95\n\t" \
  "v_mul_f32 v144, %[sc], v144\n\t" \
  "v_mul_f32 v145, %[sc], v145\n\t" \
  "v_mul_f32 v146, %[sc], v146\n\t" \
  "v_mul_f32 v147, %[sc], v147\n\t" \
  "v_mul_f32 v148, %[sc], v148\n\t" \
  "v_mul_f32 v149, %[sc], v149\n\t" \
  "v_mul_f32 v150, %[sc], v150\n\t" \
  "v_mul_f32 v151, %[sc], v151\n\t" \
  "v_cvt_pk_bf16_f32 v152, v144, v145\n\t" \
  "v_cvt_pk_bf16_f32 v153, v146, v147\n\t" \
  "v_cvt_pk_bf16_f32 v154, v148, v149\n\t" \
  "v_cvt_pk_bf16_f32 v155, v150, v151\n\t" \
  "v_accvgpr_write_b32 a180, v152\n\t" \
  "v_accvgpr_write_b32 a181, v153\n\t" \
  "v_accvgpr_write_b32 a182, v154\n\t" \
  "v_accvgpr_write_b32 a183, v155\n\t" \
  "v_accvgpr_read_b32 v128, a112\n\t" \
  "v_accvgpr_read_b32 v129, a113\n\t" \
  "v_accvgpr_read_b32 v130, a114\n\t" \
  "v_accvgpr_read_b32 v131, a115\n\t" \
  "v_accvgpr_read_b32 v132, a116\n\t" \
  "v_accvgpr_read_b32 v133, a117\n\t" \
  "v_accvgpr_read_b32 v134, a118\n\t" \
  "v_accvgpr_read_b32 v135, a119\n\t" \
  "v_mul_f32 v128, %[sc], v128\n\t" \
  "v_mul_f32 v129, %[sc], v129\n\t" \
  "v_mul_f32 v130, %[sc], v130\n\t" \
  "v_mul_f32 v131, %[sc], v131\n\t" \
  "v_mul_f32 v132, %[sc], v132\n\t" \
  "v_mul_f32 v133, %[sc], v133\n\t" \
  "v_mul_f32 v134, %[sc], v134\n\t" \
  "v_mul_f32 v135, %[sc], v135\n\t" \
  "v_cvt_pk_bf16_f32 v136, v128, v129\n\t" \
  "v_cvt_pk_bf16_f32 v137, v130, v131\n\t" \
  "v_cvt_pk_bf16_f32 v138, v132, v133\n\t" \
  "v_cvt_pk_bf16_f32 v139, v134, v135\n\t" \
  "v_accvgpr_write_b32 a184, v136\n\t" \
  "v_accvgpr_write_b32 a185, v137\n\t" \
  "v_accvgpr_write_b32 a186, v138\n\t" \
  "v_accvgpr_write_b32 a187, v139\n\t" \
  "v_accvgpr_read_b32 v144, a120\n\t" \
  "v_accvgpr_read_b32 v145, a121\n\t" \
  "v_accvgpr_read_b32 v146, a122\n\t" \
  "v_accvgpr_read_b32 v147, a123\n\t" \
  "v_accvgpr_read_b32 v148, a124\n\t" \
  "v_accvgpr_read_b32 v149, a125\n\t" \
  "v_accvgpr_read_b32 v150, a126\n\t" \
  "v_accvgpr_read_b32 v151, a127\n\t" \
  "v_mul_f32 v144, %[sc], v144\n\t" \
  "v_mul_f32 v145, %[sc], v145\n\t" \
  "v_mul_f32 v146, %[sc], v146\n\t" \
  "v_mul_f32 v147, %[sc], v147\n\t" \
  "v_mul_f32 v148, %[sc], v148\n\t" \
  "v_mul_f32 v149, %[sc], v149\n\t" \
  "v_mul_f32 v150, %[sc], v150\n\t" \
  "v_mul_f32 v151, %[sc], v151\n\t" \
  "v_cvt_pk_bf16_f32 v152, v144, v145\n\t" \
  "v_cvt_pk_bf16_f32 v153, v146, v147\n\t" \
  "v_cvt_pk_bf16_f32 v154, v148, v149\n\t" \
  "v_cvt_pk_bf16_f32 v155, v150, v151\n\t" \
  "v_accvgpr_write_b32 a188, v152\n\t" \
  "v_accvgpr_write_b32 a189, v153\n\t" \
  "v_accvgpr_write_b32 a190, v154\n\t" \
  "v_accvgpr_write_b32 a191, v155\n\t" \
  ""
#define ATTN_STREAM \
  "s_mov_b32 %[sp], 0\n\t" \
  "v_mfma_f32_32x32x16_bf16 v[0:15], a[192:195], a[128:131], 0\n\t" \
  "ds_read_b64_tr_b16 v[160:161], v250 offset:0\n\t" \
  "ds_read_b64_tr_b16 v[162:163], v250 offset:256\n\t" \
  "v_accvgpr_write_b32 a0, 0\n\t" \
  "v_accvgpr_write_b32 a1, 0\n\t" \
  "v_accvgpr_write_b32 a2, 0\n\t" \
  "v_accvgpr_write_b32 a3, 0\n\t" \
  "v_mfma_f32_32x32x16_bf16 v[16:31], a[192:195], a[160:163], 0\n\t" \
  "ds_read_b64_tr_b16 v[164:165], v250 offset:512\n\t" \
  "ds_read_b64_tr_b16 v[166:167], v250 offset:768\n\t" \
  "v_accvgpr_write_b32 a4, 0\n\t" \
  "v_accvgpr_write_b32 a5, 0\n\t" \
  "v_accvgpr_write_b32 a6, 0\n\t" \
  "v_accvgpr_write_b32 a7, 0\n\t" \
  "v_mfma_f32_32x32x16_bf16 v[32:47], a[224:227], a[128:131], 0\n\t" \
  "ds_read_b64_tr_b16 v[168:169], v250 offset:2048\n\t" \
  "ds_read_b64_tr_b16 v[170:171], v250 offset:2304\n\t" \
  "v_accvgpr_write_b32 a8, 0\n\t" \
  "v_accvgpr_write_b32 a9, 0\n\t" \
  "v_accvgpr_write_b32 a10, 0\n\t" \
  "v_accvgpr_write_b32 a11, 0\n\t" \
  "v_mfma_f32_32x32x16_bf16 v[48:63], a[224:227], a[160:163], 0\n\t" \
  "ds_read_b64_tr_b16 v[172:173], v250 offset:2560\n\t" \
  "ds_read_b64_tr_b16 v[174:175], v250 offset:2816\n\t" \
  "v_accvgpr_write_b32 a12, 0\n\t" \
  "v_accvgpr_write_b32 a13, 0\n\t" \
  "v_accvgpr_write_b32 a14, 0\n\t" \
  "v_accvgpr_write_b32 a15, 0\n\t" \
  "v_mfma_f32_32x32x16_bf16 v[0:15], a[196:199], a[132:135], v[0:15]\n\t" \
  "ds_read_b64_tr_b16 v[176:177], v250 offset:16384\n\t" \
  "ds_read_b64_tr_b16 v[178:179], v250 offset:16640\n\t" \
  "v_accvgpr_write_b32 a16, 0\n\t" \
  "v_accvgpr_write_b32 a17, 0\n\t" \
  "v_accvgpr_write_b32 a18, 0\n\t" \
  "v_accvgpr_write_b32 a19, 0\n\t" \
  "v_mfma_f32_32x32x16_bf16 v[16:31], a[196:199], a[164:167], v[16:31]\n\t" \
  "ds_read_b64_tr_b16 v[180:181], v250 offset:16896\n\t" \
  "ds_read_b64_tr_b16 v[182:183], v250 offset:17152\n\t" \
  "v_accvgpr_write_b32 a20, 0\n\t" \
  "v_accvgpr_write_b32 a21, 0\n\t" \
  "v_accvgpr_write_b32 a22, 0\n\t" \
  "v_accvgpr_write_b32 a23, 0\n\t" \
  "v_mfma_f32_32x32x16_bf16 v[32:47], a[228:231], a[132:135], v[32:47]\n\t" \
  "ds_read_b64_tr_b16 v[184:185], v250 offset:18432\n\t" \
  "ds_read_b64_tr_b16 v[186:187], v250 offset:18688\n\t" \
  "v_accvgpr_write_b32 a24, 0\n\t" \
  "v_accvgpr_write_b32 a25, 0\n\t" \
  "v_accvgpr_write_b32 a26, 0\n\t" \
  "v_accvgpr_write_b32 a27, 0\n\t" \
  "v_mfma_f32_32x32x16_bf16 v[48:63], a[228:231], a[164:167], v[48:63]\n\t" \
  "ds_read_b64_tr_b16 v[188:189], v250 offset:18944\n\t" \
  "ds_read_b64_tr_b16 v[190:191], v250 offset:19200\n\t" \
  "v_accvgpr_write_b32 a28, 0\n\t" \
  "v_accvgpr_write_b32 a29, 0\n\t" \
  "v_accvgpr_write_b32 a30, 0\n\t" \
  "v_accvgpr_write_b32 a31, 0\n\t" \
  "v_mfma_f32_32x32x16_bf16 v[0:15], a[200:203], a[136:139], v[0:15]\n\t" \
  "v_accvgpr_write_b32 a32, 0\n\t" \
  "v_accvgpr_write_b32 a33, 0\n\t" \
  "v_accvgpr_write_b32 a34, 0\n\t" \
  "v_accvgpr_write_b32 a35, 0\n\t" \
  "v_mfma_f32_32x32x16_bf16 v[16:31], a[200:203], a[168:171], v[16:31]\n\t" \
  "v_accvgpr_write_b32 a36, 0\n\t" \
  "v_accvgpr_write_b32 a37, 0\n\t" \
  "v_accvgpr_write_b32 a38, 0\n\t" \
  "v_accvgpr_write_b32 a39, 0\n\t" \
  "v_mfma_f32_32x32x16_bf16 v[32:47], a[232:235], a[136:139], v[32:47]\n\t" \
  "v_accvgpr_write_b32 a40, 0\n\t" \
  "v_accvgpr_write_b32 a41, 0\n\t" \
  "v_accvgpr_write_b32 a42, 0\n\t" \
  "v_accvgpr_write_b32 a43, 0\n\t" \
  "s_waitcnt lgkmcnt(0)\n\t" \
  "v_mfma_f32_32x32x16_bf16 v[48:63], a[232:235], a[168:171], v[48:63]\n\t" \
  "v_accvgpr_write_b32 a44, 0\n\t" \
  "v_accvgpr_write_b32 a45, 0\n\t" \
  "v_accvgpr_write_b32 a46, 0\n\t" \
  "v_accvgpr_write_b32 a47, 0\n\t" \
  "buffer_store_dwordx4 v[160:163], v251, %[srdwt], 0 offen offset:0\n\t" \
  "v_mfma_f32_32x32x16_bf16 v[0:15], a[204:207], a[140:143], v[0:15]\n\t" \
  "v_accvgpr_write_b32 a48, 0\n\t" \
  "v_accvgpr_write_b32 a49, 0\n\t" \
  "v_accvgpr_write_b32 a50, 0\n\t" \
  "v_accvgpr_write_b32 a51, 0\n\t" \
  "buffer_store_dwordx4 v[164:167], v251, %[srdwt], 0 offen offset:1024\n\t" \
  "v_mfma_f32_32x32x16_bf16 v[16:31], a[204:207], a[172:175], v[16:31]\n\t" \
  "v_accvgpr_write_b32 a52, 0\n\t" \
  "v_accvgpr_write_b32 a53, 0\n\t" \
  "v_accvgpr_write_b32 a54, 0\n\t" \
  "v_accvgpr_write_b32 a55, 0\n\t" \
  "buffer_store_dwordx4 v[168:171], v251, %[srdwt], 0 offen offset:2048\n\t" \
  "v_mfma_f32_32x32x16_bf16 v[32:47], a[236:239], a[140:143], v[32:47]\n\t" \
  "v_accvgpr_write_b32 a56, 0\n\t" \
  "v_accvgpr_write_b32 a57, 0\n\t" \
  "v_accvgpr_write_b32 a58, 0\n\t" \
  "v_accvgpr_write_b32 a59, 0\n\t" \
  "buffer_store_dwordx4 v[172:175], v251, %[srdwt], 0 offen offset:3072\n\t" \
  "v_mfma_f32_32x32x16_bf16 v[48:63], a[236:239], a[172:175], v[48:63]\n\t" \
  "v_accvgpr_write_b32 a60, 0\n\t" \
  "v_accvgpr_write_b32 a61, 0\n\t" \
  "v_accvgpr_write_b32 a62, 0\n\t" \
  "v_accvgpr_write_b32 a63, 0\n\t" \
  "buffer_store_dwordx4 v[176:179], v251, %[srdwtd], 0 offen offset:0\n\t" \
  "v_mfma_f32_32x32x16_bf16 v[0:15], a[208:211], a[144:147], v[0:15]\n\t" \
  "v_accvgpr_write_b32 a64, 0\n\t" \
  "v_accvgpr_write_b32 a65, 0\n\t" \
  "v_accvgpr_write_b32 a66, 0\n\t" \
  "v_accvgpr_write_b32 a67, 0\n\t" \
  "buffer_store_dwordx4 v[180:183], v251, %[srdwtd], 0 offen offset:1024\n\t" \
  "v_mfma_f32_32x32x16_bf16 v[16:31], a[208:211], a[176:179], v[16:31]\n\t" \
  "v_accvgpr_write_b32 a68, 0\n\t" \
  "v_accvgpr_write_b32 a69, 0\n\t" \
  "v_accvgpr_write_b32 a70, 0\n\t" \
  "v_accvgpr_write_b32 a71, 0\n\t" \
  "buffer_store_dwordx4 v[184:187], v251, %[srdwtd], 0 offen offset:2048\n\t" \
  "v_mfma_f32_32x32x16_bf16 v[32:47], a[240:243], a[144:147], v[32:47]\n\t" \
  "v_accvgpr_write_b32 a72, 0\n\t" \
  "v_accvgpr_write_b32 a73, 0\n\t" \
  "v_accvgpr_write_b32 a74, 0\n\t" \
  "v_accvgpr_write_b32 a75, 0\n\t" \
  "buffer_store_dwordx4 v[188:191], v251, %[srdwtd], 0 offen offset:3072\n\t" \
  "v_mfma_f32_32x32x16_bf16 v[48:63], a[240:243], a[176:179], v[48:63]\n\t" \
  "v_accvgpr_write_b32 a76, 0\n\t" \
  "v_accvgpr_write_b32 a77, 0\n\t" \
  "v_accvgpr_write_b32 a78, 0\n\t" \
  "v_accvgpr_write_b32 a79, 0\n\t" \
  "v_mfma_f32_32x32x16_bf16 v[0:15], a[212:215], a[148:151], v[0:15]\n\t" \
  "v_accvgpr_write_b32 a80, 0\n\t" \
  "v_accvgpr_write_b32 a81, 0\n\t" \
  "v_accvgpr_write_b32 a82, 0\n\t" \
  "v_accvgpr_write_b32 a83, 0\n\t" \
  "v_mfma_f32_32x32x16_bf16 v[16:31], a[212:215], a[180:183], v[16:31]\n\t" \
  "v_accvgpr_write_b32 a84, 0\n\t" \
  "v_accvgpr_write_b32 a85, 0\n\t" \
  "v_accvgpr_write_b32 a86, 0\n\t" \
  "v_accvgpr_write_b32 a87, 0\n\t" \
  "v_mfma_f32_32x32x16_bf16 v[32:47], a[244:247], a[148:151], v[32:47]\n\t" \
  "v_accvgpr_write_b32 a88, 0\n\t" \
  "v_accvgpr_write_b32 a89, 0\n\t" \
  "v_accvgpr_write_b32 a90, 0\n\t" \
  "v_accvgpr_write_b32 a91, 0\n\t" \
  "v_mfma_f32_32x32x16_bf16 v[48:63], a[244:247], a[180:183], v[48:63]\n\t" \
  "v_accvgpr_write_b32 a92, 0\n\t" \
  "v_accvgpr_write_b32 a93, 0\n\t" \
  "v_accvgpr_write_b32 a94, 0\n\t" \
  "v_accvgpr_write_b32 a95, 0\n\t" \
  "v_mfma_f32_32x32x16_bf16 v[0:15], a[216:219], a[152:155], v[0:15]\n\t" \
  "v_accvgpr_write_b32 a96, 0\n\t" \
  "v_accvgpr_write_b32 a97, 0\n\t" \
  "v_accvgpr_write_b32 a98, 0\n\t" \
  "v_accvgpr_write_b32 a99, 0\n\t" \
  "v_mfma_f32_32x32x16_bf16 v[16:31], a[216:219], a[184:187], v[16:31]\n\t" \
  "v_accvgpr_write_b32 a100, 0\n\t" \
  "v_accvgpr_write_b32 a101, 0\n\t" \
  "v_accvgpr_write_b32 a102, 0\n\t" \
  "v_accvgpr_write_b32 a103, 0\n\t" \
  "v_mfma_f32_32x32x16_bf16 v[32:47], a[248:251], a[152:155], v[32:47]\n\t" \
  "v_accvgpr_write_b32 a104, 0\n\t" \
  "v_accvgpr_write_b32 a105, 0\n\t" \
  "v_accvgpr_write_b32 a106, 0\n\t" \
  "v_accvgpr_write_b32 a107, 0\n\t" \
  "v_mfma_f32_32x32x16_bf16 v[48:63], a[248:251], a[184:187], v[48:63]\n\t" \
  "v_accvgpr_write_b32 a108, 0\n\t" \
  "v_accvgpr_write_b32 a109, 0\n\t" \
  "v_accvgpr_write_b32 a110, 0\n\t" \
  "v_accvgpr_write_b32 a111, 0\n\t" \
  "v_mfma_f32_32x32x16_bf16 v[0:15], a[220:223], a[156:159], v[0:15]\n\t" \
  "v_accvgpr_write_b32 a112, 0\n\t" \
  "v_accvgpr_write_b32 a113, 0\n\t" \
  "v_accvgpr_write_b32 a114, 0\n\t" \
  "v_accvgpr_write_b32 a115, 0\n\t" \
  "v_mfma_f32_32x32x16_bf16 v[16:31], a[220:223], a[188:191], v[16:31]\n\t" \
  "v_accvgpr_write_b32 a116, 0\n\t" \
  "v_accvgpr_write_b32 a117, 0\n\t" \
  "v_accvgpr_write_b32 a118, 0\n\t" \
  "v_accvgpr_write_b32 a119, 0\n\t" \
  "v_mfma_f32_32x32x16_bf16 v[32:47], a[252:255], a[156:159], v[32:47]\n\t" \
  "v_accvgpr_write_b32 a120, 0\n\t" \
  "v_accvgpr_write_b32 a121, 0\n\t" \
  "v_accvgpr_write_b32 a122, 0\n\t" \
  "v_accvgpr_write_b32 a123, 0\n\t" \
  "v_mfma_f32_32x32x16_bf16 v[48:63], a[252:255], a[188:191], v[48:63]\n\t" \
  "v_accvgpr_write_b32 a124, 0\n\t" \
  "v_accvgpr_write_b32 a125, 0\n\t" \
  "v_accvgpr_write_b32 a126, 0\n\t" \
  "v_accvgpr_write_b32 a127, 0\n\t" \
  "s_waitcnt vmcnt(0) lgkmcnt(0)\n\t" \
  "s_barrier\n\t" \
  "s_add_u32 m0, %[m0k], 0\n\t" \
  "ds_read_b128 a[192:195], v224 offset:16384\n\t" \
  "v_max3_f32 v238, v0, v1, v2\n\t" \
  "v_max3_f32 v240, v16, v17, v18\n\t" \
  "v_max3_f32 v238, v238, v3, v4\n\t" \
  "ds_read_b128 a[196:199], v225 offset:16384\n\t" \
  "buffer_load_dwordx4 v229, %[srdk], %[sk] offen offset:0 lds\n\t" \
  "v_max3_f32 v240, v240, v19, v20\n\t" \
  "v_max3_f32 v239, v33, v34, v35\n\t" \
  "v_max3_f32 v241, v49, v50, v51\n\t" \
  "ds_read_b128 a[200:203], v226 offset:16384\n\t" \
  "v_max3_f32 v238, v238, v5, v6\n\t" \
  "v_max3_f32 v240, v240, v21, v22\n\t" \
  "v_max3_f32 v239, v239, v36, v37\n\t" \
  "v_max3_f32 v241, v241, v52, v53\n\t" \
  "ds_read_b128 a[204:207], v227 offset:16384\n\t" \
  "buffer_load_dwordx4 v230, %[srdk], %[sk] offen offset:1024 lds\n\t" \
  "v_max3_f32 v238, v238, v7, v8\n\t" \
  "v_max3_f32 v240, v240, v23, v24\n\t" \
  "v_max3_f32 v239, v239, v38, v39\n\t" \
  "ds_read_b128 a[208:211], v224 offset:16512\n\t" \
  "v_max3_f32 v241, v241, v54, v55\n\t" \
  "v_max3_f32 v238, v238, v9, v10\n\t" \
  "v_max3_f32 v240, v240, v25, v26\n\t" \
  "ds_read_b128 a[212:215], v225 offset:16512\n\t" \
  "buffer_load_dwordx4 v229, %[srdk], %[sk] offen offset:2048 lds\n\t" \
  "v_max3_f32 v239, v239, v40, v41\n\t" \
  "v_max3_f32 v241, v241, v56, v57\n\t" \
  "v_max3_f32 v238, v238, v11, v12\n\t" \
  "v_max3_f32 v240, v240, v27, v28\n\t" \
  "ds_read_b128 a[216:219], v226 offset:16512\n\t" \
  "v_max3_f32 v239, v239, v42, v43\n\t" \
  "v_max3_f32 v241, v241, v58, v59\n\t" \
  "v_max3_f32 v238, v238, v13, v14\n\t" \
  "ds_read_b128 a[220:223], v227 offset:16512\n\t" \
  "buffer_load_dwordx4 v230, %[srdk], %[sk] offen offset:3072 lds\n\t" \
  "v_max3_f32 v240, v240, v29, v30\n\t" \
  "v_max3_f32 v239, v239, v44, v45\n\t" \
  "v_max3_f32 v241, v241, v60, v61\n\t" \
  "s_add_u32 m0, %[m0v], 16384\n\t" \
  "ds_read_b128 a[224:227], v224 offset:24576\n\t" \
  "v_max3_f32 v238, v238, v15, v32\n\t" \
  "v_max3_f32 v240, v240, v31, v48\n\t" \
  "v_max3_f32 v239, v239, v46, v47\n\t" \
  "v_max3_f32 v241, v241, v62, v63\n\t" \
  "ds_read_b128 a[228:231], v225 offset:24576\n\t" \
  "buffer_load_dwordx4 v231, %[srdv], %[sv] offen offset:0 lds\n\t" \
  "v_max_f32 v242, v238, v239\n\t" \
  "v_max_f32 v243, v240, v241\n\t" \
  "v_mov_b32 v246, v242\n\t" \
  "v_mov_b32 v247, v243\n\t" \
  "ds_read_b128 a[232:235], v226 offset:24576\n\t" \
  "s_nop 1\n\t" \
  "v_permlane32_swap_b32 v242, v246\n\t" \
  "v_permlane32_swap_b32 v243, v247\n\t" \
  "v_max_f32 v242, v242, v246\n\t" \
  "v_max_f32 v243, v243, v247\n\t" \
  "ds_read_b128 a[236:239], v227 offset:24576\n\t" \
  "buffer_load_dwordx4 v231, %[srdv], %[sv2] offen offset:1024 lds\n\t" \
  "v_add_f32 v236, %[smarg], v242\n\t" \
  "v_add_f32 v237, %[smarg], v243\n\t" \
  "v_sub_f32 v0, v0, v236\n\t" \
  "v_sub_f32 v1, v1, v236\n\t" \
  "v_sub_f32 v2, v2, v236\n\t" \
  "v_sub_f32 v3, v3, v236\n\t" \
  "v_sub_f32 v4, v4, v236\n\t" \
  "v_sub_f32 v5, v5, v236\n\t" \
  "v_sub_f32 v6, v6, v236\n\t" \
  "v_sub_f32 v7, v7, v236\n\t" \
  "v_sub_f32 v8, v8, v236\n\t" \
  "v_sub_f32 v9, v9, v236\n\t" \
  "v_sub_f32 v10, v10, v236\n\t" \
  "v_sub_f32 v11, v11, v236\n\t" \
  "v_sub_f32 v12, v12, v236\n\t" \
  "v_sub_f32 v13, v13, v236\n\t" \
  "v_sub_f32 v14, v14, v236\n\t" \
  "v_sub_f32 v15, v15, v236\n\t" \
  "v_sub_f32 v32, v32, v236\n\t" \
  "v_sub_f32 v33, v33, v236\n\t" \
  "v_sub_f32 v34, v34, v236\n\t" \
  "v_sub_f32 v35, v35, v236\n\t" \
  "v_sub_f32 v36, v36, v236\n\t" \
  "v_sub_f32 v37, v37, v236\n\t" \
  "v_sub_f32 v38, v38, v236\n\t" \
  "v_sub_f32 v39, v39, v236\n\t" \
  "v_sub_f32 v40, v40, v236\n\t" \
  "v_sub_f32 v41, v41, v236\n\t" \
  "v_sub_f32 v42, v42, v236\n\t" \
  "v_sub_f32 v43, v43, v236\n\t" \
  "v_sub_f32 v44, v44, v236\n\t" \
  "v_sub_f32 v45, v45, v236\n\t" \
  "v_sub_f32 v46, v46, v236\n\t" \
  "v_sub_f32 v47, v47, v236\n\t" \
  "v_sub_f32 v128, 0, v236\n\t" \
  "v_sub_f32 v129, 0, v236\n\t" \
  "v_sub_f32 v130, 0, v236\n\t" \
  "v_sub_f32 v131, 0, v236\n\t" \
  "v_sub_f32 v132, 0, v236\n\t" \
  "v_sub_f32 v133, 0, v236\n\t" \
  "v_sub_f32 v134, 0, v236\n\t" \
  "v_sub_f32 v135, 0, v236\n\t" \
  "v_sub_f32 v136, 0, v236\n\t" \
  "v_sub_f32 v137, 0, v236\n\t" \
  "v_sub_f32 v138, 0, v236\n\t" \
  "v_sub_f32 v139, 0, v236\n\t" \
  "v_sub_f32 v140, 0, v236\n\t" \
  "v_sub_f32 v141, 0, v236\n\t" \
  "v_sub_f32 v142, 0, v236\n\t" \
  "v_sub_f32 v143, 0, v236\n\t" \
  "v_mov_b32 v232, 0\n\t" \
  "v_mov_b32 v233, 0\n\t" \
  "v_sub_f32 v16, v16, v237\n\t" \
  "v_sub_f32 v17, v17, v237\n\t" \
  "v_sub_f32 v18, v18, v237\n\t" \
  "v_sub_f32 v19, v19, v237\n\t" \
  "v_sub_f32 v20, v20, v237\n\t" \
  "v_sub_f32 v21, v21, v237\n\t" \
  "v_sub_f32 v22, v22, v237\n\t" \
  "v_sub_f32 v23, v23, v237\n\t" \
  "v_sub_f32 v24, v24, v237\n\t" \
  "v_sub_f32 v25, v25, v237\n\t" \
  "v_sub_f32 v26, v26, v237\n\t" \
  "v_sub_f32 v27, v27, v237\n\t" \
  "v_sub_f32 v28, v28, v237\n\t" \
  "v_sub_f32 v29, v29, v237\n\t" \
  "v_sub_f32 v30, v30, v237\n\t" \
  "v_sub_f32 v31, v31, v237\n\t" \
  "v_sub_f32 v48, v48, v237\n\t" \
  "v_sub_f32 v49, v49, v237\n\t" \
  "v_sub_f32 v50, v50, v237\n\t" \
  "v_sub_f32 v51, v51, v237\n\t" \
  "v_sub_f32 v52, v52, v237\n\t" \
  "v_sub_f32 v53, v53, v237\n\t" \
  "v_sub_f32 v54, v54, v237\n\t" \
  "v_sub_f32 v55, v55, v237\n\t" \
  "v_sub_f32 v56, v56, v237\n\t" \
  "v_sub_f32 v57, v57, v237\n\t" \
  "v_sub_f32 v58, v58, v237\n\t" \
  "v_sub_f32 v59, v59, v237\n\t" \
  "v_sub_f32 v60, v60, v237\n\t" \
  "v_sub_f32 v61, v61, v237\n\t" \
  "v_sub_f32 v62, v62, v237\n\t" \
  "v_sub_f32 v63, v63, v237\n\t" \
  "v_sub_f32 v144, 0, v237\n\t" \
  "v_sub_f32 v145, 0, v237\n\t" \
  "v_sub_f32 v146, 0, v237\n\t" \
  "v_sub_f32 v147, 0, v237\n\t" \
  "v_sub_f32 v148, 0, v237\n\t" \
  "v_sub_f32 v149, 0, v237\n\t" \
  "v_sub_f32 v150, 0, v237\n\t" \
  "v_sub_f32 v151, 0, v237\n\t" \
  "v_sub_f32 v152, 0, v237\n\t" \
  "v_sub_f32 v153, 0, v237\n\t" \
  "v_sub_f32 v154, 0, v237\n\t" \
  "v_sub_f32 v155, 0, v237\n\t" \
  "v_sub_f32 v156, 0, v237\n\t" \
  "v_sub_f32 v157, 0, v237\n\t" \
  "v_sub_f32 v158, 0, v237\n\t" \
  "v_sub_f32 v159, 0, v237\n\t" \
  "v_mov_b32 v234, 0\n\t" \
  "v_mov_b32 v235, 0\n\t" \
  "ds_read_b128 a[240:243], v224 offset:24704\n\t" \
  "v_exp_f32 v0, v0\n\t" \
  "v_exp_f32 v1, v1\n\t" \
  "s_nop 0\n\t" \
  "ds_read_b128 a[244:247], v225 offset:24704\n\t" \
  "buffer_load_dwordx4 v231, %[srdv], %[sv] offen offset:2048 lds\n\t" \
  "v_add_f32 v232, v232, v0\n\t" \
  "v_add_f32 v233, v233, v1\n\t" \
  "v_cvt_pk_bf16_f32 v0, v0, v1\n\t" \
  "v_exp_f32 v2, v2\n\t" \
  "s_nop 0\n\t" \
  "ds_read_b128 a[248:251], v226 offset:24704\n\t" \
  "v_add_f32 v232, v232, v2\n\t" \
  "v_exp_f32 v3, v3\n\t" \
  "s_nop 0\n\t" \
  "ds_read_b128 a[252:255], v227 offset:24704\n\t" \
  "buffer_load_dwordx4 v231, %[srdv], %[sv2] offen offset:3072 lds\n\t" \
  "v_add_f32 v233, v233, v3\n\t" \
  "v_cvt_pk_bf16_f32 v1, v2, v3\n\t" \
  "v_exp_f32 v4, v4\n\t" \
  "s_nop 0\n\t" \
  "ds_read_b64_tr_b16 v[160:161], v228 offset:0\n\t" \
  "s_add_u32 %[sk], %[sk], 16384\n\t" \
  "v_add_f32 v232, v232, v4\n\t" \
  "v_exp_f32 v5, v5\n\t" \
  "v_exp_f32 v6, v6\n\t" \
  "s_nop 0\n\t" \
  "ds_read_b64_tr_b16 v[162:163], v228 offset:2048\n\t" \
  "s_add_u32 %[sv], %[sv], 16384\n\t" \
  "v_add_f32 v233, v233, v5\n\t" \
  "v_add_f32 v232, v232, v6\n\t" \
  "v_cvt_pk_bf16_f32 v2, v4, v5\n\t" \
  "v_exp_f32 v7, v7\n\t" \
  "s_nop 0\n\t" \
  "ds_read_b64_tr_b16 v[164:165], v228 offset:512\n\t" \
  "s_add_u32 %[sv2], %[sv2], 16384\n\t" \
  "v_add_f32 v233, v233, v7\n\t" \
  "v_cvt_pk_bf16_f32 v3, v6, v7\n\t" \
  "v_exp_f32 v8, v8\n\t" \
  "s_nop 0\n\t" \
  "ds_read_b64_tr_b16 v[166:167], v228 offset:2560\n\t" \
  "v_add_f32 v232, v232, v8\n\t" \
  "v_exp_f32 v9, v9\n\t" \
  "s_nop 0\n\t" \
  "ds_read_b64_tr_b16 v[168:169], v228 offset:1024\n\t" \
  "v_add_f32 v233, v233, v9\n\t" \
  "v_cvt_pk_bf16_f32 v8, v8, v9\n\t" \
  "v_exp_f32 v10, v10\n\t" \
  "v_exp_f32 v11, v11\n\t" \
  "s_nop 0\n\t" \
  "ds_read_b64_tr_b16 v[170:171], v228 offset:3072\n\t" \
  "v_add_f32 v232, v232, v10\n\t" \
  "v_add_f32 v233, v233, v11\n\t" \
  "v_cvt_pk_bf16_f32 v9, v10, v11\n\t" \
  "v_exp_f32 v12, v12\n\t" \
  "s_nop 0\n\t" \
  "ds_read_b64_tr_b16 v[172:173], v228 offset:1536\n\t" \
  "v_add_f32 v232, v232, v12\n\t" \
  "v_exp_f32 v13, v13\n\t" \
  "s_nop 0\n\t" \
  "ds_read_b64_tr_b16 v[174:175], v228 offset:3584\n\t" \
  "v_add_f32 v233, v233, v13\n\t" \
  "v_cvt_pk_bf16_f32 v10, v12, v13\n\t" \
  "v_exp_f32 v14, v14\n\t" \
  "s_nop 0\n\t" \
  "ds_read_b64_tr_b16 v[176:177], v228 offset:4096\n\t" \
  "v_add_f32 v232, v232, v14\n\t" \
  "v_exp_f32 v15, v15\n\t" \
  "v_exp_f32 v16, v16\n\t" \
  "s_nop 0\n\t" \
  "ds_read_b64_tr_b16 v[178:179], v228 offset:6144\n\t" \
  "v_add_f32 v233, v233, v15\n\t" \
  "v_add_f32 v234, v234, v16\n\t" \
  "v_cvt_pk_bf16_f32 v11, v14, v15\n\t" \
  "v_exp_f32 v17, v17\n\t" \
  "s_nop 0\n\t" \
  "ds_read_b64_tr_b16 v[180:181], v228 offset:4608\n\t" \
  "v_add_f32 v235, v235, v17\n\t" \
  "v_cvt_pk_bf16_f32 v16, v16, v17\n\t" \
  "v_exp_f32 v18, v18\n\t" \
  "s_nop 0\n\t" \
  "ds_read_b64_tr_b16 v[182:183], v228 offset:6656\n\t" \
  "v_add_f32 v234, v234, v18\n\t" \
  "v_exp_f32 v19, v19\n\t" \
  "s_nop 0\n\t" \
  "ds_read_b64_tr_b16 v[184:185], v228 offset:5120\n\t" \
  "v_add_f32 v235, v235, v19\n\t" \
  "v_cvt_pk_bf16_f32 v17, v18, v19\n\t" \
  "v_exp_f32 v20, v20\n\t" \
  "v_exp_f32 v21, v21\n\t" \
  "s_nop 0\n\t" \
  "ds_read_b64_tr_b16 v[186:187], v228 offset:7168\n\t" \
  "v_add_f32 v234, v234, v20\n\t" \
  "v_add_f32 v235, v235, v21\n\t" \
  "v_cvt_pk_bf16_f32 v18, v20, v21\n\t" \
  "v_exp_f32 v22, v22\n\t" \
  "s_nop 0\n\t" \
  "ds_read_b64_tr_b16 v[188:189], v228 offset:5632\n\t" \
  "v_add_f32 v234, v234, v22\n\t" \
  "v_exp_f32 v23, v23\n\t" \
  "s_nop 0\n\t" \
  "ds_read_b64_tr_b16 v[190:191], v228 offset:7680\n\t" \
  "v_add_f32 v235, v235, v23\n\t" \
  "v_cvt_pk_bf16_f32 v19, v22, v23\n\t" \
  "v_exp_f32 v24, v24\n\t" \
  "s_nop 0\n\t" \
  "s_waitcnt lgkmcnt(8)\n\t" \
  "s_mov_b32 %[sc], 16\n\t" \
  "s_branch LMID_%=\n\t" \
  "LLOOP_%=:\n\t" \
  "v_mfma_f32_32x32x16_bf16 v[0:15], a[192:195], a[128:131], v[128:143]\n\t" \
  "ds_read_b64_tr_b16 v[192:193], v228 offset:24576\n\t" \
  "v_add_f32 v234, v234, v88\n\t" \
  "v_exp_f32 v89, v89\n\t" \
  "v_exp_f32 v90, v90\n\t" \
  "v_mfma_f32_32x32x16_bf16 v[16:31], a[192:195], a[160:163], v[144:159]\n\t" \
  "ds_read_b64_tr_b16 v[194:195], v228 offset:26624\n\t" \
  "v_add_f32 v235, v235, v89\n\t" \
  "v_add_f32 v234, v234, v90\n\t" \
  "v_cvt_pk_bf16_f32 v88, v88, v89\n\t" \
  "v_exp_f32 v91, v91\n\t" \
  "v_mfma_f32_32x32x16_bf16 v[32:47], a[224:227], a[128:131], v[128:143]\n\t" \
  "ds_read_b64_tr_b16 v[196:197], v228 offset:25088\n\t" \
  "v_add_f32 v235, v235, v91\n\t" \
  "v_cvt_pk_bf16_f32 v89, v90, v91\n\t" \
  "v_exp_f32 v92, v92\n\t" \
  "v_mfma_f32_32x32x16_bf16 v[48:63], a[224:227], a[160:163], v[144:159]\n\t" \
  "ds_read_b64_tr_b16 v[198:199], v228 offset:27136\n\t" \
  "v_add_f32 v234, v234, v92\n\t" \
  "v_exp_f32 v93, v93\n\t" \
  "v_exp_f32 v94, v94\n\t" \
  "v_mfma_f32_32x32x16_bf16 v[0:15], a[196:199], a[132:135], v[0:15]\n\t" \
  "ds_read_b64_tr_b16 v[200:201], v228 offset:25600\n\t" \
  "v_add_f32 v235, v235, v93\n\t" \
  "v_add_f32 v234, v234, v94\n\t" \
  "v_cvt_pk_bf16_f32 v90, v92, v93\n\t" \
  "v_exp_f32 v95, v95\n\t" \
  "v_mfma_f32_32x32x16_bf16 v[16:31], a[196:199], a[164:167], v[16:31]\n\t" \
  "ds_read_b64_tr_b16 v[202:203], v228 offset:27648\n\t" \
  "v_add_f32 v235, v235, v95\n\t" \
  "v_cvt_pk_bf16_f32 v91, v94, v95\n\t" \
  "v_exp_f32 v96, v96\n\t" \
  "v_mfma_f32_32x32x16_bf16 v[32:47], a[228:231], a[132:135], v[32:47]\n\t" \
  "ds_read_b64_tr_b16 v[204:205], v228 offset:26112\n\t" \
  "v_add_f32 v232, v232, v96\n\t" \
  "v_exp_f32 v97, v97\n\t" \
  "v_exp_f32 v98, v98\n\t" \
  "v_mfma_f32_32x32x16_bf16 v[48:63], a[228:231], a[164:167], v[48:63]\n\t" \
  "ds_read_b64_tr_b16 v[206:207], v228 offset:28160\n\t" \
  "v_add_f32 v233, v233, v97\n\t" \
  "v_add_f32 v232, v232, v98\n\t" \
  "v_cvt_pk_bf16_f32 v96, v96, v97\n\t" \
  "v_exp_f32 v99, v99\n\t" \
  "v_mfma_f32_32x32x16_bf16 v[0:15], a[200:203], a[136:139], v[0:15]\n\t" \
  "ds_read_b64_tr_b16 v[208:209], v228 offset:28672\n\t" \
  "v_add_f32 v233, v233, v99\n\t" \
  "v_cvt_pk_bf16_f32 v97, v98, v99\n\t" \
  "v_exp_f32 v100, v100\n\t" \
  "v_mfma_f32_32x32x16_bf16 v[16:31], a[200:203], a[168:171], v[16:31]\n\t" \
  "ds_read_b64_tr_b16 v[210:211], v228 offset:30720\n\t" \
  "v_add_f32 v232, v232, v100\n\t" \
  "v_exp_f32 v101, v101\n\t" \
  "v_mfma_f32_32x32x16_bf16 v[32:47], a[232:235], a[136:139], v[32:47]\n\t" \
  "ds_read_b64_tr_b16 v[212:213], v228 offset:29184\n\t" \
  "v_add_f32 v233, v233, v101\n\t" \
  "v_cvt_pk_bf16_f32 v98, v100, v101\n\t" \
  "v_exp_f32 v102, v102\n\t" \
  "v_exp_f32 v103, v103\n\t" \
  "v_mfma_f32_32x32x16_bf16 v[48:63], a[232:235], a[168:171], v[48:63]\n\t" \
  "ds_read_b64_tr_b16 v[214:215], v228 offset:31232\n\t" \
  "v_add_f32 v232, v232, v102\n\t" \
  "v_add_f32 v233, v233, v103\n\t" \
  "v_cvt_pk_bf16_f32 v99, v102, v103\n\t" \
  "v_exp_f32 v104, v104\n\t" \
  "v_mfma_f32_32x32x16_bf16 v[0:15], a[204:207], a[140:143], v[0:15]\n\t" \
  "ds_read_b64_tr_b16 v[216:217], v228 offset:29696\n\t" \
  "v_add_f32 v232, v232, v104\n\t" \
  "v_exp_f32 v105, v105\n\t" \
  "v_mfma_f32_32x32x16_bf16 v[16:31], a[204:207], a[172:175], v[16:31]\n\t" \
  "ds_read_b64_tr_b16 v[218:219], v228 offset:31744\n\t" \
  "v_add_f32 v233, v233, v105\n\t" \
  "v_cvt_pk_bf16_f32 v104, v104, v105\n\t" \
  "v_exp_f32 v106, v106\n\t" \
  "v_exp_f32 v107, v107\n\t" \
  "v_mfma_f32_32x32x16_bf16 v[32:47], a[236:239], a[140:143], v[32:47]\n\t" \
  "ds_read_b64_tr_b16 v[220:221], v228 offset:30208\n\t" \
  "v_add_f32 v232, v232, v106\n\t" \
  "v_add_f32 v233, v233, v107\n\t" \
  "v_cvt_pk_bf16_f32 v105, v106, v107\n\t" \
  "v_exp_f32 v108, v108\n\t" \
  "v_mfma_f32_32x32x16_bf16 v[48:63], a[236:239], a[172:175], v[48:63]\n\t" \
  "ds_read_b64_tr_b16 v[222:223], v228 offset:32256\n\t" \
  "v_add_f32 v232, v232, v108\n\t" \
  "v_exp_f32 v109, v109\n\t" \
  "v_mfma_f32_32x32x16_bf16 v[0:15], a[208:211], a[144:147], v[0:15]\n\t" \
  "v_add_f32 v233, v233, v109\n\t" \
  "v_cvt_pk_bf16_f32 v106, v108, v109\n\t" \
  "v_exp_f32 v110, v110\n\t" \
  "v_exp_f32 v111, v111\n\t" \
  "v_mfma_f32_32x32x16_bf16 v[16:31], a[208:211], a[176:179], v[16:31]\n\t" \
  "v_add_f32 v232, v232, v110\n\t" \
  "v_add_f32 v233, v233, v111\n\t" \
  "v_cvt_pk_bf16_f32 v107, v110, v111\n\t" \
  "v_exp_f32 v112, v112\n\t" \
  "v_mfma_f32_32x32x16_bf16 v[32:47], a[240:243], a[144:147], v[32:47]\n\t" \
  "v_add_f32 v234, v234, v112\n\t" \
  "v_exp_f32 v113, v113\n\t" \
  "v_mfma_f32_32x32x16_bf16 v[48:63], a[240:243], a[176:179], v[48:63]\n\t" \
  "v_add_f32 v235, v235, v113\n\t" \
  "v_cvt_pk_bf16_f32 v112, v112, v113\n\t" \
  "v_exp_f32 v114, v114\n\t" \
  "v_mfma_f32_32x32x16_bf16 v[0:15], a[212:215], a[148:151], v[0:15]\n\t" \
  "v_add_f32 v234, v234, v114\n\t" \
  "v_exp_f32 v115, v115\n\t" \
  "v_exp_f32 v116, v116\n\t" \
  "v_mfma_f32_32x32x16_bf16 v[16:31], a[212:215], a[180:183], v[16:31]\n\t" \
  "v_add_f32 v235, v235, v115\n\t" \
  "v_add_f32 v234, v234, v116\n\t" \
  "v_cvt_pk_bf16_f32 v113, v114, v115\n\t" \
  "v_exp_f32 v117, v117\n\t" \
  "v_mfma_f32_32x32x16_bf16 v[32:47], a[244:247], a[148:151], v[32:47]\n\t" \
  "v_add_f32 v235, v235, v117\n\t" \
  "v_cvt_pk_bf16_f32 v114, v116, v117\n\t" \
  "v_exp_f32 v118, v118\n\t" \
  "v_mfma_f32_32x32x16_bf16 v[48:63], a[244:247], a[180:183], v[48:63]\n\t" \
  "v_add_f32 v234, v234, v118\n\t" \
  "v_exp_f32 v119, v119\n\t" \
  "v_exp_f32 v120, v120\n\t" \
  "v_mfma_f32_32x32x16_bf16 v[0:15], a[216:219], a[152:155], v[0:15]\n\t" \
  "v_add_f32 v235, v235, v119\n\t" \
  "v_add_f32 v234, v234, v120\n\t" \
  "v_cvt_pk_bf16_f32 v115, v118, v119\n\t" \
  "v_exp_f32 v121, v121\n\t" \
  "v_mfma_f32_32x32x16_bf16 v[16:31], a[216:219], a[184:187], v[16:31]\n\t" \
  "v_add_f32 v235, v235, v121\n\t" \
  "v_cvt_pk_bf16_f32 v120, v120, v121\n\t" \
  "v_exp_f32 v122, v122\n\t" \
  "v_mfma_f32_32x32x16_bf16 v[32:47], a[248:251], a[152:155], v[32:47]\n\t" \
  "v_add_f32 v234, v234, v122\n\t" \
  "v_exp_f32 v123, v123\n\t" \
  "v_exp_f32 v124, v124\n\t" \
  "v_mfma_f32_32x32x16_bf16 v[48:63], a[248:251], a[184:187], v[48:63]\n\t" \
  "v_add_f32 v235, v235, v123\n\t" \
  "v_add_f32 v234, v234, v124\n\t" \
  "v_cvt_pk_bf16_f32 v121, v122, v123\n\t" \
  "v_exp_f32 v125, v125\n\t" \
  "v_mfma_f32_32x32x16_bf16 v[0:15], a[220:223], a[156:159], v[0:15]\n\t" \
  "v_add_f32 v235, v235, v125\n\t" \
  "v_cvt_pk_bf16_f32 v122, v124, v125\n\t" \
  "v_exp_f32 v126, v126\n\t" \
  "v_mfma_f32_32x32x16_bf16 v[16:31], a[220:223], a[188:191], v[16:31]\n\t" \
  "v_add_f32 v234, v234, v126\n\t" \
  "v_exp_f32 v127, v127\n\t" \
  "v_mfma_f32_32x32x16_bf16 v[32:47], a[252:255], a[156:159], v[32:47]\n\t" \
  "v_add_f32 v235, v235, v127\n\t" \
  "v_cvt_pk_bf16_f32 v123, v126, v127\n\t" \
  "v_mfma_f32_32x32x16_bf16 v[48:63], a[252:255], a[188:191], v[48:63]\n\t" \
  "s_waitcnt vmcnt(0) lgkmcnt(0)\n\t" \
  "s_barrier\n\t" \
  "v_mfma_f32_32x32x16_bf16 a[0:15], v[160:163], v[64:67], a[0:15]\n\t" \
  "s_add_u32 m0, %[m0k], 0\n\t" \
  "ds_read_b128 a[192:195], v224 offset:16384\n\t" \
  "v_max3_f32 v238, v0, v1, v2\n\t" \
  "v_max3_f32 v240, v16, v17, v18\n\t" \
  "v_max3_f32 v238, v238, v3, v4\n\t" \
  "v_mfma_f32_32x32x16_bf16 a[16:31], v[160:163], v[80:83], a[16:31]\n\t" \
  "ds_read_b128 a[196:199], v225 offset:16384\n\t" \
  "buffer_load_dwordx4 v229, %[srdk], %[sk] offen offset:0 lds\n\t" \
  "v_max3_f32 v240, v240, v19, v20\n\t" \
  "v_max3_f32 v239, v33, v34, v35\n\t" \
  "v_max3_f32 v241, v49, v50, v51\n\t" \
  "v_mfma_f32_32x32x16_bf16 a[32:47], v[164:167], v[64:67], a[32:47]\n\t" \
  "ds_read_b128 a[200:203], v226 offset:16384\n\t" \
  "v_max3_f32 v238, v238, v5, v6\n\t" \
  "v_max3_f32 v240, v240, v21, v22\n\t" \
  "v_max3_f32 v239, v239, v36, v37\n\t" \
  "v_max3_f32 v241, v241, v52, v53\n\t" \
  "v_mfma_f32_32x32x16_bf16 a[48:63], v[164:167], v[80:83], a[48:63]\n\t" \
  "ds_read_b128 a[204:207], v227 offset:16384\n\t" \
  "buffer_load_dwordx4 v230, %[srdk], %[sk] offen offset:1024 lds\n\t" \
  "v_max3_f32 v238, v238, v7, v8\n\t" \
  "v_max3_f32 v240, v240, v23, v24\n\t" \
  "v_max3_f32 v239, v239, v38, v39\n\t" \
  "v_mfma_f32_32x32x16_bf16 a[64:79], v[168:171], v[64:67], a[64:79]\n\t" \
  "ds_read_b128 a[208:211], v224 offset:16512\n\t" \
  "v_max3_f32 v241, v241, v54, v55\n\t" \
  "v_max3_f32 v238, v238, v9, v10\n\t" \
  "v_max3_f32 v240, v240, v25, v26\n\t" \
  "v_mfma_f32_32x32x16_bf16 a[80:95], v[168:171], v[80:83], a[80:95]\n\t" \
  "ds_read_b128 a[212:215], v225 offset:16512\n\t" \
  "buffer_load_dwordx4 v229, %[srdk], %[sk] offen offset:2048 lds\n\t" \
  "v_max3_f32 v239, v239, v40, v41\n\t" \
  "v_max3_f32 v241, v241, v56, v57\n\t" \
  "v_max3_f32 v238, v238, v11, v12\n\t" \
  "v_max3_f32 v240, v240, v27, v28\n\t" \
  "v_mfma_f32_32x32x16_bf16 a[96:111], v[172:175], v[64:67], a[96:111]\n\t" \
  "ds_read_b128 a[216:219], v226 offset:16512\n\t" \
  "v_max3_f32 v239, v239, v42, v43\n\t" \
  "v_max3_f32 v241, v241, v58, v59\n\t" \
  "v_max3_f32 v238, v238, v13, v14\n\t" \
  "v_mfma_f32_32x32x16_bf16 a[112:127], v[172:175], v[80:83], a[112:127]\n\t" \
  "ds_read_b128 a[220:223], v227 offset:16512\n\t" \
  "buffer_load_dwordx4 v230, %[srdk], %[sk] offen offset:3072 lds\n\t" \
  "v_max3_f32 v240, v240, v29, v30\n\t" \
  "v_max3_f32 v239, v239, v44, v45\n\t" \
  "v_max3_f32 v241, v241, v60, v61\n\t" \
  "v_mfma_f32_32x32x16_bf16 a[0:15], v[176:179], v[72:75], a[0:15]\n\t" \
  "s_add_u32 m0, %[m0v], 16384\n\t" \
  "ds_read_b128 a[224:227], v224 offset:24576\n\t" \
  "v_max3_f32 v238, v238, v15, v32\n\t" \
  "v_max3_f32 v240, v240, v31, v48\n\t" \
  "v_max3_f32 v239, v239, v46, v47\n\t" \
  "v_max3_f32 v241, v241, v62, v63\n\t" \
  "v_mfma_f32_32x32x16_bf16 a[16:31], v[176:179], v[88:91], a[16:31]\n\t" \
  "ds_read_b128 a[228:231], v225 offset:24576\n\t" \
  "buffer_load_dwordx4 v231, %[srdv], %[sv] offen offset:0 lds\n\t" \
  "v_max_f32 v242, v238, v239\n\t" \
  "v_max_f32 v243, v240, v241\n\t" \
  "v_mov_b32 v246, v242\n\t" \
  "v_mov_b32 v247, v243\n\t" \
  "v_mfma_f32_32x32x16_bf16 a[32:47], v[180:183], v[72:75], a[32:47]\n\t" \
  "ds_read_b128 a[232:235], v226 offset:24576\n\t" \
  "v_permlane32_swap_b32 v242, v246\n\t" \
  "v_permlane32_swap_b32 v243, v247\n\t" \
  "v_max_f32 v242, v242, v246\n\t" \
  "v_max_f32 v243, v243, v247\n\t" \
  "v_mfma_f32_32x32x16_bf16 a[48:63], v[180:183], v[88:91], a[48:63]\n\t" \
  "ds_read_b128 a[236:239], v227 offset:24576\n\t" \
  "buffer_load_dwordx4 v231, %[srdv], %[sv2] offen offset:1024 lds\n\t" \
  "v_max_f32 v248, v242, v243\n\t" \
  "v_cmp_lt_f32 vcc, %[sthr], v248\n\t" \
  "s_cbranch_vccnz LRA0_%=\n\t" \
  "LRAB0_%=:\n\t" \
  "v_mfma_f32_32x32x16_bf16 a[64:79], v[184:187], v[72:75], a[64:79]\n\t" \
  "ds_read_b128 a[240:243], v224 offset:24704\n\t" \
  "v_exp_f32 v0, v0\n\t" \
  "v_exp_f32 v1, v1\n\t" \
  "v_mfma_f32_32x32x16_bf16 a[80:95], v[184:187], v[88:91], a[80:95]\n\t" \
  "ds_read_b128 a[244:247], v225 offset:24704\n\t" \
  "buffer_load_dwordx4 v231, %[srdv], %[sv] offen offset:2048 lds\n\t" \
  "v_add_f32 v232, v232, v0\n\t" \
  "v_add_f32 v233, v233, v1\n\t" \
  "v_cvt_pk_bf16_f32 v0, v0, v1\n\t" \
  "v_exp_f32 v2, v2\n\t" \
  "v_mfma_f32_32x32x16_bf16 a[96:111], v[188:191], v[72:75], a[96:111]\n\t" \
  "ds_read_b128 a[248:251], v226 offset:24704\n\t" \
  "v_add_f32 v232, v232, v2\n\t" \
  "v_exp_f32 v3, v3\n\t" \
  "v_mfma_f32_32x32x16_bf16 a[112:127], v[188:191], v[88:91], a[112:127]\n\t" \
  "ds_read_b128 a[252:255], v227 offset:24704\n\t" \
  "buffer_load_dwordx4 v231, %[srdv], %[sv2] offen offset:3072 lds\n\t" \
  "v_add_f32 v233, v233, v3\n\t" \
  "v_cvt_pk_bf16_f32 v1, v2, v3\n\t" \
  "v_exp_f32 v4, v4\n\t" \
  "v_mfma_f32_32x32x16_bf16 a[0:15], v[192:195], v[96:99], a[0:15]\n\t" \
  "ds_read_b64_tr_b16 v[160:161], v228 offset:0\n\t" \
  "s_add_u32 %[sk], %[sk], 16384\n\t" \
  "v_add_f32 v232, v232, v4\n\t" \
  "v_exp_f32 v5, v5\n\t" \
  "v_exp_f32 v6, v6\n\t" \
  "v_mfma_f32_32x32x16_bf16 a[16:31], v[192:195], v[112:115], a[16:31]\n\t" \
  "ds_read_b64_tr_b16 v[162:163], v228 offset:2048\n\t" \
  "s_add_u32 %[sv], %[sv], 16384\n\t" \
  "v_add_f32 v233, v233, v5\n\t" \
  "v_add_f32 v232, v232, v6\n\t" \
  "v_cvt_pk_bf16_f32 v2, v4, v5\n\t" \
  "v_exp_f32 v7, v7\n\t" \
  "v_mfma_f32_32x32x16_bf16 a[32:47], v[196:199], v[96:99], a[32:47]\n\t" \
  "ds_read_b64_tr_b16 v[164:165], v228 offset:512\n\t" \
  "s_add_u32 %[sv2], %[sv2], 16384\n\t" \
  "v_add_f32 v233, v233, v7\n\t" \
  "v_cvt_pk_bf16_f32 v3, v6, v7\n\t" \
  "v_exp_f32 v8, v8\n\t" \
  "v_mfma_f32_32x32x16_bf16 a[48:63], v[196:199], v[112:115], a[48:63]\n\t" \
  "ds_read_b64_tr_b16 v[166:167], v228 offset:2560\n\t" \
  "v_add_f32 v232, v232, v8\n\t" \
  "v_exp_f32 v9, v9\n\t" \
  "v_mfma_f32_32x32x16_bf16 a[64:79], v[200:203], v[96:99], a[64:79]\n\t" \
  "ds_read_b64_tr_b16 v[168:169], v228 offset:1024\n\t" \
  "v_add_f32 v233, v233, v9\n\t" \
  "v_cvt_pk_bf16_f32 v8, v8, v9\n\t" \
  "v_exp_f32 v10, v10\n\t" \
  "v_exp_f32 v11, v11\n\t" \
  "v_mfma_f32_32x32x16_bf16 a[80:95], v[200:203], v[112:115], a[80:95]\n\t" \
  "ds_read_b64_tr_b16 v[170:171], v228 offset:3072\n\t" \
  "v_add_f32 v232, v232, v10\n\t" \
  "v_add_f32 v233, v233, v11\n\t" \
  "v_cvt_pk_bf16_f32 v9, v10, v11\n\t" \
  "v_exp_f32 v12, v12\n\t" \
  "v_mfma_f32_32x32x16_bf16 a[96:111], v[204:207], v[96:99], a[96:111]\n\t" \
  "ds_read_b64_tr_b16 v[172:173], v228 offset:1536\n\t" \
  "v_add_f32 v232, v232, v12\n\t" \
  "v_exp_f32 v13, v13\n\t" \
  "v_mfma_f32_32x32x16_bf16 a[112:127], v[204:207], v[112:115], a[112:127]\n\t" \
  "ds_read_b64_tr_b16 v[174:175], v228 offset:3584\n\t" \
  "v_add_f32 v233, v233, v13\n\t" \
  "v_cvt_pk_bf16_f32 v10, v12, v13\n\t" \
  "v_exp_f32 v14, v14\n\t" \
  "v_mfma_f32_32x32x16_bf16 a[0:15], v[208:211], v[104:107], a[0:15]\n\t" \
  "ds_read_b64_tr_b16 v[176:177], v228 offset:4096\n\t" \
  "v_add_f32 v232, v232, v14\n\t" \
  "v_exp_f32 v15, v15\n\t" \
  "v_exp_f32 v16, v16\n\t" \
  "v_mfma_f32_32x32x16_bf16 a[16:31], v[208:211], v[120:123], a[16:31]\n\t" \
  "ds_read_b64_tr_b16 v[178:179], v228 offset:6144\n\t" \
  "v_add_f32 v233, v233, v15\n\t" \
  "v_add_f32 v234, v234, v16\n\t" \
  "v_cvt_pk_bf16_f32 v11, v14, v15\n\t" \
  "v_exp_f32 v17, v17\n\t" \
  "v_mfma_f32_32x32x16_bf16 a[32:47], v[212:215], v[104:107], a[32:47]\n\t" \
  "ds_read_b64_tr_b16 v[180:181], v228 offset:4608\n\t" \
  "v_add_f32 v235, v235, v17\n\t" \
  "v_cvt_pk_bf16_f32 v16, v16, v17\n\t" \
  "v_exp_f32 v18, v18\n\t" \
  "v_mfma_f32_32x32x16_bf16 a[48:63], v[212:215], v[120:123], a[48:63]\n\t" \
  "ds_read_b64_tr_b16 v[182:183], v228 offset:6656\n\t" \
  "v_add_f32 v234, v234, v18\n\t" \
  "v_exp_f32 v19, v19\n\t" \
  "v_mfma_f32_32x32x16_bf16 a[64:79], v[216:219], v[104:107], a[64:79]\n\t" \
  "ds_read_b64_tr_b16 v[184:185], v228 offset:5120\n\t" \
  "v_add_f32 v235, v235, v19\n\t" \
  "v_cvt_pk_bf16_f32 v17, v18, v19\n\t" \
  "v_exp_f32 v20, v20\n\t" \
  "v_exp_f32 v21, v21\n\t" \
  "v_mfma_f32_32x32x16_bf16 a[80:95], v[216:219], v[120:123], a[80:95]\n\t" \
  "ds_read_b64_tr_b16 v[186:187], v228 offset:7168\n\t" \
  "v_add_f32 v234, v234, v20\n\t" \
  "v_add_f32 v235, v235, v21\n\t" \
  "v_cvt_pk_bf16_f32 v18, v20, v21\n\t" \
  "v_exp_f32 v22, v22\n\t" \
  "v_mfma_f32_32x32x16_bf16 a[96:111], v[220:223], v[104:107], a[96:111]\n\t" \
  "ds_read_b64_tr_b16 v[188:189], v228 offset:5632\n\t" \
  "v_add_f32 v234, v234, v22\n\t" \
  "v_exp_f32 v23, v23\n\t" \
  "v_mfma_f32_32x32x16_bf16 a[112:127], v[220:223], v[120:123], a[112:127]\n\t" \
  "ds_read_b64_tr_b16 v[190:191], v228 offset:7680\n\t" \
  "v_add_f32 v235, v235, v23\n\t" \
  "v_cvt_pk_bf16_f32 v19, v22, v23\n\t" \
  "v_exp_f32 v24, v24\n\t" \
  "s_cmp_lg_u32 %[sp], 0\n\t" \
  "s_cbranch_scc1 LRB0_%=\n\t" \
  "LRBB0_%=:\n\t" \
  "s_waitcnt lgkmcnt(8)\n\t" \
  "LMID_%=:\n\t" \
  "v_mfma_f32_32x32x16_bf16 v[64:79], a[192:195], a[128:131], v[128:143]\n\t" \
  "ds_read_b64_tr_b16 v[192:193], v228 offset:8192\n\t" \
  "v_add_f32 v234, v234, v24\n\t" \
  "v_exp_f32 v25, v25\n\t" \
  "v_exp_f32 v26, v26\n\t" \
  "v_mfma_f32_32x32x16_bf16 v[80:95], a[192:195], a[160:163], v[144:159]\n\t" \
  "ds_read_b64_tr_b16 v[194:195], v228 offset:10240\n\t" \
  "v_add_f32 v235, v235, v25\n\t" \
  "v_add_f32 v234, v234, v26\n\t" \
  "v_cvt_pk_bf16_f32 v24, v24, v25\n\t" \
  "v_exp_f32 v27, v27\n\t" \
  "v_mfma_f32_32x32x16_bf16 v[96:111], a[224:227], a[128:131], v[128:143]\n\t" \
  "ds_read_b64_tr_b16 v[196:197], v228 offset:8704\n\t" \
  "v_add_f32 v235, v235, v27\n\t" \
  "v_cvt_pk_bf16_f32 v25, v26, v27\n\t" \
  "v_exp_f32 v28, v28\n\t" \
  "v_mfma_f32_32x32x16_bf16 v[112:127], a[224:227], a[160:163], v[144:159]\n\t" \
  "ds_read_b64_tr_b16 v[198:199], v228 offset:10752\n\t" \
  "v_add_f32 v234, v234, v28\n\t" \
  "v_exp_f32 v29, v29\n\t" \
  "v_exp_f32 v30, v30\n\t" \
  "v_mfma_f32_32x32x16_bf16 v[64:79], a[196:199], a[132:135], v[64:79]\n\t" \
  "ds_read_b64_tr_b16 v[200:201], v228 offset:9216\n\t" \
  "v_add_f32 v235, v235, v29\n\t" \
  "v_add_f32 v234, v234, v30\n\t" \
  "v_cvt_pk_bf16_f32 v26, v28, v29\n\t" \
  "v_exp_f32 v31, v31\n\t" \
  "v_mfma_f32_32x32x16_bf16 v[80:95], a[196:199], a[164:167], v[80:95]\n\t" \
  "ds_read_b64_tr_b16 v[202:203], v228 offset:11264\n\t" \
  "v_add_f32 v235, v235, v31\n\t" \
  "v_cvt_pk_bf16_f32 v27, v30, v31\n\t" \
  "v_exp_f32 v32, v32\n\t" \
  "v_mfma_f32_32x32x16_bf16 v[96:111], a[228:231], a[132:135], v[96:111]\n\t" \
  "ds_read_b64_tr_b16 v[204:205], v228 offset:9728\n\t" \
  "v_add_f32 v232, v232, v32\n\t" \
  "v_exp_f32 v33, v33\n\t" \
  "v_exp_f32 v34, v34\n\t" \
  "v_mfma_f32_32x32x16_bf16 v[112:127], a[228:231], a[164:167], v[112:127]\n\t" \
  "ds_read_b64_tr_b16 v[206:207], v228 offset:11776\n\t" \
  "v_add_f32 v233, v233, v33\n\t" \
  "v_add_f32 v232, v232, v34\n\t" \
  "v_cvt_pk_bf16_f32 v32, v32, v33\n\t" \
  "v_exp_f32 v35, v35\n\t" \
  "v_mfma_f32_32x32x16_bf16 v[64:79], a[200:203], a[136:139], v[64:79]\n\t" \
  "ds_read_b64_tr_b16 v[208:209], v228 offset:12288\n\t" \
  "v_add_f32 v233, v233, v35\n\t" \
  "v_cvt_pk_bf16_f32 v33, v34, v35\n\t" \
  "v_exp_f32 v36, v36\n\t" \
  "v_mfma_f32_32x32x16_bf16 v[80:95], a[200:203], a[168:171], v[80:95]\n\t" \
  "ds_read_b64_tr_b16 v[210:211], v228 offset:14336\n\t" \
  "v_add_f32 v232, v232, v36\n\t" \
  "v_exp_f32 v37, v37\n\t" \
  "v_mfma_f32_32x32x16_bf16 v[96:111], a[232:235], a[136:139], v[96:111]\n\t" \
  "ds_read_b64_tr_b16 v[212:213], v228 offset:12800\n\t" \
  "v_add_f32 v233, v233, v37\n\t" \
  "v_cvt_pk_bf16_f32 v34, v36, v37\n\t" \
  "v_exp_f32 v38, v38\n\t" \
  "v_exp_f32 v39, v39\n\t" \
  "v_mfma_f32_32x32x16_bf16 v[112:127], a[232:235], a[168:171], v[112:127]\n\t" \
  "ds_read_b64_tr_b16 v[214:215], v228 offset:14848\n\t" \
  "v_add_f32 v232, v232, v38\n\t" \
  "v_add_f32 v233, v233, v39\n\t" \
  "v_cvt_pk_bf16_f32 v35, v38, v39\n\t" \
  "v_exp_f32 v40, v40\n\t" \
  "v_mfma_f32_32x32x16_bf16 v[64:79], a[204:207], a[140:143], v[64:79]\n\t" \
  "ds_read_b64_tr_b16 v[216:217], v228 offset:13312\n\t" \
  "v_add_f32 v232, v232, v40\n\t" \
  "v_exp_f32 v41, v41\n\t" \
  "v_mfma_f32_32x32x16_bf16 v[80:95], a[204:207], a[172:175], v[80:95]\n\t" \
  "ds_read_b64_tr_b16 v[218:219], v228 offset:15360\n\t" \
  "v_add_f32 v233, v233, v41\n\t" \
  "v_cvt_pk_bf16_f32 v40, v40, v41\n\t" \
  "v_exp_f32 v42, v42\n\t" \
  "v_exp_f32 v43, v43\n\t" \
  "v_mfma_f32_32x32x16_bf16 v[96:111], a[236:239], a[140:143], v[96:111]\n\t" \
  "ds_read_b64_tr_b16 v[220:221], v228 offset:13824\n\t" \
  "v_add_f32 v232, v232, v42\n\t" \
  "v_add_f32 v233, v233, v43\n\t" \
  "v_cvt_pk_bf16_f32 v41, v42, v43\n\t" \
  "v_exp_f32 v44, v44\n\t" \
  "v_mfma_f32_32x32x16_bf16 v[112:127], a[236:239], a[172:175], v[112:127]\n\t" \
  "ds_read_b64_tr_b16 v[222:223], v228 offset:15872\n\t" \
  "v_add_f32 v232, v232, v44\n\t" \
  "v_exp_f32 v45, v45\n\t" \
  "v_mfma_f32_32x32x16_bf16 v[64:79], a[208:211], a[144:147], v[64:79]\n\t" \
  "v_add_f32 v233, v233, v45\n\t" \
  "v_cvt_pk_bf16_f32 v42, v44, v45\n\t" \
  "v_exp_f32 v46, v46\n\t" \
  "v_exp_f32 v47, v47\n\t" \
  "v_mfma_f32_32x32x16_bf16 v[80:95], a[208:211], a[176:179], v[80:95]\n\t" \
  "v_add_f32 v232, v232, v46\n\t" \
  "v_add_f32 v233, v233, v47\n\t" \
  "v_cvt_pk_bf16_f32 v43, v46, v47\n\t" \
  "v_exp_f32 v48, v48\n\t" \
  "v_mfma_f32_32x32x16_bf16 v[96:111], a[240:243], a[144:147], v[96:111]\n\t" \
  "v_add_f32 v234, v234, v48\n\t" \
  "v_exp_f32 v49, v49\n\t" \
  "v_mfma_f32_32x32x16_bf16 v[112:127], a[240:243], a[176:179], v[112:127]\n\t" \
  "v_add_f32 v235, v235, v49\n\t" \
  "v_cvt_pk_bf16_f32 v48, v48, v49\n\t" \
  "v_exp_f32 v50, v50\n\t" \
  "v_mfma_f32_32x32x16_bf16 v[64:79], a[212:215], a[148:151], v[64:79]\n\t" \
  "v_add_f32 v234, v234, v50\n\t" \
  "v_exp_f32 v51, v51\n\t" \
  "v_exp_f32 v52, v52\n\t" \
  "v_mfma_f32_32x32x16_bf16 v[80:95], a[212:215], a[180:183], v[80:95]\n\t" \
  "v_add_f32 v235, v235, v51\n\t" \
  "v_add_f32 v234, v234, v52\n\t" \
  "v_cvt_pk_bf16_f32 v49, v50, v51\n\t" \
  "v_exp_f32 v53, v53\n\t" \
  "v_mfma_f32_32x32x16_bf16 v[96:111], a[244:247], a[148:151], v[96:111]\n\t" \
  "v_add_f32 v235, v235, v53\n\t" \
  "v_cvt_pk_bf16_f32 v50, v52, v53\n\t" \
  "v_exp_f32 v54, v54\n\t" \
  "v_mfma_f32_32x32x16_bf16 v[112:127], a[244:247], a[180:183], v[112:127]\n\t" \
  "v_add_f32 v234, v234, v54\n\t" \
  "v_exp_f32 v55, v55\n\t" \
  "v_exp_f32 v56, v56\n\t" \
  "v_mfma_f32_32x32x16_bf16 v[64:79], a[216:219], a[152:155], v[64:79]\n\t" \
  "v_add_f32 v235, v235, v55\n\t" \
  "v_add_f32 v234, v234, v56\n\t" \
  "v_cvt_pk_bf16_f32 v51, v54, v55\n\t" \
  "v_exp_f32 v57, v57\n\t" \
  "v_mfma_f32_32x32x16_bf16 v[80:95], a[216:219], a[184:187], v[80:95]\n\t" \
  "v_add_f32 v235, v235, v57\n\t" \
  "v_cvt_pk_bf16_f32 v56, v56, v57\n\t" \
  "v_exp_f32 v58, v58\n\t" \
  "v_mfma_f32_32x32x16_bf16 v[96:111], a[248:251], a[152:155], v[96:111]\n\t" \
  "v_add_f32 v234, v234, v58\n\t" \
  "v_exp_f32 v59, v59\n\t" \
  "v_exp_f32 v60, v60\n\t" \
  "v_mfma_f32_32x32x16_bf16 v[112:127], a[248:251], a[184:187], v[112:127]\n\t" \
  "v_add_f32 v235, v235, v59\n\t" \
  "v_add_f32 v234, v234, v60\n\t" \
  "v_cvt_pk_bf16_f32 v57, v58, v59\n\t" \
  "v_exp_f32 v61, v61\n\t" \
  "v_mfma_f32_32x32x16_bf16 v[64:79], a[220:223], a[156:159], v[64:79]\n\t" \
  "v_add_f32 v235, v235, v61\n\t" \
  "v_cvt_pk_bf16_f32 v58, v60, v61\n\t" \
  "v_exp_f32 v62, v62\n\t" \
  "v_mfma_f32_32x32x16_bf16 v[80:95], a[220:223], a[188:191], v[80:95]\n\t" \
  "v_add_f32 v234, v234, v62\n\t" \
  "v_exp_f32 v63, v63\n\t" \
  "v_mfma_f32_32x32x16_bf16 v[96:111], a[252:255], a[156:159], v[96:111]\n\t" \
  "v_add_f32 v235, v235, v63\n\t" \
  "v_cvt_pk_bf16_f32 v59, v62, v63\n\t" \
  "v_mfma_f32_32x32x16_bf16 v[112:127], a[252:255], a[188:191], v[112:127]\n\t" \
  "s_waitcnt vmcnt(0) lgkmcnt(0)\n\t" \
  "s_barrier\n\t" \
  "v_mfma_f32_32x32x16_bf16 a[0:15], v[160:163], v[0:3], a[0:15]\n\t" \
  "s_add_u32 m0, %[m0k], 16384\n\t" \
  "ds_read_b128 a[192:195], v224 offset:0\n\t" \
  "v_max3_f32 v238, v64, v65, v66\n\t" \
  "v_max3_f32 v240, v80, v81, v82\n\t" \
  "v_max3_f32 v238, v238, v67, v68\n\t" \
  "v_mfma_f32_32x32x16_bf16 a[16:31], v[160:163], v[16:19], a[16:31]\n\t" \
  "ds_read_b128 a[196:199], v225 offset:0\n\t" \
  "buffer_load_dwordx4 v229, %[srdk], %[sk] offen offset:0 lds\n\t" \
  "v_max3_f32 v240, v240, v83, v84\n\t" \
  "v_max3_f32 v239, v97, v98, v99\n\t" \
  "v_max3_f32 v241, v113, v114, v115\n\t" \
  "v_mfma_f32_32x32x16_bf16 a[32:47], v[164:167], v[0:3], a[32:47]\n\t" \
  "ds_read_b128 a[200:203], v226 offset:0\n\t" \
  "v_max3_f32 v238, v238, v69, v70\n\t" \
  "v_max3_f32 v240, v240, v85, v86\n\t" \
  "v_max3_f32 v239, v239, v100, v101\n\t" \
  "v_max3_f32 v241, v241, v116, v117\n\t" \
  "v_mfma_f32_32x32x16_bf16 a[48:63], v[164:167], v[16:19], a[48:63]\n\t" \
  "ds_read_b128 a[204:207], v227 offset:0\n\t" \
  "buffer_load_dwordx4 v230, %[srdk], %[sk] offen offset:1024 lds\n\t" \
  "v_max3_f32 v238, v238, v71, v72\n\t" \
  "v_max3_f32 v240, v240, v87, v88\n\t" \
  "v_max3_f32 v239, v239, v102, v103\n\t" \
  "v_mfma_f32_32x32x16_bf16 a[64:79], v[168:171], v[0:3], a[64:79]\n\t" \
  "ds_read_b128 a[208:211], v224 offset:128\n\t" \
  "v_max3_f32 v241, v241, v118, v119\n\t" \
  "v_max3_f32 v238, v238, v73, v74\n\t" \
  "v_max3_f32 v240, v240, v89, v90\n\t" \
  "v_mfma_f32_32x32x16_bf16 a[80:95], v[168:171], v[16:19], a[80:95]\n\t" \
  "ds_read_b128 a[212:215], v225 offset:128\n\t" \
  "buffer_load_dwordx4 v229, %[srdk], %[sk] offen offset:2048 lds\n\t" \
  "v_max3_f32 v239, v239, v104, v105\n\t" \
  "v_max3_f32 v241, v241, v120, v121\n\t" \
  "v_max3_f32 v238, v238, v75, v76\n\t" \
  "v_max3_f32 v240, v240, v91, v92\n\t" \
  "v_mfma_f32_32x32x16_bf16 a[96:111], v[172:175], v[0:3], a[96:111]\n\t" \
  "ds_read_b128 a[216:219], v226 offset:128\n\t" \
  "v_max3_f32 v239, v239, v106, v107\n\t" \
  "v_max3_f32 v241, v241, v122, v123\n\t" \
  "v_max3_f32 v238, v238, v77, v78\n\t" \
  "v_mfma_f32_32x32x16_bf16 a[112:127], v[172:175], v[16:19], a[112:127]\n\t" \
  "ds_read_b128 a[220:223], v227 offset:128\n\t" \
  "buffer_load_dwordx4 v230, %[srdk], %[sk] offen offset:3072 lds\n\t" \
  "v_max3_f32 v240, v240, v93, v94\n\t" \
  "v_max3_f32 v239, v239, v108, v109\n\t" \
  "v_max3_f32 v241, v241, v124, v125\n\t" \
  "v_mfma_f32_32x32x16_bf16 a[0:15], v[176:179], v[8:11], a[0:15]\n\t" \
  "s_add_u32 m0, %[m0v], 0\n\t" \
  "ds_read_b128 a[224:227], v224 offset:8192\n\t" \
  "v_max3_f32 v238, v238, v79, v96\n\t" \
  "v_max3_f32 v240, v240, v95, v112\n\t" \
  "v_max3_f32 v239, v239, v110, v111\n\t" \
  "v_max3_f32 v241, v241, v126, v127\n\t" \
  "v_mfma_f32_32x32x16_bf16 a[16:31], v[176:179], v[24:27], a[16:31]\n\t" \
  "ds_read_b128 a[228:231], v225 offset:8192\n\t" \
  "buffer_load_dwordx4 v231, %[srdv], %[sv] offen offset:0 lds\n\t" \
  "v_max_f32 v242, v238, v239\n\t" \
  "v_max_f32 v243, v240, v241\n\t" \
  "v_mov_b32 v246, v242\n\t" \
  "v_mov_b32 v247, v243\n\t" \
  "v_mfma_f32_32x32x16_bf16 a[32:47], v[180:183], v[8:11], a[32:47]\n\t" \
  "ds_read_b128 a[232:235], v226 offset:8192\n\t" \
  "v_permlane32_swap_b32 v242, v246\n\t" \
  "v_permlane32_swap_b32 v243, v247\n\t" \
  "v_max_f32 v242, v242, v246\n\t" \
  "v_max_f32 v243, v243, v247\n\t" \
  "v_mfma_f32_32x32x16_bf16 a[48:63], v[180:183], v[24:27], a[48:63]\n\t" \
  "ds_read_b128 a[236:239], v227 offset:8192\n\t" \
  "buffer_load_dwordx4 v231, %[srdv], %[sv2] offen offset:1024 lds\n\t" \
  "v_max_f32 v248, v242, v243\n\t" \
  "v_cmp_lt_f32 vcc, %[sthr], v248\n\t" \
  "s_cbranch_vccnz LRA1_%=\n\t" \
  "LRAB1_%=:\n\t" \
  "v_mfma_f32_32x32x16_bf16 a[64:79], v[184:187], v[8:11], a[64:79]\n\t" \
  "ds_read_b128 a[240:243], v224 offset:8320\n\t" \
  "v_exp_f32 v64, v64\n\t" \
  "v_exp_f32 v65, v65\n\t" \
  "v_mfma_f32_32x32x16_bf16 a[80:95], v[184:187], v[24:27], a[80:95]\n\t" \
  "ds_read_b128 a[244:247], v225 offset:8320\n\t" \
  "buffer_load_dwordx4 v231, %[srdv], %[sv] offen offset:2048 lds\n\t" \
  "v_add_f32 v232, v232, v64\n\t" \
  "v_add_f32 v233, v233, v65\n\t" \
  "v_cvt_pk_bf16_f32 v64, v64, v65\n\t" \
  "v_exp_f32 v66, v66\n\t" \
  "v_mfma_f32_32x32x16_bf16 a[96:111], v[188:191], v[8:11], a[96:111]\n\t" \
  "ds_read_b128 a[248:251], v226 offset:8320\n\t" \
  "v_add_f32 v232, v232, v66\n\t" \
  "v_exp_f32 v67, v67\n\t" \
  "v_mfma_f32_32x32x16_bf16 a[112:127], v[188:191], v[24:27], a[112:127]\n\t" \
  "ds_read_b128 a[252:255], v227 offset:8320\n\t" \
  "buffer_load_dwordx4 v231, %[srdv], %[sv2] offen offset:3072 lds\n\t" \
  "v_add_f32 v233, v233, v67\n\t" \
  "v_cvt_pk_bf16_f32 v65, v66, v67\n\t" \
  "v_exp_f32 v68, v68\n\t" \
  "v_mfma_f32_32x32x16_bf16 a[0:15], v[192:195], v[32:35], a[0:15]\n\t" \
  "ds_read_b64_tr_b16 v[160:161], v228 offset:16384\n\t" \
  "s_add_u32 %[sk], %[sk], 16384\n\t" \
  "v_add_f32 v232, v232, v68\n\t" \
  "v_exp_f32 v69, v69\n\t" \
  "v_exp_f32 v70, v70\n\t" \
  "v_mfma_f32_32x32x16_bf16 a[16:31], v[192:195], v[48:51], a[16:31]\n\t" \
  "ds_read_b64_tr_b16 v[162:163], v228 offset:18432\n\t" \
  "s_add_u32 %[sv], %[sv], 16384\n\t" \
  "v_add_f32 v233, v233, v69\n\t" \
  "v_add_f32 v232, v232, v70\n\t" \
  "v_cvt_pk_bf16_f32 v66, v68, v69\n\t" \
  "v_exp_f32 v71, v71\n\t" \
  "v_mfma_f32_32x32x16_bf16 a[32:47], v[196:199], v[32:35], a[32:47]\n\t" \
  "ds_read_b64_tr_b16 v[164:165], v228 offset:16896\n\t" \
  "s_add_u32 %[sv2], %[sv2], 16384\n\t" \
  "v_add_f32 v233, v233, v71\n\t" \
  "v_cvt_pk_bf16_f32 v67, v70, v71\n\t" \
  "v_exp_f32 v72, v72\n\t" \
  "v_mfma_f32_32x32x16_bf16 a[48:63], v[196:199], v[48:51], a[48:63]\n\t" \
  "ds_read_b64_tr_b16 v[166:167], v228 offset:18944\n\t" \
  "v_add_f32 v232, v232, v72\n\t" \
  "v_exp_f32 v73, v73\n\t" \
  "v_mfma_f32_32x32x16_bf16 a[64:79], v[200:203], v[32:35], a[64:79]\n\t" \
  "ds_read_b64_tr_b16 v[168:169], v228 offset:17408\n\t" \
  "v_add_f32 v233, v233, v73\n\t" \
  "v_cvt_pk_bf16_f32 v72, v72, v73\n\t" \
  "v_exp_f32 v74, v74\n\t" \
  "v_exp_f32 v75, v75\n\t" \
  "v_mfma_f32_32x32x16_bf16 a[80:95], v[200:203], v[48:51], a[80:95]\n\t" \
  "ds_read_b64_tr_b16 v[170:171], v228 offset:19456\n\t" \
  "v_add_f32 v232, v232, v74\n\t" \
  "v_add_f32 v233, v233, v75\n\t" \
  "v_cvt_pk_bf16_f32 v73, v74, v75\n\t" \
  "v_exp_f32 v76, v76\n\t" \
  "v_mfma_f32_32x32x16_bf16 a[96:111], v[204:207], v[32:35], a[96:111]\n\t" \
  "ds_read_b64_tr_b16 v[172:173], v228 offset:17920\n\t" \
  "v_add_f32 v232, v232, v76\n\t" \
  "v_exp_f32 v77, v77\n\t" \
  "v_mfma_f32_32x32x16_bf16 a[112:127], v[204:207], v[48:51], a[112:127]\n\t" \
  "ds_read_b64_tr_b16 v[174:175], v228 offset:19968\n\t" \
  "v_add_f32 v233, v233, v77\n\t" \
  "v_cvt_pk_bf16_f32 v74, v76, v77\n\t" \
  "v_exp_f32 v78, v78\n\t" \
  "v_mfma_f32_32x32x16_bf16 a[0:15], v[208:211], v[40:43], a[0:15]\n\t" \
  "ds_read_b64_tr_b16 v[176:177], v228 offset:20480\n\t" \
  "v_add_f32 v232, v232, v78\n\t" \
  "v_exp_f32 v79, v79\n\t" \
  "v_exp_f32 v80, v80\n\t" \
  "v_mfma_f32_32x32x16_bf16 a[16:31], v[208:211], v[56:59], a[16:31]\n\t" \
  "ds_read_b64_tr_b16 v[178:179], v228 offset:22528\n\t" \
  "v_add_f32 v233, v233, v79\n\t" \
  "v_add_f32 v234, v234, v80\n\t" \
  "v_cvt_pk_bf16_f32 v75, v78, v79\n\t" \
  "v_exp_f32 v81, v81\n\t" \
  "v_mfma_f32_32x32x16_bf16 a[32:47], v[212:215], v[40:43], a[32:47]\n\t" \
  "ds_read_b64_tr_b16 v[180:181], v228 offset:20992\n\t" \
  "v_add_f32 v235, v235, v81\n\t" \
  "v_cvt_pk_bf16_f32 v80, v80, v81\n\t" \
  "v_exp_f32 v82, v82\n\t" \
  "v_mfma_f32_32x32x16_bf16 a[48:63], v[212:215], v[56:59], a[48:63]\n\t" \
  "ds_read_b64_tr_b16 v[182:183], v228 offset:23040\n\t" \
  "v_add_f32 v234, v234, v82\n\t" \
  "v_exp_f32 v83, v83\n\t" \
  "v_mfma_f32_32x32x16_bf16 a[64:79], v[216:219], v[40:43], a[64:79]\n\t" \
  "ds_read_b64_tr_b16 v[184:185], v228 offset:21504\n\t" \
  "v_add_f32 v235, v235, v83\n\t" \
  "v_cvt_pk_bf16_f32 v81, v82, v83\n\t" \
  "v_exp_f32 v84, v84\n\t" \
  "v_exp_f32 v85, v85\n\t" \
  "v_mfma_f32_32x32x16_bf16 a[80:95], v[216:219], v[56:59], a[80:95]\n\t" \
  "ds_read_b64_tr_b16 v[186:187], v228 offset:23552\n\t" \
  "v_add_f32 v234, v234, v84\n\t" \
  "v_add_f32 v235, v235, v85\n\t" \
  "v_cvt_pk_bf16_f32 v82, v84, v85\n\t" \
  "v_exp_f32 v86, v86\n\t" \
  "v_mfma_f32_32x32x16_bf16 a[96:111], v[220:223], v[40:43], a[96:111]\n\t" \
  "ds_read_b64_tr_b16 v[188:189], v228 offset:22016\n\t" \
  "v_add_f32 v234, v234, v86\n\t" \
  "v_exp_f32 v87, v87\n\t" \
  "v_mfma_f32_32x32x16_bf16 a[112:127], v[220:223], v[56:59], a[112:127]\n\t" \
  "ds_read_b64_tr_b16 v[190:191], v228 offset:24064\n\t" \
  "v_add_f32 v235, v235, v87\n\t" \
  "v_cvt_pk_bf16_f32 v83, v86, v87\n\t" \
  "v_exp_f32 v88, v88\n\t" \
  "s_cmp_lg_u32 %[sp], 0\n\t" \
  "s_cbranch_scc1 LRB1_%=\n\t" \
  "LRBB1_%=:\n\t" \
  "s_waitcnt lgkmcnt(8)\n\t" \
  "s_sub_u32 %[sc], %[sc], 1\n\t" \
  "s_cmp_lg_u32 %[sc], 0\n\t" \
  "s_cbranch_scc1 LLOOP_%=\n\t" \
  "ds_read_b64_tr_b16 v[192:193], v228 offset:24576\n\t" \
  "ds_read_b64_tr_b16 v[194:195], v228 offset:26624\n\t" \
  "ds_read_b64_tr_b16 v[196:197], v228 offset:25088\n\t" \
  "ds_read_b64_tr_b16 v[198:199], v228 offset:27136\n\t" \
  "ds_read_b64_tr_b16 v[200:201], v228 offset:25600\n\t" \
  "ds_read_b64_tr_b16 v[202:203], v228 offset:27648\n\t" \
  "ds_read_b64_tr_b16 v[204:205], v228 offset:26112\n\t" \
  "ds_read_b64_tr_b16 v[206:207], v228 offset:28160\n\t" \
  "ds_read_b64_tr_b16 v[208:209], v228 offset:28672\n\t" \
  "ds_read_b64_tr_b16 v[210:211], v228 offset:30720\n\t" \
  "ds_read_b64_tr_b16 v[212:213], v228 offset:29184\n\t" \
  "ds_read_b64_tr_b16 v[214:215], v228 offset:31232\n\t" \
  "ds_read_b64_tr_b16 v[216:217], v228 offset:29696\n\t" \
  "ds_read_b64_tr_b16 v[218:219], v228 offset:31744\n\t" \
  "ds_read_b64_tr_b16 v[220:221], v228 offset:30208\n\t" \
  "ds_read_b64_tr_b16 v[222:223], v228 offset:32256\n\t" \
  "v_add_f32 v234, v234, v88\n\t" \
  "v_exp_f32 v89, v89\n\t" \
  "v_exp_f32 v90, v90\n\t" \
  "s_nop 0\n\t" \
  "v_add_f32 v235, v235, v89\n\t" \
  "v_add_f32 v234, v234, v90\n\t" \
  "v_cvt_pk_bf16_f32 v88, v88, v89\n\t" \
  "v_exp_f32 v91, v91\n\t" \
  "s_nop 0\n\t" \
  "v_add_f32 v235, v235, v91\n\t" \
  "v_cvt_pk_bf16_f32 v89, v90, v91\n\t" \
  "v_exp_f32 v92, v92\n\t" \
  "s_nop 0\n\t" \
  "v_add_f32 v234, v234, v92\n\t" \
  "v_exp_f32 v93, v93\n\t" \
  "v_exp_f32 v94, v94\n\t" \
  "s_nop 0\n\t" \
  "v_add_f32 v235, v235, v93\n\t" \
  "v_add_f32 v234, v234, v94\n\t" \
  "v_cvt_pk_bf16_f32 v90, v92, v93\n\t" \
  "v_exp_f32 v95, v95\n\t" \
  "s_nop 0\n\t" \
  "v_add_f32 v235, v235, v95\n\t" \
  "v_cvt_pk_bf16_f32 v91, v94, v95\n\t" \
  "v_exp_f32 v96, v96\n\t" \
  "s_nop 0\n\t" \
  "v_add_f32 v232, v232, v96\n\t" \
  "v_exp_f32 v97, v97\n\t" \
  "v_exp_f32 v98, v98\n\t" \
  "s_nop 0\n\t" \
  "v_add_f32 v233, v233, v97\n\t" \
  "v_add_f32 v232, v232, v98\n\t" \
  "v_cvt_pk_bf16_f32 v96, v96, v97\n\t" \
  "v_exp_f32 v99, v99\n\t" \
  "s_nop 0\n\t" \
  "v_add_f32 v233, v233, v99\n\t" \
  "v_cvt_pk_bf16_f32 v97, v98, v99\n\t" \
  "v_exp_f32 v100, v100\n\t" \
  "s_nop 0\n\t" \
  "v_add_f32 v232, v232, v100\n\t" \
  "v_exp_f32 v101, v101\n\t" \
  "s_nop 0\n\t" \
  "v_add_f32 v233, v233, v101\n\t" \
  "v_cvt_pk_bf16_f32 v98, v100, v101\n\t" \
  "v_exp_f32 v102, v102\n\t" \
  "v_exp_f32 v103, v103\n\t" \
  "s_nop 0\n\t" \
  "v_add_f32 v232, v232, v102\n\t" \
  "v_add_f32 v233, v233, v103\n\t" \
  "v_cvt_pk_bf16_f32 v99, v102, v103\n\t" \
  "v_exp_f32 v104, v104\n\t" \
  "s_nop 0\n\t" \
  "v_add_f32 v232, v232, v104\n\t" \
  "v_exp_f32 v105, v105\n\t" \
  "s_nop 0\n\t" \
  "v_add_f32 v233, v233, v105\n\t" \
  "v_cvt_pk_bf16_f32 v104, v104, v105\n\t" \
  "v_exp_f32 v106, v106\n\t" \
  "v_exp_f32 v107, v107\n\t" \
  "s_nop 0\n\t" \
  "v_add_f32 v232, v232, v106\n\t" \
  "v_add_f32 v233, v233, v107\n\t" \
  "v_cvt_pk_bf16_f32 v105, v106, v107\n\t" \
  "v_exp_f32 v108, v108\n\t" \
  "s_nop 0\n\t" \
  "v_add_f32 v232, v232, v108\n\t" \
  "v_exp_f32 v109, v109\n\t" \
  "s_nop 0\n\t" \
  "v_add_f32 v233, v233, v109\n\t" \
  "v_cvt_pk_bf16_f32 v106, v108, v109\n\t" \
  "v_exp_f32 v110, v110\n\t" \
  "v_exp_f32 v111, v111\n\t" \
  "s_nop 0\n\t" \
  "v_add_f32 v232, v232, v110\n\t" \
  "v_add_f32 v233, v233, v111\n\t" \
  "v_cvt_pk_bf16_f32 v107, v110, v111\n\t" \
  "v_exp_f32 v112, v112\n\t" \
  "s_nop 0\n\t" \
  "v_add_f32 v234, v234, v112\n\t" \
  "v_exp_f32 v113, v113\n\t" \
  "s_nop 0\n\t" \
  "v_add_f32 v235, v235, v113\n\t" \
  "v_cvt_pk_bf16_f32 v112, v112, v113\n\t" \
  "v_exp_f32 v114, v114\n\t" \
  "s_nop 0\n\t" \
  "v_add_f32 v234, v234, v114\n\t" \
  "v_exp_f32 v115, v115\n\t" \
  "v_exp_f32 v116, v116\n\t" \
  "s_nop 0\n\t" \
  "v_add_f32 v235, v235, v115\n\t" \
  "v_add_f32 v234, v234, v116\n\t" \
  "v_cvt_pk_bf16_f32 v113, v114, v115\n\t" \
  "v_exp_f32 v117, v117\n\t" \
  "s_nop 0\n\t" \
  "v_add_f32 v235, v235, v117\n\t" \
  "v_cvt_pk_bf16_f32 v114, v116, v117\n\t" \
  "v_exp_f32 v118, v118\n\t" \
  "s_nop 0\n\t" \
  "v_add_f32 v234, v234, v118\n\t" \
  "v_exp_f32 v119, v119\n\t" \
  "v_exp_f32 v120, v120\n\t" \
  "s_nop 0\n\t" \
  "v_add_f32 v235, v235, v119\n\t" \
  "v_add_f32 v234, v234, v120\n\t" \
  "v_cvt_pk_bf16_f32 v115, v118, v119\n\t" \
  "v_exp_f32 v121, v121\n\t" \
  "s_nop 0\n\t" \
  "v_add_f32 v235, v235, v121\n\t" \
  "v_cvt_pk_bf16_f32 v120, v120, v121\n\t" \
  "v_exp_f32 v122, v122\n\t" \
  "s_nop 0\n\t" \
  "v_add_f32 v234, v234, v122\n\t" \
  "v_exp_f32 v123, v123\n\t" \
  "v_exp_f32 v124, v124\n\t" \
  "s_nop 0\n\t" \
  "v_add_f32 v235, v235, v123\n\t" \
  "v_add_f32 v234, v234, v124\n\t" \
  "v_cvt_pk_bf16_f32 v121, v122, v123\n\t" \
  "v_exp_f32 v125, v125\n\t" \
  "s_nop 0\n\t" \
  "v_add_f32 v235, v235, v125\n\t" \
  "v_cvt_pk_bf16_f32 v122, v124, v125\n\t" \
  "v_exp_f32 v126, v126\n\t" \
  "s_nop 0\n\t" \
  "v_add_f32 v234, v234, v126\n\t" \
  "v_exp_f32 v127, v127\n\t" \
  "s_nop 0\n\t" \
  "v_add_f32 v235, v235, v127\n\t" \
  "v_cvt_pk_bf16_f32 v123, v126, v127\n\t" \
  "s_waitcnt vmcnt(0) lgkmcnt(0)\n\t" \
  "s_nop 1\n\t" \
  "v_mfma_f32_32x32x16_bf16 a[0:15], v[160:163], v[64:67], a[0:15]\n\t" \
  "v_mfma_f32_32x32x16_bf16 a[16:31], v[160:163], v[80:83], a[16:31]\n\t" \
  "v_mfma_f32_32x32x16_bf16 a[32:47], v[164:167], v[64:67], a[32:47]\n\t" \
  "v_mfma_f32_32x32x16_bf16 a[48:63], v[164:167], v[80:83], a[48:63]\n\t" \
  "v_mfma_f32_32x32x16_bf16 a[64:79], v[168:171], v[64:67], a[64:79]\n\t" \
  "v_mfma_f32_32x32x16_bf16 a[80:95], v[168:171], v[80:83], a[80:95]\n\t" \
  "v_mfma_f32_32x32x16_bf16 a[96:111], v[172:175], v[64:67], a[96:111]\n\t" \
  "v_mfma_f32_32x32x16_bf16 a[112:127], v[172:175], v[80:83], a[112:127]\n\t" \
  "v_mfma_f32_32x32x16_bf16 a[0:15], v[176:179], v[72:75], a[0:15]\n\t" \
  "v_mfma_f32_32x32x16_bf16 a[16:31], v[176:179], v[88:91], a[16:31]\n\t" \
  "v_mfma_f32_32x32x16_bf16 a[32:47], v[180:183], v[72:75], a[32:47]\n\t" \
  "v_mfma_f32_32x32x16_bf16 a[48:63], v[180:183], v[88:91], a[48:63]\n\t" \
  "v_mfma_f32_32x32x16_bf16 a[64:79], v[184:187], v[72:75], a[64:79]\n\t" \
  "v_mfma_f32_32x32x16_bf16 a[80:95], v[184:187], v[88:91], a[80:95]\n\t" \
  "v_mfma_f32_32x32x16_bf16 a[96:111], v[188:191], v[72:75], a[96:111]\n\t" \
  "v_mfma_f32_32x32x16_bf16 a[112:127], v[188:191], v[88:91], a[112:127]\n\t" \
  "v_mfma_f32_32x32x16_bf16 a[0:15], v[192:195], v[96:99], a[0:15]\n\t" \
  "v_mfma_f32_32x32x16_bf16 a[16:31], v[192:195], v[112:115], a[16:31]\n\t" \
  "v_mfma_f32_32x32x16_bf16 a[32:47], v[196:199], v[96:99], a[32:47]\n\t" \
  "v_mfma_f32_32x32x16_bf16 a[48:63], v[196:199], v[112:115], a[48:63]\n\t" \
  "v_mfma_f32_32x32x16_bf16 a[64:79], v[200:203], v[96:99], a[64:79]\n\t" \
  "v_mfma_f32_32x32x16_bf16 a[80:95], v[200:203], v[112:115], a[80:95]\n\t" \
  "v_mfma_f32_32x32x16_bf16 a[96:111], v[204:207], v[96:99], a[96:111]\n\t" \
  "v_mfma_f32_32x32x16_bf16 a[112:127], v[204:207], v[112:115], a[112:127]\n\t" \
  "v_mfma_f32_32x32x16_bf16 a[0:15], v[208:211], v[104:107], a[0:15]\n\t" \
  "v_mfma_f32_32x32x16_bf16 a[16:31], v[208:211], v[120:123], a[16:31]\n\t" \
  "v_mfma_f32_32x32x16_bf16 a[32:47], v[212:215], v[104:107], a[32:47]\n\t" \
  "v_mfma_f32_32x32x16_bf16 a[48:63], v[212:215], v[120:123], a[48:63]\n\t" \
  "v_mfma_f32_32x32x16_bf16 a[64:79], v[216:219], v[104:107], a[64:79]\n\t" \
  "v_mfma_f32_32x32x16_bf16 a[80:95], v[216:219], v[120:123], a[80:95]\n\t" \
  "v_mfma_f32_32x32x16_bf16 a[96:111], v[220:223], v[104:107], a[96:111]\n\t" \
  "v_mfma_f32_32x32x16_bf16 a[112:127], v[220:223], v[120:123], a[112:127]\n\t" \
  "s_nop 7\n\t" \
  "s_nop 7\n\t" \
  "s_branch LEND_%=\n\t" \
  "LRA0_%=:\n\t" \
  "v_add_f32 v246, %[smarg], v242\n\t" \
  "v_max_f32 v246, 0, v246\n\t" \
  "v_add_f32 v247, %[smarg], v243\n\t" \
  "v_max_f32 v247, 0, v247\n\t" \
  "v_add_f32 v236, v236, v246\n\t" \
  "v_exp_f32_e64 v244, -v246\n\t" \
  "v_add_f32 v237, v237, v247\n\t" \
  "v_exp_f32_e64 v245, -v247\n\t" \
  "v_sub_f32 v0, v0, v246\n\t" \
  "v_sub_f32 v1, v1, v246\n\t" \
  "v_sub_f32 v2, v2, v246\n\t" \
  "v_sub_f32 v3, v3, v246\n\t" \
  "v_sub_f32 v4, v4, v246\n\t" \
  "v_sub_f32 v5, v5, v246\n\t" \
  "v_sub_f32 v6, v6, v246\n\t" \
  "v_sub_f32 v7, v7, v246\n\t" \
  "v_sub_f32 v8, v8, v246\n\t" \
  "v_sub_f32 v9, v9, v246\n\t" \
  "v_sub_f32 v10, v10, v246\n\t" \
  "v_sub_f32 v11, v11, v246\n\t" \
  "v_sub_f32 v12, v12, v246\n\t" \
  "v_sub_f32 v13, v13, v246\n\t" \
  "v_sub_f32 v14, v14, v246\n\t" \
  "v_sub_f32 v15, v15, v246\n\t" \
  "v_sub_f32 v32, v32, v246\n\t" \
  "v_sub_f32 v33, v33, v246\n\t" \
  "v_sub_f32 v34, v34, v246\n\t" \
  "v_sub_f32 v35, v35, v246\n\t" \
  "v_sub_f32 v36, v36, v246\n\t" \
  "v_sub_f32 v37, v37, v246\n\t" \
  "v_sub_f32 v38, v38, v246\n\t" \
  "v_sub_f32 v39, v39, v246\n\t" \
  "v_sub_f32 v40, v40, v246\n\t" \
  "v_sub_f32 v41, v41, v246\n\t" \
  "v_sub_f32 v42, v42, v246\n\t" \
  "v_sub_f32 v43, v43, v246\n\t" \
  "v_sub_f32 v44, v44, v246\n\t" \
  "v_sub_f32 v45, v45, v246\n\t" \
  "v_sub_f32 v46, v46, v246\n\t" \
  "v_sub_f32 v47, v47, v246\n\t" \
  "v_sub_f32 v128, 0, v236\n\t" \
  "v_sub_f32 v129, 0, v236\n\t" \
  "v_sub_f32 v130, 0, v236\n\t" \
  "v_sub_f32 v131, 0, v236\n\t" \
  "v_sub_f32 v132, 0, v236\n\t" \
  "v_sub_f32 v133, 0, v236\n\t" \
  "v_sub_f32 v134, 0, v236\n\t" \
  "v_sub_f32 v135, 0, v236\n\t" \
  "v_sub_f32 v136, 0, v236\n\t" \
  "v_sub_f32 v137, 0, v236\n\t" \
  "v_sub_f32 v138, 0, v236\n\t" \
  "v_sub_f32 v139, 0, v236\n\t" \
  "v_sub_f32 v140, 0, v236\n\t" \
  "v_sub_f32 v141, 0, v236\n\t" \
  "v_sub_f32 v142, 0, v236\n\t" \
  "v_sub_f32 v143, 0, v236\n\t" \
  "v_mul_f32 v232, v232, v244\n\t" \
  "v_mul_f32 v233, v233, v244\n\t" \
  "v_sub_f32 v16, v16, v247\n\t" \
  "v_sub_f32 v17, v17, v247\n\t" \
  "v_sub_f32 v18, v18, v247\n\t" \
  "v_sub_f32 v19, v19, v247\n\t" \
  "v_sub_f32 v20, v20, v247\n\t" \
  "v_sub_f32 v21, v21, v247\n\t" \
  "v_sub_f32 v22, v22, v247\n\t" \
  "v_sub_f32 v23, v23, v247\n\t" \
  "v_sub_f32 v24, v24, v247\n\t" \
  "v_sub_f32 v25, v25, v247\n\t" \
  "v_sub_f32 v26, v26, v247\n\t" \
  "v_sub_f32 v27, v27, v247\n\t" \
  "v_sub_f32 v28, v28, v247\n\t" \
  "v_sub_f32 v29, v29, v247\n\t" \
  "v_sub_f32 v30, v30, v247\n\t" \
  "v_sub_f32 v31, v31, v247\n\t" \
  "v_sub_f32 v48, v48, v247\n\t" \
  "v_sub_f32 v49, v49, v247\n\t" \
  "v_sub_f32 v50, v50, v247\n\t" \
  "v_sub_f32 v51, v51, v247\n\t" \
  "v_sub_f32 v52, v52, v247\n\t" \
  "v_sub_f32 v53, v53, v247\n\t" \
  "v_sub_f32 v54, v54, v247\n\t" \
  "v_sub_f32 v55, v55, v247\n\t" \
  "v_sub_f32 v56, v56, v247\n\t" \
  "v_sub_f32 v57, v57, v247\n\t" \
  "v_sub_f32 v58, v58, v247\n\t" \
  "v_sub_f32 v59, v59, v247\n\t" \
  "v_sub_f32 v60, v60, v247\n\t" \
  "v_sub_f32 v61, v61, v247\n\t" \
  "v_sub_f32 v62, v62, v247\n\t" \
  "v_sub_f32 v63, v63, v247\n\t" \
  "v_sub_f32 v144, 0, v237\n\t" \
  "v_sub_f32 v145, 0, v237\n\t" \
  "v_sub_f32 v146, 0, v237\n\t" \
  "v_sub_f32 v147, 0, v237\n\t" \
  "v_sub_f32 v148, 0, v237\n\t" \
  "v_sub_f32 v149, 0, v237\n\t" \
  "v_sub_f32 v150, 0, v237\n\t" \
  "v_sub_f32 v151, 0, v237\n\t" \
  "v_sub_f32 v152, 0, v237\n\t" \
  "v_sub_f32 v153, 0, v237\n\t" \
  "v_sub_f32 v154, 0, v237\n\t" \
  "v_sub_f32 v155, 0, v237\n\t" \
  "v_sub_f32 v156, 0, v237\n\t" \
  "v_sub_f32 v157, 0, v237\n\t" \
  "v_sub_f32 v158, 0, v237\n\t" \
  "v_sub_f32 v159, 0, v237\n\t" \
  "v_mul_f32 v234, v234, v245\n\t" \
  "v_mul_f32 v235, v235, v245\n\t" \
  "s_mov_b32 %[sp], 1\n\t" \
  "s_branch LRAB0_%=\n\t" \
  "LRB0_%=:\n\t" \
  "s_nop 7\n\t" \
  "s_nop 7\n\t" \
  "v_accvgpr_read_b32 v246, a0\n\t" \
  "v_mul_f32 v246, v244, v246\n\t" \
  "v_accvgpr_write_b32 a0, v246\n\t" \
  "v_accvgpr_read_b32 v247, a1\n\t" \
  "v_mul_f32 v247, v244, v247\n\t" \
  "v_accvgpr_write_b32 a1, v247\n\t" \
  "v_accvgpr_read_b32 v246, a2\n\t" \
  "v_mul_f32 v246, v244, v246\n\t" \
  "v_accvgpr_write_b32 a2, v246\n\t" \
  "v_accvgpr_read_b32 v247, a3\n\t" \
  "v_mul_f32 v247, v244, v247\n\t" \
  "v_accvgpr_write_b32 a3, v247\n\t" \
  "v_accvgpr_read_b32 v246, a4\n\t" \
  "v_mul_f32 v246, v244, v246\n\t" \
  "v_accvgpr_write_b32 a4, v246\n\t" \
  "v_accvgpr_read_b32 v247, a5\n\t" \
  "v_mul_f32 v247, v244, v247\n\t" \
  "v_accvgpr_write_b32 a5, v247\n\t" \
  "v_accvgpr_read_b32 v246, a6\n\t" \
  "v_mul_f32 v246, v244, v246\n\t" \
  "v_accvgpr_write_b32 a6, v246\n\t" \
  "v_accvgpr_read_b32 v247, a7\n\t" \
  "v_mul_f32 v247, v244, v247\n\t" \
  "v_accvgpr_write_b32 a7, v247\n\t" \
  "v_accvgpr_read_b32 v246, a8\n\t" \
  "v_mul_f32 v246, v244, v246\n\t" \
  "v_accvgpr_write_b32 a8, v246\n\t" \
  "v_accvgpr_read_b32 v247, a9\n\t" \
  "v_mul_f32 v247, v244, v247\n\t" \
  "v_accvgpr_write_b32 a9, v247\n\t" \
  "v_accvgpr_read_b32 v246, a10\n\t" \
  "v_mul_f32 v246, v244, v246\n\t" \
  "v_accvgpr_write_b32 a10, v246\n\t" \
  "v_accvgpr_read_b32 v247, a11\n\t" \
  "v_mul_f32 v247, v244, v247\n\t" \
  "v_accvgpr_write_b32 a11, v247\n\t" \
  "v_accvgpr_read_b32 v246, a12\n\t" \
  "v_mul_f32 v246, v244, v246\n\t" \
  "v_accvgpr_write_b32 a12, v246\n\t" \
  "v_accvgpr_read_b32 v247, a13\n\t" \
  "v_mul_f32 v247, v244, v247\n\t" \
  "v_accvgpr_write_b32 a13, v247\n\t" \
  "v_accvgpr_read_b32 v246, a14\n\t" \
  "v_mul_f32 v246, v244, v246\n\t" \
  "v_accvgpr_write_b32 a14, v246\n\t" \
  "v_accvgpr_read_b32 v247, a15\n\t" \
  "v_mul_f32 v247, v244, v247\n\t" \
  "v_accvgpr_write_b32 a15, v247\n\t" \
  "v_accvgpr_read_b32 v246, a16\n\t" \
  "v_mul_f32 v246, v245, v246\n\t" \
  "v_accvgpr_write_b32 a16, v246\n\t" \
  "v_accvgpr_read_b32 v247, a17\n\t" \
  "v_mul_f32 v247, v245, v247\n\t" \
  "v_accvgpr_write_b32 a17, v247\n\t" \
  "v_accvgpr_read_b32 v246, a18\n\t" \
  "v_mul_f32 v246, v245, v246\n\t" \
  "v_accvgpr_write_b32 a18, v246\n\t" \
  "v_accvgpr_read_b32 v247, a19\n\t" \
  "v_mul_f32 v247, v245, v247\n\t" \
  "v_accvgpr_write_b32 a19, v247\n\t" \
  "v_accvgpr_read_b32 v246, a20\n\t" \
  "v_mul_f32 v246, v245, v246\n\t" \
  "v_accvgpr_write_b32 a20, v246\n\t" \
  "v_accvgpr_read_b32 v247, a21\n\t" \
  "v_mul_f32 v247, v245, v247\n\t" \
  "v_accvgpr_write_b32 a21, v247\n\t" \
  "v_accvgpr_read_b32 v246, a22\n\t" \
  "v_mul_f32 v246, v245, v246\n\t" \
  "v_accvgpr_write_b32 a22, v246\n\t" \
  "v_accvgpr_read_b32 v247, a23\n\t" \
  "v_mul_f32 v247, v245, v247\n\t" \
  "v_accvgpr_write_b32 a23, v247\n\t" \
  "v_accvgpr_read_b32 v246, a24\n\t" \
  "v_mul_f32 v246, v245, v246\n\t" \
  "v_accvgpr_write_b32 a24, v246\n\t" \
  "v_accvgpr_read_b32 v247, a25\n\t" \
  "v_mul_f32 v247, v245, v247\n\t" \
  "v_accvgpr_write_b32 a25, v247\n\t" \
  "v_accvgpr_read_b32 v246, a26\n\t" \
  "v_mul_f32 v246, v245, v246\n\t" \
  "v_accvgpr_write_b32 a26, v246\n\t" \
  "v_accvgpr_read_b32 v247, a27\n\t" \
  "v_mul_f32 v247, v245, v247\n\t" \
  "v_accvgpr_write_b32 a27, v247\n\t" \
  "v_accvgpr_read_b32 v246, a28\n\t" \
  "v_mul_f32 v246, v245, v246\n\t" \
  "v_accvgpr_write_b32 a28, v246\n\t" \
  "v_accvgpr_read_b32 v247, a29\n\t" \
  "v_mul_f32 v247, v245, v247\n\t" \
  "v_accvgpr_write_b32 a29, v247\n\t" \
  "v_accvgpr_read_b32 v246, a30\n\t" \
  "v_mul_f32 v246, v245, v246\n\t" \
  "v_accvgpr_write_b32 a30, v246\n\t" \
  "v_accvgpr_read_b32 v247, a31\n\t" \
  "v_mul_f32 v247, v245, v247\n\t" \
  "v_accvgpr_write_b32 a31, v247\n\t" \
  "v_accvgpr_read_b32 v246, a32\n\t" \
  "v_mul_f32 v246, v244, v246\n\t" \
  "v_accvgpr_write_b32 a32, v246\n\t" \
  "v_accvgpr_read_b32 v247, a33\n\t" \
  "v_mul_f32 v247, v244, v247\n\t" \
  "v_accvgpr_write_b32 a33, v247\n\t" \
  "v_accvgpr_read_b32 v246, a34\n\t" \
  "v_mul_f32 v246, v244, v246\n\t" \
  "v_accvgpr_write_b32 a34, v246\n\t" \
  "v_accvgpr_read_b32 v247, a35\n\t" \
  "v_mul_f32 v247, v244, v247\n\t" \
  "v_accvgpr_write_b32 a35, v247\n\t" \
  "v_accvgpr_read_b32 v246, a36\n\t" \
  "v_mul_f32 v246, v244, v246\n\t" \
  "v_accvgpr_write_b32 a36, v246\n\t" \
  "v_accvgpr_read_b32 v247, a37\n\t" \
  "v_mul_f32 v247, v244, v247\n\t" \
  "v_accvgpr_write_b32 a37, v247\n\t" \
  "v_accvgpr_read_b32 v246, a38\n\t" \
  "v_mul_f32 v246, v244, v246\n\t" \
  "v_accvgpr_write_b32 a38, v246\n\t" \
  "v_accvgpr_read_b32 v247, a39\n\t" \
  "v_mul_f32 v247, v244, v247\n\t" \
  "v_accvgpr_write_b32 a39, v247\n\t" \
  "v_accvgpr_read_b32 v246, a40\n\t" \
  "v_mul_f32 v246, v244, v246\n\t" \
  "v_accvgpr_write_b32 a40, v246\n\t" \
  "v_accvgpr_read_b32 v247, a41\n\t" \
  "v_mul_f32 v247, v244, v247\n\t" \
  "v_accvgpr_write_b32 a41, v247\n\t" \
  "v_accvgpr_read_b32 v246, a42\n\t" \
  "v_mul_f32 v246, v244, v246\n\t" \
  "v_accvgpr_write_b32 a42, v246\n\t" \
  "v_accvgpr_read_b32 v247, a43\n\t" \
  "v_mul_f32 v247, v244, v247\n\t" \
  "v_accvgpr_write_b32 a43, v247\n\t" \
  "v_accvgpr_read_b32 v246, a44\n\t" \
  "v_mul_f32 v246, v244, v246\n\t" \
  "v_accvgpr_write_b32 a44, v246\n\t" \
  "v_accvgpr_read_b32 v247, a45\n\t" \
  "v_mul_f32 v247, v244, v247\n\t" \
  "v_accvgpr_write_b32 a45, v247\n\t" \
  "v_accvgpr_read_b32 v246, a46\n\t" \
  "v_mul_f32 v246, v244, v246\n\t" \
  "v_accvgpr_write_b32 a46, v246\n\t" \
  "v_accvgpr_read_b32 v247, a47\n\t" \
  "v_mul_f32 v247, v244, v247\n\t" \
  "v_accvgpr_write_b32 a47, v247\n\t" \
  "v_accvgpr_read_b32 v246, a48\n\t" \
  "v_mul_f32 v246, v245, v246\n\t" \
  "v_accvgpr_write_b32 a48, v246\n\t" \
  "v_accvgpr_read_b32 v247, a49\n\t" \
  "v_mul_f32 v247, v245, v247\n\t" \
  "v_accvgpr_write_b32 a49, v247\n\t" \
  "v_accvgpr_read_b32 v246, a50\n\t" \
  "v_mul_f32 v246, v245, v246\n\t" \
  "v_accvgpr_write_b32 a50, v246\n\t" \
  "v_accvgpr_read_b32 v247, a51\n\t" \
  "v_mul_f32 v247, v245, v247\n\t" \
  "v_accvgpr_write_b32 a51, v247\n\t" \
  "v_accvgpr_read_b32 v246, a52\n\t" \
  "v_mul_f32 v246, v245, v246\n\t" \
  "v_accvgpr_write_b32 a52, v246\n\t" \
  "v_accvgpr_read_b32 v247, a53\n\t" \
  "v_mul_f32 v247, v245, v247\n\t" \
  "v_accvgpr_write_b32 a53, v247\n\t" \
  "v_accvgpr_read_b32 v246, a54\n\t" \
  "v_mul_f32 v246, v245, v246\n\t" \
  "v_accvgpr_write_b32 a54, v246\n\t" \
  "v_accvgpr_read_b32 v247, a55\n\t" \
  "v_mul_f32 v247, v245, v247\n\t" \
  "v_accvgpr_write_b32 a55, v247\n\t" \
  "v_accvgpr_read_b32 v246, a56\n\t" \
  "v_mul_f32 v246, v245, v246\n\t" \
  "v_accvgpr_write_b32 a56, v246\n\t" \
  "v_accvgpr_read_b32 v247, a57\n\t" \
  "v_mul_f32 v247, v245, v247\n\t" \
  "v_accvgpr_write_b32 a57, v247\n\t" \
  "v_accvgpr_read_b32 v246, a58\n\t" \
  "v_mul_f32 v246, v245, v246\n\t" \
  "v_accvgpr_write_b32 a58, v246\n\t" \
  "v_accvgpr_read_b32 v247, a59\n\t" \
  "v_mul_f32 v247, v245, v247\n\t" \
  "v_accvgpr_write_b32 a59, v247\n\t" \
  "v_accvgpr_read_b32 v246, a60\n\t" \
  "v_mul_f32 v246, v245, v246\n\t" \
  "v_accvgpr_write_b32 a60, v246\n\t" \
  "v_accvgpr_read_b32 v247, a61\n\t" \
  "v_mul_f32 v247, v245, v247\n\t" \
  "v_accvgpr_write_b32 a61, v247\n\t" \
  "v_accvgpr_read_b32 v246, a62\n\t" \
  "v_mul_f32 v246, v245, v246\n\t" \
  "v_accvgpr_write_b32 a62, v246\n\t" \
  "v_accvgpr_read_b32 v247, a63\n\t" \
  "v_mul_f32 v247, v245, v247\n\t" \
  "v_accvgpr_write_b32 a63, v247\n\t" \
  "v_accvgpr_read_b32 v246, a64\n\t" \
  "v_mul_f32 v246, v244, v246\n\t" \
  "v_accvgpr_write_b32 a64, v246\n\t" \
  "v_accvgpr_read_b32 v247, a65\n\t" \
  "v_mul_f32 v247, v244, v247\n\t" \
  "v_accvgpr_write_b32 a65, v247\n\t" \
  "v_accvgpr_read_b32 v246, a66\n\t" \
  "v_mul_f32 v246, v244, v246\n\t" \
  "v_accvgpr_write_b32 a66, v246\n\t" \
  "v_accvgpr_read_b32 v247, a67\n\t" \
  "v_mul_f32 v247, v244, v247\n\t" \
  "v_accvgpr_write_b32 a67, v247\n\t" \
  "v_accvgpr_read_b32 v246, a68\n\t" \
  "v_mul_f32 v246, v244, v246\n\t" \
  "v_accvgpr_write_b32 a68, v246\n\t" \
  "v_accvgpr_read_b32 v247, a69\n\t" \
  "v_mul_f32 v247, v244, v247\n\t" \
  "v_accvgpr_write_b32 a69, v247\n\t" \
  "v_accvgpr_read_b32 v246, a70\n\t" \
  "v_mul_f32 v246, v244, v246\n\t" \
  "v_accvgpr_write_b32 a70, v246\n\t" \
  "v_accvgpr_read_b32 v247, a71\n\t" \
  "v_mul_f32 v247, v244, v247\n\t" \
  "v_accvgpr_write_b32 a71, v247\n\t" \
  "v_accvgpr_read_b32 v246, a72\n\t" \
  "v_mul_f32 v246, v244, v246\n\t" \
  "v_accvgpr_write_b32 a72, v246\n\t" \
  "v_accvgpr_read_b32 v247, a73\n\t" \
  "v_mul_f32 v247, v244, v247\n\t" \
  "v_accvgpr_write_b32 a73, v247\n\t" \
  "v_accvgpr_read_b32 v246, a74\n\t" \
  "v_mul_f32 v246, v244, v246\n\t" \
  "v_accvgpr_write_b32 a74, v246\n\t" \
  "v_accvgpr_read_b32 v247, a75\n\t" \
  "v_mul_f32 v247, v244, v247\n\t" \
  "v_accvgpr_write_b32 a75, v247\n\t" \
  "v_accvgpr_read_b32 v246, a76\n\t" \
  "v_mul_f32 v246, v244, v246\n\t" \
  "v_accvgpr_write_b32 a76, v246\n\t" \
  "v_accvgpr_read_b32 v247, a77\n\t" \
  "v_mul_f32 v247, v244, v247\n\t" \
  "v_accvgpr_write_b32 a77, v247\n\t" \
  "v_accvgpr_read_b32 v246, a78\n\t" \
  "v_mul_f32 v246, v244, v246\n\t" \
  "v_accvgpr_write_b32 a78, v246\n\t" \
  "v_accvgpr_read_b32 v247, a79\n\t" \
  "v_mul_f32 v247, v244, v247\n\t" \
  "v_accvgpr_write_b32 a79, v247\n\t" \
  "v_accvgpr_read_b32 v246, a80\n\t" \
  "v_mul_f32 v246, v245, v246\n\t" \
  "v_accvgpr_write_b32 a80, v246\n\t" \
  "v_accvgpr_read_b32 v247, a81\n\t" \
  "v_mul_f32 v247, v245, v247\n\t" \
  "v_accvgpr_write_b32 a81, v247\n\t" \
  "v_accvgpr_read_b32 v246, a82\n\t" \
  "v_mul_f32 v246, v245, v246\n\t" \
  "v_accvgpr_write_b32 a82, v246\n\t" \
  "v_accvgpr_read_b32 v247, a83\n\t" \
  "v_mul_f32 v247, v245, v247\n\t" \
  "v_accvgpr_write_b32 a83, v247\n\t" \
  "v_accvgpr_read_b32 v246, a84\n\t" \
  "v_mul_f32 v246, v245, v246\n\t" \
  "v_accvgpr_write_b32 a84, v246\n\t" \
  "v_accvgpr_read_b32 v247, a85\n\t" \
  "v_mul_f32 v247, v245, v247\n\t" \
  "v_accvgpr_write_b32 a85, v247\n\t" \
  "v_accvgpr_read_b32 v246, a86\n\t" \
  "v_mul_f32 v246, v245, v246\n\t" \
  "v_accvgpr_write_b32 a86, v246\n\t" \
  "v_accvgpr_read_b32 v247, a87\n\t" \
  "v_mul_f32 v247, v245, v247\n\t" \
  "v_accvgpr_write_b32 a87, v247\n\t" \
  "v_accvgpr_read_b32 v246, a88\n\t" \
  "v_mul_f32 v246, v245, v246\n\t" \
  "v_accvgpr_write_b32 a88, v246\n\t" \
  "v_accvgpr_read_b32 v247, a89\n\t" \
  "v_mul_f32 v247, v245, v247\n\t" \
  "v_accvgpr_write_b32 a89, v247\n\t" \
  "v_accvgpr_read_b32 v246, a90\n\t" \
  "v_mul_f32 v246, v245, v246\n\t" \
  "v_accvgpr_write_b32 a90, v246\n\t" \
  "v_accvgpr_read_b32 v247, a91\n\t" \
  "v_mul_f32 v247, v245, v247\n\t" \
  "v_accvgpr_write_b32 a91, v247\n\t" \
  "v_accvgpr_read_b32 v246, a92\n\t" \
  "v_mul_f32 v246, v245, v246\n\t" \
  "v_accvgpr_write_b32 a92, v246\n\t" \
  "v_accvgpr_read_b32 v247, a93\n\t" \
  "v_mul_f32 v247, v245, v247\n\t" \
  "v_accvgpr_write_b32 a93, v247\n\t" \
  "v_accvgpr_read_b32 v246, a94\n\t" \
  "v_mul_f32 v246, v245, v246\n\t" \
  "v_accvgpr_write_b32 a94, v246\n\t" \
  "v_accvgpr_read_b32 v247, a95\n\t" \
  "v_mul_f32 v247, v245, v247\n\t" \
  "v_accvgpr_write_b32 a95, v247\n\t" \
  "v_accvgpr_read_b32 v246, a96\n\t" \
  "v_mul_f32 v246, v244, v246\n\t" \
  "v_accvgpr_write_b32 a96, v246\n\t" \
  "v_accvgpr_read_b32 v247, a97\n\t" \
  "v_mul_f32 v247, v244, v247\n\t" \
  "v_accvgpr_write_b32 a97, v247\n\t" \
  "v_accvgpr_read_b32 v246, a98\n\t" \
  "v_mul_f32 v246, v244, v246\n\t" \
  "v_accvgpr_write_b32 a98, v246\n\t" \
  "v_accvgpr_read_b32 v247, a99\n\t" \
  "v_mul_f32 v247, v244, v247\n\t" \
  "v_accvgpr_write_b32 a99, v247\n\t" \
  "v_accvgpr_read_b32 v246, a100\n\t" \
  "v_mul_f32 v246, v244, v246\n\t" \
  "v_accvgpr_write_b32 a100, v246\n\t" \
  "v_accvgpr_read_b32 v247, a101\n\t" \
  "v_mul_f32 v247, v244, v247\n\t" \
  "v_accvgpr_write_b32 a101, v247\n\t" \
  "v_accvgpr_read_b32 v246, a102\n\t" \
  "v_mul_f32 v246, v244, v246\n\t" \
  "v_accvgpr_write_b32 a102, v246\n\t" \
  "v_accvgpr_read_b32 v247, a103\n\t" \
  "v_mul_f32 v247, v244, v247\n\t" \
  "v_accvgpr_write_b32 a103, v247\n\t" \
  "v_accvgpr_read_b32 v246, a104\n\t" \
  "v_mul_f32 v246, v244, v246\n\t" \
  "v_accvgpr_write_b32 a104, v246\n\t" \
  "v_accvgpr_read_b32 v247, a105\n\t" \
  "v_mul_f32 v247, v244, v247\n\t" \
  "v_accvgpr_write_b32 a105, v247\n\t" \
  "v_accvgpr_read_b32 v246, a106\n\t" \
  "v_mul_f32 v246, v244, v246\n\t" \
  "v_accvgpr_write_b32 a106, v246\n\t" \
  "v_accvgpr_read_b32 v247, a107\n\t" \
  "v_mul_f32 v247, v244, v247\n\t" \
  "v_accvgpr_write_b32 a107, v247\n\t" \
  "v_accvgpr_read_b32 v246, a108\n\t" \
  "v_mul_f32 v246, v244, v246\n\t" \
  "v_accvgpr_write_b32 a108, v246\n\t" \
  "v_accvgpr_read_b32 v247, a109\n\t" \
  "v_mul_f32 v247, v244, v247\n\t" \
  "v_accvgpr_write_b32 a109, v247\n\t" \
  "v_accvgpr_read_b32 v246, a110\n\t" \
  "v_mul_f32 v246, v244, v246\n\t" \
  "v_accvgpr_write_b32 a110, v246\n\t" \
  "v_accvgpr_read_b32 v247, a111\n\t" \
  "v_mul_f32 v247, v244, v247\n\t" \
  "v_accvgpr_write_b32 a111, v247\n\t" \
  "v_accvgpr_read_b32 v246, a112\n\t" \
  "v_mul_f32 v246, v245, v246\n\t" \
  "v_accvgpr_write_b32 a112, v246\n\t" \
  "v_accvgpr_read_b32 v247, a113\n\t" \
  "v_mul_f32 v247, v245, v247\n\t" \
  "v_accvgpr_write_b32 a113, v247\n\t" \
  "v_accvgpr_read_b32 v246, a114\n\t" \
  "v_mul_f32 v246, v245, v246\n\t" \
  "v_accvgpr_write_b32 a114, v246\n\t" \
  "v_accvgpr_read_b32 v247, a115\n\t" \
  "v_mul_f32 v247, v245, v247\n\t" \
  "v_accvgpr_write_b32 a115, v247\n\t" \
  "v_accvgpr_read_b32 v246, a116\n\t" \
  "v_mul_f32 v246, v245, v246\n\t" \
  "v_accvgpr_write_b32 a116, v246\n\t" \
  "v_accvgpr_read_b32 v247, a117\n\t" \
  "v_mul_f32 v247, v245, v247\n\t" \
  "v_accvgpr_write_b32 a117, v247\n\t" \
  "v_accvgpr_read_b32 v246, a118\n\t" \
  "v_mul_f32 v246, v245, v246\n\t" \
  "v_accvgpr_write_b32 a118, v246\n\t" \
  "v_accvgpr_read_b32 v247, a119\n\t" \
  "v_mul_f32 v247, v245, v247\n\t" \
  "v_accvgpr_write_b32 a119, v247\n\t" \
  "v_accvgpr_read_b32 v246, a120\n\t" \
  "v_mul_f32 v246, v245, v246\n\t" \
  "v_accvgpr_write_b32 a120, v246\n\t" \
  "v_accvgpr_read_b32 v247, a121\n\t" \
  "v_mul_f32 v247, v245, v247\n\t" \
  "v_accvgpr_write_b32 a121, v247\n\t" \
  "v_accvgpr_read_b32 v246, a122\n\t" \
  "v_mul_f32 v246, v245, v246\n\t" \
  "v_accvgpr_write_b32 a122, v246\n\t" \
  "v_accvgpr_read_b32 v247, a123\n\t" \
  "v_mul_f32 v247, v245, v247\n\t" \
  "v_accvgpr_write_b32 a123, v247\n\t" \
  "v_accvgpr_read_b32 v246, a124\n\t" \
  "v_mul_f32 v246, v245, v246\n\t" \
  "v_accvgpr_write_b32 a124, v246\n\t" \
  "v_accvgpr_read_b32 v247, a125\n\t" \
  "v_mul_f32 v247, v245, v247\n\t" \
  "v_accvgpr_write_b32 a125, v247\n\t" \
  "v_accvgpr_read_b32 v246, a126\n\t" \
  "v_mul_f32 v246, v245, v246\n\t" \
  "v_accvgpr_write_b32 a126, v246\n\t" \
  "v_accvgpr_read_b32 v247, a127\n\t" \
  "v_mul_f32 v247, v245, v247\n\t" \
  "v_accvgpr_write_b32 a127, v247\n\t" \
  "s_nop 1\n\t" \
  "s_mov_b32 %[sp], 0\n\t" \
  "s_branch LRBB0_%=\n\t" \
  "LRA1_%=:\n\t" \
  "v_add_f32 v246, %[smarg], v242\n\t" \
  "v_max_f32 v246, 0, v246\n\t" \
  "v_add_f32 v247, %[smarg], v243\n\t" \
  "v_max_f32 v247, 0, v247\n\t" \
  "v_add_f32 v236, v236, v246\n\t" \
  "v_exp_f32_e64 v244, -v246\n\t" \
  "v_add_f32 v237, v237, v247\n\t" \
  "v_exp_f32_e64 v245, -v247\n\t" \
  "v_sub_f32 v64, v64, v246\n\t" \
  "v_sub_f32 v65, v65, v246\n\t" \
  "v_sub_f32 v66, v66, v246\n\t" \
  "v_sub_f32 v67, v67, v246\n\t" \
  "v_sub_f32 v68, v68, v246\n\t" \
  "v_sub_f32 v69, v69, v246\n\t" \
  "v_sub_f32 v70, v70, v246\n\t" \
  "v_sub_f32 v71, v71, v246\n\t" \
  "v_sub_f32 v72, v72, v246\n\t" \
  "v_sub_f32 v73, v73, v246\n\t" \
  "v_sub_f32 v74, v74, v246\n\t" \
  "v_sub_f32 v75, v75, v246\n\t" \
  "v_sub_f32 v76, v76, v246\n\t" \
  "v_sub_f32 v77, v77, v246\n\t" \
  "v_sub_f32 v78, v78, v246\n\t" \
  "v_sub_f32 v79, v79, v246\n\t" \
  "v_sub_f32 v96, v96, v246\n\t" \
  "v_sub_f32 v97, v97, v246\n\t" \
  "v_sub_f32 v98, v98, v246\n\t" \
  "v_sub_f32 v99, v99, v246\n\t" \
  "v_sub_f32 v100, v100, v246\n\t" \
  "v_sub_f32 v101, v101, v246\n\t" \
  "v_sub_f32 v102, v102, v246\n\t" \
  "v_sub_f32 v103, v103, v246\n\t" \
  "v_sub_f32 v104, v104, v246\n\t" \
  "v_sub_f32 v105, v105, v246\n\t" \
  "v_sub_f32 v106, v106, v246\n\t" \
  "v_sub_f32 v107, v107, v246\n\t" \
  "v_sub_f32 v108, v108, v246\n\t" \
  "v_sub_f32 v109, v109, v246\n\t" \
  "v_sub_f32 v110, v110, v246\n\t" \
  "v_sub_f32 v111, v111, v246\n\t" \
  "v_sub_f32 v128, 0, v236\n\t" \
  "v_sub_f32 v129, 0, v236\n\t" \
  "v_sub_f32 v130, 0, v236\n\t" \
  "v_sub_f32 v131, 0, v236\n\t" \
  "v_sub_f32 v132, 0, v236\n\t" \
  "v_sub_f32 v133, 0, v236\n\t" \
  "v_sub_f32 v134, 0, v236\n\t" \
  "v_sub_f32 v135, 0, v236\n\t" \
  "v_sub_f32 v136, 0, v236\n\t" \
  "v_sub_f32 v137, 0, v236\n\t" \
  "v_sub_f32 v138, 0, v236\n\t" \
  "v_sub_f32 v139, 0, v236\n\t" \
  "v_sub_f32 v140, 0, v236\n\t" \
  "v_sub_f32 v141, 0, v236\n\t" \
  "v_sub_f32 v142, 0, v236\n\t" \
  "v_sub_f32 v143, 0, v236\n\t" \
  "v_mul_f32 v232, v232, v244\n\t" \
  "v_mul_f32 v233, v233, v244\n\t" \
  "v_sub_f32 v80, v80, v247\n\t" \
  "v_sub_f32 v81, v81, v247\n\t" \
  "v_sub_f32 v82, v82, v247\n\t" \
  "v_sub_f32 v83, v83, v247\n\t" \
  "v_sub_f32 v84, v84, v247\n\t" \
  "v_sub_f32 v85, v85, v247\n\t" \
  "v_sub_f32 v86, v86, v247\n\t" \
  "v_sub_f32 v87, v87, v247\n\t" \
  "v_sub_f32 v88, v88, v247\n\t" \
  "v_sub_f32 v89, v89, v247\n\t" \
  "v_sub_f32 v90, v90, v247\n\t" \
  "v_sub_f32 v91, v91, v247\n\t" \
  "v_sub_f32 v92, v92, v247\n\t" \
  "v_sub_f32 v93, v93, v247\n\t" \
  "v_sub_f32 v94, v94, v247\n\t" \
  "v_sub_f32 v95, v95, v247\n\t" \
  "v_sub_f32 v112, v112, v247\n\t" \
  "v_sub_f32 v113, v113, v247\n\t" \
  "v_sub_f32 v114, v114, v247\n\t" \
  "v_sub_f32 v115, v115, v247\n\t" \
  "v_sub_f32 v116, v116, v247\n\t" \
  "v_sub_f32 v117, v117, v247\n\t" \
  "v_sub_f32 v118, v118, v247\n\t" \
  "v_sub_f32 v119, v119, v247\n\t" \
  "v_sub_f32 v120, v120, v247\n\t" \
  "v_sub_f32 v121, v121, v247\n\t" \
  "v_sub_f32 v122, v122, v247\n\t" \
  "v_sub_f32 v123, v123, v247\n\t" \
  "v_sub_f32 v124, v124, v247\n\t" \
  "v_sub_f32 v125, v125, v247\n\t" \
  "v_sub_f32 v126, v126, v247\n\t" \
  "v_sub_f32 v127, v127, v247\n\t" \
  "v_sub_f32 v144, 0, v237\n\t" \
  "v_sub_f32 v145, 0, v237\n\t" \
  "v_sub_f32 v146, 0, v237\n\t" \
  "v_sub_f32 v147, 0, v237\n\t" \
  "v_sub_f32 v148, 0, v237\n\t" \
  "v_sub_f32 v149, 0, v237\n\t" \
  "v_sub_f32 v150, 0, v237\n\t" \
  "v_sub_f32 v151, 0, v237\n\t" \
  "v_sub_f32 v152, 0, v237\n\t" \
  "v_sub_f32 v153, 0, v237\n\t" \
  "v_sub_f32 v154, 0, v237\n\t" \
  "v_sub_f32 v155, 0, v237\n\t" \
  "v_sub_f32 v156, 0, v237\n\t" \
  "v_sub_f32 v157, 0, v237\n\t" \
  "v_sub_f32 v158, 0, v237\n\t" \
  "v_sub_f32 v159, 0, v237\n\t" \
  "v_mul_f32 v234, v234, v245\n\t" \
  "v_mul_f32 v235, v235, v245\n\t" \
  "s_mov_b32 %[sp], 1\n\t" \
  "s_branch LRAB1_%=\n\t" \
  "LRB1_%=:\n\t" \
  "s_nop 7\n\t" \
  "s_nop 7\n\t" \
  "v_accvgpr_read_b32 v246, a0\n\t" \
  "v_mul_f32 v246, v244, v246\n\t" \
  "v_accvgpr_write_b32 a0, v246\n\t" \
  "v_accvgpr_read_b32 v247, a1\n\t" \
  "v_mul_f32 v247, v244, v247\n\t" \
  "v_accvgpr_write_b32 a1, v247\n\t" \
  "v_accvgpr_read_b32 v246, a2\n\t" \
  "v_mul_f32 v246, v244, v246\n\t" \
  "v_accvgpr_write_b32 a2, v246\n\t" \
  "v_accvgpr_read_b32 v247, a3\n\t" \
  "v_mul_f32 v247, v244, v247\n\t" \
  "v_accvgpr_write_b32 a3, v247\n\t" \
  "v_accvgpr_read_b32 v246, a4\n\t" \
  "v_mul_f32 v246, v244, v246\n\t" \
  "v_accvgpr_write_b32 a4, v246\n\t" \
  "v_accvgpr_read_b32 v247, a5\n\t" \
  "v_mul_f32 v247, v244, v247\n\t" \
  "v_accvgpr_write_b32 a5, v247\n\t" \
  "v_accvgpr_read_b32 v246, a6\n\t" \
  "v_mul_f32 v246, v244, v246\n\t" \
  "v_accvgpr_write_b32 a6, v246\n\t" \
  "v_accvgpr_read_b32 v247, a7\n\t" \
  "v_mul_f32 v247, v244, v247\n\t" \
  "v_accvgpr_write_b32 a7, v247\n\t" \
  "v_accvgpr_read_b32 v246, a8\n\t" \
  "v_mul_f32 v246, v244, v246\n\t" \
  "v_accvgpr_write_b32 a8, v246\n\t" \
  "v_accvgpr_read_b32 v247, a9\n\t" \
  "v_mul_f32 v247, v244, v247\n\t" \
  "v_accvgpr_write_b32 a9, v247\n\t" \
  "v_accvgpr_read_b32 v246, a10\n\t" \
  "v_mul_f32 v246, v244, v246\n\t" \
  "v_accvgpr_write_b32 a10, v246\n\t" \
  "v_accvgpr_read_b32 v247, a11\n\t" \
  "v_mul_f32 v247, v244, v247\n\t" \
  "v_accvgpr_write_b32 a11, v247\n\t" \
  "v_accvgpr_read_b32 v246, a12\n\t" \
  "v_mul_f32 v246, v244, v246\n\t" \
  "v_accvgpr_write_b32 a12, v246\n\t" \
  "v_accvgpr_read_b32 v247, a13\n\t" \
  "v_mul_f32 v247, v244, v247\n\t" \
  "v_accvgpr_write_b32 a13, v247\n\t" \
  "v_accvgpr_read_b32 v246, a14\n\t" \
  "v_mul_f32 v246, v244, v246\n\t" \
  "v_accvgpr_write_b32 a14, v246\n\t" \
  "v_accvgpr_read_b32 v247, a15\n\t" \
  "v_mul_f32 v247, v244, v247\n\t" \
  "v_accvgpr_write_b32 a15, v247\n\t" \
  "v_accvgpr_read_b32 v246, a16\n\t" \
  "v_mul_f32 v246, v245, v246\n\t" \
  "v_accvgpr_write_b32 a16, v246\n\t" \
  "v_accvgpr_read_b32 v247, a17\n\t" \
  "v_mul_f32 v247, v245, v247\n\t" \
  "v_accvgpr_write_b32 a17, v247\n\t" \
  "v_accvgpr_read_b32 v246, a18\n\t" \
  "v_mul_f32 v246, v245, v246\n\t" \
  "v_accvgpr_write_b32 a18, v246\n\t" \
  "v_accvgpr_read_b32 v247, a19\n\t" \
  "v_mul_f32 v247, v245, v247\n\t" \
  "v_accvgpr_write_b32 a19, v247\n\t" \
  "v_accvgpr_read_b32 v246, a20\n\t" \
  "v_mul_f32 v246, v245, v246\n\t" \
  "v_accvgpr_write_b32 a20, v246\n\t" \
  "v_accvgpr_read_b32 v247, a21\n\t" \
  "v_mul_f32 v247, v245, v247\n\t" \
  "v_accvgpr_write_b32 a21, v247\n\t" \
  "v_accvgpr_read_b32 v246, a22\n\t" \
  "v_mul_f32 v246, v245, v246\n\t" \
  "v_accvgpr_write_b32 a22, v246\n\t" \
  "v_accvgpr_read_b32 v247, a23\n\t" \
  "v_mul_f32 v247, v245, v247\n\t" \
  "v_accvgpr_write_b32 a23, v247\n\t" \
  "v_accvgpr_read_b32 v246, a24\n\t" \
  "v_mul_f32 v246, v245, v246\n\t" \
  "v_accvgpr_write_b32 a24, v246\n\t" \
  "v_accvgpr_read_b32 v247, a25\n\t" \
  "v_mul_f32 v247, v245, v247\n\t" \
  "v_accvgpr_write_b32 a25, v247\n\t" \
  "v_accvgpr_read_b32 v246, a26\n\t" \
  "v_mul_f32 v246, v245, v246\n\t" \
  "v_accvgpr_write_b32 a26, v246\n\t" \
  "v_accvgpr_read_b32 v247, a27\n\t" \
  "v_mul_f32 v247, v245, v247\n\t" \
  "v_accvgpr_write_b32 a27, v247\n\t" \
  "v_accvgpr_read_b32 v246, a28\n\t" \
  "v_mul_f32 v246, v245, v246\n\t" \
  "v_accvgpr_write_b32 a28, v246\n\t" \
  "v_accvgpr_read_b32 v247, a29\n\t" \
  "v_mul_f32 v247, v245, v247\n\t" \
  "v_accvgpr_write_b32 a29, v247\n\t" \
  "v_accvgpr_read_b32 v246, a30\n\t" \
  "v_mul_f32 v246, v245, v246\n\t" \
  "v_accvgpr_write_b32 a30, v246\n\t" \
  "v_accvgpr_read_b32 v247, a31\n\t" \
  "v_mul_f32 v247, v245, v247\n\t" \
  "v_accvgpr_write_b32 a31, v247\n\t" \
  "v_accvgpr_read_b32 v246, a32\n\t" \
  "v_mul_f32 v246, v244, v246\n\t" \
  "v_accvgpr_write_b32 a32, v246\n\t" \
  "v_accvgpr_read_b32 v247, a33\n\t" \
  "v_mul_f32 v247, v244, v247\n\t" \
  "v_accvgpr_write_b32 a33, v247\n\t" \
  "v_accvgpr_read_b32 v246, a34\n\t" \
  "v_mul_f32 v246, v244, v246\n\t" \
  "v_accvgpr_write_b32 a34, v246\n\t" \
  "v_accvgpr_read_b32 v247, a35\n\t" \
  "v_mul_f32 v247, v244, v247\n\t" \
  "v_accvgpr_write_b32 a35, v247\n\t" \
  "v_accvgpr_read_b32 v246, a36\n\t" \
  "v_mul_f32 v246, v244, v246\n\t" \
  "v_accvgpr_write_b32 a36, v246\n\t" \
  "v_accvgpr_read_b32 v247, a37\n\t" \
  "v_mul_f32 v247, v244, v247\n\t" \
  "v_accvgpr_write_b32 a37, v247\n\t" \
  "v_accvgpr_read_b32 v246, a38\n\t" \
  "v_mul_f32 v246, v244, v246\n\t" \
  "v_accvgpr_write_b32 a38, v246\n\t" \
  "v_accvgpr_read_b32 v247, a39\n\t" \
  "v_mul_f32 v247, v244, v247\n\t" \
  "v_accvgpr_write_b32 a39, v247\n\t" \
  "v_accvgpr_read_b32 v246, a40\n\t" \
  "v_mul_f32 v246, v244, v246\n\t" \
  "v_accvgpr_write_b32 a40, v246\n\t" \
  "v_accvgpr_read_b32 v247, a41\n\t" \
  "v_mul_f32 v247, v244, v247\n\t" \
  "v_accvgpr_write_b32 a41, v247\n\t" \
  "v_accvgpr_read_b32 v246, a42\n\t" \
  "v_mul_f32 v246, v244, v246\n\t" \
  "v_accvgpr_write_b32 a42, v246\n\t" \
  "v_accvgpr_read_b32 v247, a43\n\t" \
  "v_mul_f32 v247, v244, v247\n\t" \
  "v_accvgpr_write_b32 a43, v247\n\t" \
  "v_accvgpr_read_b32 v246, a44\n\t" \
  "v_mul_f32 v246, v244, v246\n\t" \
  "v_accvgpr_write_b32 a44, v246\n\t" \
  "v_accvgpr_read_b32 v247, a45\n\t" \
  "v_mul_f32 v247, v244, v247\n\t" \
  "v_accvgpr_write_b32 a45, v247\n\t" \
  "v_accvgpr_read_b32 v246, a46\n\t" \
  "v_mul_f32 v246, v244, v246\n\t" \
  "v_accvgpr_write_b32 a46, v246\n\t" \
  "v_accvgpr_read_b32 v247, a47\n\t" \
  "v_mul_f32 v247, v244, v247\n\t" \
  "v_accvgpr_write_b32 a47, v247\n\t" \
  "v_accvgpr_read_b32 v246, a48\n\t" \
  "v_mul_f32 v246, v245, v246\n\t" \
  "v_accvgpr_write_b32 a48, v246\n\t" \
  "v_accvgpr_read_b32 v247, a49\n\t" \
  "v_mul_f32 v247, v245, v247\n\t" \
  "v_accvgpr_write_b32 a49, v247\n\t" \
  "v_accvgpr_read_b32 v246, a50\n\t" \
  "v_mul_f32 v246, v245, v246\n\t" \
  "v_accvgpr_write_b32 a50, v246\n\t" \
  "v_accvgpr_read_b32 v247, a51\n\t" \
  "v_mul_f32 v247, v245, v247\n\t" \
  "v_accvgpr_write_b32 a51, v247\n\t" \
  "v_accvgpr_read_b32 v246, a52\n\t" \
  "v_mul_f32 v246, v245, v246\n\t" \
  "v_accvgpr_write_b32 a52, v246\n\t" \
  "v_accvgpr_read_b32 v247, a53\n\t" \
  "v_mul_f32 v247, v245, v247\n\t" \
  "v_accvgpr_write_b32 a53, v247\n\t" \
  "v_accvgpr_read_b32 v246, a54\n\t" \
  "v_mul_f32 v246, v245, v246\n\t" \
  "v_accvgpr_write_b32 a54, v246\n\t" \
  "v_accvgpr_read_b32 v247, a55\n\t" \
  "v_mul_f32 v247, v245, v247\n\t" \
  "v_accvgpr_write_b32 a55, v247\n\t" \
  "v_accvgpr_read_b32 v246, a56\n\t" \
  "v_mul_f32 v246, v245, v246\n\t" \
  "v_accvgpr_write_b32 a56, v246\n\t" \
  "v_accvgpr_read_b32 v247, a57\n\t" \
  "v_mul_f32 v247, v245, v247\n\t" \
  "v_accvgpr_write_b32 a57, v247\n\t" \
  "v_accvgpr_read_b32 v246, a58\n\t" \
  "v_mul_f32 v246, v245, v246\n\t" \
  "v_accvgpr_write_b32 a58, v246\n\t" \
  "v_accvgpr_read_b32 v247, a59\n\t" \
  "v_mul_f32 v247, v245, v247\n\t" \
  "v_accvgpr_write_b32 a59, v247\n\t" \
  "v_accvgpr_read_b32 v246, a60\n\t" \
  "v_mul_f32 v246, v245, v246\n\t" \
  "v_accvgpr_write_b32 a60, v246\n\t" \
  "v_accvgpr_read_b32 v247, a61\n\t" \
  "v_mul_f32 v247, v245, v247\n\t" \
  "v_accvgpr_write_b32 a61, v247\n\t" \
  "v_accvgpr_read_b32 v246, a62\n\t" \
  "v_mul_f32 v246, v245, v246\n\t" \
  "v_accvgpr_write_b32 a62, v246\n\t" \
  "v_accvgpr_read_b32 v247, a63\n\t" \
  "v_mul_f32 v247, v245, v247\n\t" \
  "v_accvgpr_write_b32 a63, v247\n\t" \
  "v_accvgpr_read_b32 v246, a64\n\t" \
  "v_mul_f32 v246, v244, v246\n\t" \
  "v_accvgpr_write_b32 a64, v246\n\t" \
  "v_accvgpr_read_b32 v247, a65\n\t" \
  "v_mul_f32 v247, v244, v247\n\t" \
  "v_accvgpr_write_b32 a65, v247\n\t" \
  "v_accvgpr_read_b32 v246, a66\n\t" \
  "v_mul_f32 v246, v244, v246\n\t" \
  "v_accvgpr_write_b32 a66, v246\n\t" \
  "v_accvgpr_read_b32 v247, a67\n\t" \
  "v_mul_f32 v247, v244, v247\n\t" \
  "v_accvgpr_write_b32 a67, v247\n\t" \
  "v_accvgpr_read_b32 v246, a68\n\t" \
  "v_mul_f32 v246, v244, v246\n\t" \
  "v_accvgpr_write_b32 a68, v246\n\t" \
  "v_accvgpr_read_b32 v247, a69\n\t" \
  "v_mul_f32 v247, v244, v247\n\t" \
  "v_accvgpr_write_b32 a69, v247\n\t" \
  "v_accvgpr_read_b32 v246, a70\n\t" \
  "v_mul_f32 v246, v244, v246\n\t" \
  "v_accvgpr_write_b32 a70, v246\n\t" \
  "v_accvgpr_read_b32 v247, a71\n\t" \
  "v_mul_f32 v247, v244, v247\n\t" \
  "v_accvgpr_write_b32 a71, v247\n\t" \
  "v_accvgpr_read_b32 v246, a72\n\t" \
  "v_mul_f32 v246, v244, v246\n\t" \
  "v_accvgpr_write_b32 a72, v246\n\t" \
  "v_accvgpr_read_b32 v247, a73\n\t" \
  "v_mul_f32 v247, v244, v247\n\t" \
  "v_accvgpr_write_b32 a73, v247\n\t" \
  "v_accvgpr_read_b32 v246, a74\n\t" \
  "v_mul_f32 v246, v244, v246\n\t" \
  "v_accvgpr_write_b32 a74, v246\n\t" \
  "v_accvgpr_read_b32 v247, a75\n\t" \
  "v_mul_f32 v247, v244, v247\n\t" \
  "v_accvgpr_write_b32 a75, v247\n\t" \
  "v_accvgpr_read_b32 v246, a76\n\t" \
  "v_mul_f32 v246, v244, v246\n\t" \
  "v_accvgpr_write_b32 a76, v246\n\t" \
  "v_accvgpr_read_b32 v247, a77\n\t" \
  "v_mul_f32 v247, v244, v247\n\t" \
  "v_accvgpr_write_b32 a77, v247\n\t" \
  "v_accvgpr_read_b32 v246, a78\n\t" \
  "v_mul_f32 v246, v244, v246\n\t" \
  "v_accvgpr_write_b32 a78, v246\n\t" \
  "v_accvgpr_read_b32 v247, a79\n\t" \
  "v_mul_f32 v247, v244, v247\n\t" \
  "v_accvgpr_write_b32 a79, v247\n\t" \
  "v_accvgpr_read_b32 v246, a80\n\t" \
  "v_mul_f32 v246, v245, v246\n\t" \
  "v_accvgpr_write_b32 a80, v246\n\t" \
  "v_accvgpr_read_b32 v247, a81\n\t" \
  "v_mul_f32 v247, v245, v247\n\t" \
  "v_accvgpr_write_b32 a81, v247\n\t" \
  "v_accvgpr_read_b32 v246, a82\n\t" \
  "v_mul_f32 v246, v245, v246\n\t" \
  "v_accvgpr_write_b32 a82, v246\n\t" \
  "v_accvgpr_read_b32 v247, a83\n\t" \
  "v_mul_f32 v247, v245, v247\n\t" \
  "v_accvgpr_write_b32 a83, v247\n\t" \
  "v_accvgpr_read_b32 v246, a84\n\t" \
  "v_mul_f32 v246, v245, v246\n\t" \
  "v_accvgpr_write_b32 a84, v246\n\t" \
  "v_accvgpr_read_b32 v247, a85\n\t" \
  "v_mul_f32 v247, v245, v247\n\t" \
  "v_accvgpr_write_b32 a85, v247\n\t" \
  "v_accvgpr_read_b32 v246, a86\n\t" \
  "v_mul_f32 v246, v245, v246\n\t" \
  "v_accvgpr_write_b32 a86, v246\n\t" \
  "v_accvgpr_read_b32 v247, a87\n\t" \
  "v_mul_f32 v247, v245, v247\n\t" \
  "v_accvgpr_write_b32 a87, v247\n\t" \
  "v_accvgpr_read_b32 v246, a88\n\t" \
  "v_mul_f32 v246, v245, v246\n\t" \
  "v_accvgpr_write_b32 a88, v246\n\t" \
  "v_accvgpr_read_b32 v247, a89\n\t" \
  "v_mul_f32 v247, v245, v247\n\t" \
  "v_accvgpr_write_b32 a89, v247\n\t" \
  "v_accvgpr_read_b32 v246, a90\n\t" \
  "v_mul_f32 v246, v245, v246\n\t" \
  "v_accvgpr_write_b32 a90, v246\n\t" \
  "v_accvgpr_read_b32 v247, a91\n\t" \
  "v_mul_f32 v247, v245, v247\n\t" \
  "v_accvgpr_write_b32 a91, v247\n\t" \
  "v_accvgpr_read_b32 v246, a92\n\t" \
  "v_mul_f32 v246, v245, v246\n\t" \
  "v_accvgpr_write_b32 a92, v246\n\t" \
  "v_accvgpr_read_b32 v247, a93\n\t" \
  "v_mul_f32 v247, v245, v247\n\t" \
  "v_accvgpr_write_b32 a93, v247\n\t" \
  "v_accvgpr_read_b32 v246, a94\n\t" \
  "v_mul_f32 v246, v245, v246\n\t" \
  "v_accvgpr_write_b32 a94, v246\n\t" \
  "v_accvgpr_read_b32 v247, a95\n\t" \
  "v_mul_f32 v247, v245, v247\n\t" \
  "v_accvgpr_write_b32 a95, v247\n\t" \
  "v_accvgpr_read_b32 v246, a96\n\t" \
  "v_mul_f32 v246, v244, v246\n\t" \
  "v_accvgpr_write_b32 a96, v246\n\t" \
  "v_accvgpr_read_b32 v247, a97\n\t" \
  "v_mul_f32 v247, v244, v247\n\t" \
  "v_accvgpr_write_b32 a97, v247\n\t" \
  "v_accvgpr_read_b32 v246, a98\n\t" \
  "v_mul_f32 v246, v244, v246\n\t" \
  "v_accvgpr_write_b32 a98, v246\n\t" \
  "v_accvgpr_read_b32 v247, a99\n\t" \
  "v_mul_f32 v247, v244, v247\n\t" \
  "v_accvgpr_write_b32 a99, v247\n\t" \
  "v_accvgpr_read_b32 v246, a100\n\t" \
  "v_mul_f32 v246, v244, v246\n\t" \
  "v_accvgpr_write_b32 a100, v246\n\t" \
  "v_accvgpr_read_b32 v247, a101\n\t" \
  "v_mul_f32 v247, v244, v247\n\t" \
  "v_accvgpr_write_b32 a101, v247\n\t" \
  "v_accvgpr_read_b32 v246, a102\n\t" \
  "v_mul_f32 v246, v244, v246\n\t" \
  "v_accvgpr_write_b32 a102, v246\n\t" \
  "v_accvgpr_read_b32 v247, a103\n\t" \
  "v_mul_f32 v247, v244, v247\n\t" \
  "v_accvgpr_write_b32 a103, v247\n\t" \
  "v_accvgpr_read_b32 v246, a104\n\t" \
  "v_mul_f32 v246, v244, v246\n\t" \
  "v_accvgpr_write_b32 a104, v246\n\t" \
  "v_accvgpr_read_b32 v247, a105\n\t" \
  "v_mul_f32 v247, v244, v247\n\t" \
  "v_accvgpr_write_b32 a105, v247\n\t" \
  "v_accvgpr_read_b32 v246, a106\n\t" \
  "v_mul_f32 v246, v244, v246\n\t" \
  "v_accvgpr_write_b32 a106, v246\n\t" \
  "v_accvgpr_read_b32 v247, a107\n\t" \
  "v_mul_f32 v247, v244, v247\n\t" \
  "v_accvgpr_write_b32 a107, v247\n\t" \
  "v_accvgpr_read_b32 v246, a108\n\t" \
  "v_mul_f32 v246, v244, v246\n\t" \
  "v_accvgpr_write_b32 a108, v246\n\t" \
  "v_accvgpr_read_b32 v247, a109\n\t" \
  "v_mul_f32 v247, v244, v247\n\t" \
  "v_accvgpr_write_b32 a109, v247\n\t" \
  "v_accvgpr_read_b32 v246, a110\n\t" \
  "v_mul_f32 v246, v244, v246\n\t" \
  "v_accvgpr_write_b32 a110, v246\n\t" \
  "v_accvgpr_read_b32 v247, a111\n\t" \
  "v_mul_f32 v247, v244, v247\n\t" \
  "v_accvgpr_write_b32 a111, v247\n\t" \
  "v_accvgpr_read_b32 v246, a112\n\t" \
  "v_mul_f32 v246, v245, v246\n\t" \
  "v_accvgpr_write_b32 a112, v246\n\t" \
  "v_accvgpr_read_b32 v247, a113\n\t" \
  "v_mul_f32 v247, v245, v247\n\t" \
  "v_accvgpr_write_b32 a113, v247\n\t" \
  "v_accvgpr_read_b32 v246, a114\n\t" \
  "v_mul_f32 v246, v245, v246\n\t" \
  "v_accvgpr_write_b32 a114, v246\n\t" \
  "v_accvgpr_read_b32 v247, a115\n\t" \
  "v_mul_f32 v247, v245, v247\n\t" \
  "v_accvgpr_write_b32 a115, v247\n\t" \
  "v_accvgpr_read_b32 v246, a116\n\t" \
  "v_mul_f32 v246, v245, v246\n\t" \
  "v_accvgpr_write_b32 a116, v246\n\t" \
  "v_accvgpr_read_b32 v247, a117\n\t" \
  "v_mul_f32 v247, v245, v247\n\t" \
  "v_accvgpr_write_b32 a117, v247\n\t" \
  "v_accvgpr_read_b32 v246, a118\n\t" \
  "v_mul_f32 v246, v245, v246\n\t" \
  "v_accvgpr_write_b32 a118, v246\n\t" \
  "v_accvgpr_read_b32 v247, a119\n\t" \
  "v_mul_f32 v247, v245, v247\n\t" \
  "v_accvgpr_write_b32 a119, v247\n\t" \
  "v_accvgpr_read_b32 v246, a120\n\t" \
  "v_mul_f32 v246, v245, v246\n\t" \
  "v_accvgpr_write_b32 a120, v246\n\t" \
  "v_accvgpr_read_b32 v247, a121\n\t" \
  "v_mul_f32 v247, v245, v247\n\t" \
  "v_accvgpr_write_b32 a121, v247\n\t" \
  "v_accvgpr_read_b32 v246, a122\n\t" \
  "v_mul_f32 v246, v245, v246\n\t" \
  "v_accvgpr_write_b32 a122, v246\n\t" \
  "v_accvgpr_read_b32 v247, a123\n\t" \
  "v_mul_f32 v247, v245, v247\n\t" \
  "v_accvgpr_write_b32 a123, v247\n\t" \
  "v_accvgpr_read_b32 v246, a124\n\t" \
  "v_mul_f32 v246, v245, v246\n\t" \
  "v_accvgpr_write_b32 a124, v246\n\t" \
  "v_accvgpr_read_b32 v247, a125\n\t" \
  "v_mul_f32 v247, v245, v247\n\t" \
  "v_accvgpr_write_b32 a125, v247\n\t" \
  "v_accvgpr_read_b32 v246, a126\n\t" \
  "v_mul_f32 v246, v245, v246\n\t" \
  "v_accvgpr_write_b32 a126, v246\n\t" \
  "v_accvgpr_read_b32 v247, a127\n\t" \
  "v_mul_f32 v247, v245, v247\n\t" \
  "v_accvgpr_write_b32 a127, v247\n\t" \
  "s_nop 1\n\t" \
  "s_mov_b32 %[sp], 0\n\t" \
  "s_branch LRBB1_%=\n\t" \
  "LEND_%=:\n\t" \
  ""

template <int R> DI float acc_rd() { float r; asm volatile("v_accvgpr_read_b32 %0, a[%c1]" : "=v"(r) : "i"(R)); return r; }
template <int I, int... Js> DI void o_store1(__amdgpu_buffer_rsrc_t srd, unsigned voff, float il, iseq<Js...>) {
  constexpr int qb = I >> 3, db = (I >> 1) & 3, ss = I & 1;
  float o[8] = {acc_rd<16 * (2 * db + qb) + 8 * ss + Js>()...};
  u32x4 w;
  #pragma unroll
  for (int e = 0; e < 4; ++e) w[e] = cvtpk(o[2 * e] * il, o[2 * e + 1] * il);
  __builtin_amdgcn_raw_buffer_store_b128(w, srd, voff + 1024u * (I & 3), qb * 262144 + 4096 * ((I >> 2) & 1), 0);
}
template <int... Is> DI void o_stores(__amdgpu_buffer_rsrc_t srd, unsigned voff, float il0, float il1, iseq<Is...>) { (o_store1<Is>(srd, voff, (Is >> 3) ? il1 : il0, make_iseq<8>{}), ...); }
template <int I> DI void k_read0(const unsigned (&vkb)[4]) {
  constexpr int kb = I >> 3, d0 = I & 7, ak = 192 + 32 * kb + 4 * d0;
  asm volatile("ds_read_b128 a[%c1:%c2], %0 offset:%c3" :: "v"(vkb[d0 & 3]), "i"(ak), "i"(ak + 3), "i"(8192 * kb + 128 * (d0 >> 2)) : "memory", ACLOB);
}
template <int... Is> DI void k_reads0(const unsigned (&vkb)[4], iseq<Is...>) { (k_read0<Is>(vkb), ...); }

extern "C" __global__ __launch_bounds__(256, 1)
void k_attn(const unsigned short* __restrict__ XN, const unsigned short* __restrict__ Kc, const unsigned short* __restrict__ Vc,
            const unsigned short* __restrict__ Wq, const unsigned short* __restrict__ Wg, const unsigned short* __restrict__ Wu,
            unsigned short* __restrict__ Ob, unsigned short* __restrict__ Hb, const unsigned short* __restrict__ Wo, const unsigned short* __restrict__ Wd,
            char* __restrict__ Wt) {
  extern __shared__ __attribute__((aligned(16))) char lds[];
  const unsigned wid = rfl(threadIdx.x >> 6);
  const unsigned blk = blockIdx.x;
  const unsigned h = (blk & 7u) * 4u + ((blk >> 3) & 3u), y = blk >> 5;
  const unsigned row0 = 256u * y + 64u * wid;
  const unsigned ldsb = (unsigned)(size_t)lds;
  const unsigned m0k = rfl(ldsb + KRING + 4096u * wid), m0v = rfl(ldsb + VRING + 4096u * wid), m0w = rfl(ldsb + WRING + 4096u * wid);
  const unsigned sofk = rfl(KRS * 16u * wid);
  const __amdgpu_buffer_rsrc_t srdK = __builtin_amdgcn_make_buffer_rsrc((void*)((const char*)Kc + (size_t)h * S * 256), (short)0, S * 256, SRD_WORD3);
  const __amdgpu_buffer_rsrc_t srdV = __builtin_amdgcn_make_buffer_rsrc((void*)((const char*)Vc + (size_t)h * S * 256), (short)0, S * 256, SRD_WORD3);
  const bool wd_blk = blk < 112u;

  {
    const unsigned lane = lane_id(), hi = lane >> 5;
    const unsigned jf = h < 28u ? h : h - 28u;
    const unsigned sw = ((lane & 1u) << 1) | ((lane >> 1) & 1u);
    const unsigned vwb = ldsb + WRING + 2048u * hi + 64u * ((lane >> 2) & 3u) + 32u * ((lane >> 4) & 1u) + 8u * sw;
    const unsigned vwsq = 8192u * ((lane >> 2) & 7u) + 64u * hi + 16u * (lane & 3u);
    const unsigned vwsg = 3584u * ((lane >> 2) & 7u) + 64u * hi + 16u * (lane & 3u);
    const unsigned vks0 = KRS * (lane >> 4) + 16u * ((lane & 15u) ^ (lane >> 4)), vks1 = KRS * (lane >> 4) + 16u * ((lane & 15u) ^ (4u + (lane >> 4)));
    const unsigned vvs = KRS * ((lane >> 2) & 7u) + 64u * hi + 16u * (lane & 3u);
    const unsigned sofq = rfl(8192u * 16u * wid), sofg = rfl(3584u * 16u * wid);
    const int gurec = h < 28u ? DM * 3584 - (int)(jf * 128u) : 0;
    const __amdgpu_buffer_rsrc_t srdQ = __builtin_amdgcn_make_buffer_rsrc((void*)((const char*)Wq + h * 256u), (short)0, DM * 8192 - (int)(h * 256u), SRD_WORD3);
    const __amdgpu_buffer_rsrc_t srdG = __builtin_amdgcn_make_buffer_rsrc((void*)((const char*)Wg + jf * 128u), (short)0, gurec, SRD_WORD3);
    const __amdgpu_buffer_rsrc_t srdU = __builtin_amdgcn_make_buffer_rsrc((void*)((const char*)Wu + jf * 128u), (short)0, gurec, SRD_WORD3);
    const __amdgpu_buffer_rsrc_t srdX = __builtin_amdgcn_make_buffer_rsrc((void*)((const char*)XN + (size_t)row0 * 1024), (short)0, 64 * 1024, SRD_WORD3);
    const __amdgpu_buffer_rsrc_t srdWo = __builtin_amdgcn_make_buffer_rsrc((void*)Wo, (short)0, H * D * 1024, SRD_WORD3);
    const __amdgpu_buffer_rsrc_t srdWd = __builtin_amdgcn_make_buffer_rsrc((void*)Wd, (short)0, wd_blk ? DF * 1024 : 0, SRD_WORD3);
    const __amdgpu_buffer_rsrc_t srdH = __builtin_amdgcn_make_buffer_rsrc((void*)((char*)Hb + (size_t)(row0 >> 5) * (112 * 1024) + jf * 4096u), (short)0, h < 28u ? 2 * 112 * 1024 : 0, SRD_WORD3);
    const unsigned sort = rfl((blk >> 3) * 131072u + (blk & 7u) * 128u + 32768u * wid);
    const float C = 0.12751743082f;
    unsigned stmp;
    asm volatile("s_nop 4\n\t" PRO_STREAM
                 : [st] "=&s"(stmp)
                 : "{v248}"(vwb), "{v249}"(vwb + 65536u), "{v250}"(vwsq), "{v251}"(vwsg), "{v252}"(16u * lane), "{v253}"(vks0), "{v254}"(vks1), "{v255}"(vvs),
                   [srdq] "s"(srdQ), [srdg] "s"(srdG), [srdu] "s"(srdU), [srdx] "s"(srdX), [srdh] "s"(srdH), [srdk] "s"(srdK), [srdv] "s"(srdV),
                   [srdwo] "s"(srdWo), [srdwd] "s"(srdWd), [m0w] "s"(m0w), [m0k] "s"(m0k), [m0v] "s"(m0v), [sofq] "s"(sofq), [sofg] "s"(sofg), [sofk] "s"(sofk),
                   [sort] "s"(sort), [sc] "s"(C)
                 : VCLOB_PRO, ACLOB, "vcc", "scc", "m0", "memory");
  }

  float lsum0, lsum1;
  {
    const unsigned ln = lane_id(), l32 = ln & 31u, hi = ln >> 5;
    unsigned vkb[4];
    #pragma unroll
    for (int j = 0; j < 4; ++j) vkb[j] = ldsb + KRING + 256u * l32 + 16u * ((2u * j + hi) ^ (l32 & 7u));
    const unsigned sw = ((ln & 1u) << 1) | ((ln >> 1) & 1u);
    const unsigned vvb = ldsb + VRING + ((sw << 3) | (((ln >> 2) & 3u) << 6) | (((ln >> 4) & 1u) << 5) | (hi << 8));
    const unsigned vks0 = KRS * (ln >> 4) + 16u * ((ln & 15u) ^ (ln >> 4)), vks1 = KRS * (ln >> 4) + 16u * ((ln & 15u) ^ (4u + (ln >> 4)));
    const unsigned vvs = KRS * ((ln >> 2) & 7u) + 64u * hi + 16u * (ln & 3u);
    const unsigned rt_rd = ldsb + WRING + 65536u + 4096u * wid + 1024u * hi + 64u * ((ln >> 2) & 3u) + 32u * ((ln >> 4) & 1u) + 8u * sw;
    char* wt = Wt + ((size_t)(blk & 7u) * 46u + (blk >> 3)) * 16384u + 4096u * wid;
    const __amdgpu_buffer_rsrc_t srdWt = __builtin_amdgcn_make_buffer_rsrc((void*)wt, (short)0, 4096, SRD_WORD3);
    const __amdgpu_buffer_rsrc_t srdWtd = __builtin_amdgcn_make_buffer_rsrc((void*)(wt + 32 * 16384), (short)0, wd_blk ? 4096 : 0, SRD_WORD3);
    asm volatile("s_waitcnt vmcnt(0)\n\ts_barrier" ::: "memory");
    k_reads0(vkb, make_iseq<16>{});
    asm volatile("s_waitcnt lgkmcnt(0)\n\ts_nop 4" ::: "memory");
    float r00, r01, r10, r11;
    unsigned sk = 2u * KTILEB + sofk, sv = KTILEB + sofk, sv2 = KTILEB + sofk - 896u, scnt, spend;
    asm volatile(ATTN_STREAM
                 : "={v232}"(r00), "={v233}"(r01), "={v234}"(r10), "={v235}"(r11), [sk] "+s"(sk), [sv] "+s"(sv), [sv2] "+s"(sv2), [sc] "=&s"(scnt), [sp] "=&s"(spend)
                 : "{v224}"(vkb[0]), "{v225}"(vkb[1]), "{v226}"(vkb[2]), "{v227}"(vkb[3]), "{v228}"(vvb), "{v229}"(vks0), "{v230}"(vks1), "{v231}"(vvs), "{v250}"(rt_rd), "{v251}"(16u * ln),
                   [srdwt] "s"(srdWt), [srdwtd] "s"(srdWtd), [srdk] "s"(srdK), [srdv] "s"(srdV), [m0k] "s"(m0k), [m0v] "s"(m0v), [sthr] "s"(THR), [smarg] "s"(MARG)
                 : VCLOB_ATT, ACLOB, "vcc", "scc", "m0", "memory");
    const float rs0 = r00 + r01, rs1 = r10 + r11;
    auto p0 = __builtin_amdgcn_permlane32_swap(__float_as_uint(rs0), __float_as_uint(rs0), false, false);
    auto p1 = __builtin_amdgcn_permlane32_swap(__float_as_uint(rs1), __float_as_uint(rs1), false, false);
    lsum0 = __uint_as_float(p0[0]) + __uint_as_float(p0[1]); lsum1 = __uint_as_float(p1[0]) + __uint_as_float(p1[1]);
  }
  {
    const unsigned lb = lane_id();
    const __amdgpu_buffer_rsrc_t srdO = __builtin_amdgcn_make_buffer_rsrc((void*)((char*)Ob + (size_t)(row0 >> 5) * 262144 + h * 8192u), (short)0, 262144 + 8192, SRD_WORD3);
    const float il0 = lsum0 > 0.f ? __builtin_amdgcn_rcpf(lsum0) : 0.f, il1 = lsum1 > 0.f ? __builtin_amdgcn_rcpf(lsum1) : 0.f;
    o_stores(srdO, 16u * lb, il0, il1, make_iseq<16>{});
  }
}

constexpr int FSL = 6, FSLB = 24576;
constexpr size_t SHM_FIN = FSL * FSLB;
constexpr int FTOT = 92, FS = 64, FH = 38;
typedef __attribute__((address_space(1))) unsigned gu32;
struct FC {
  unsigned vasrc, vard, vwrd, m0a, m0w;
  __amdgpu_buffer_rsrc_t srdA;
  unsigned aoO, aoH, wo0;
};
DI void f_issue(FC& f, const unsigned u, const unsigned slot) {
  const bool isS = u < (unsigned)FS;
  const unsigned ao = isS ? f.aoO + 4096u * u : f.aoH + 4096u * (u - FS);
  const unsigned wo = f.wo0 + 8192u * u;
  #pragma unroll
  for (int j = 0; j < 4; ++j)
    asm volatile("s_mov_b32 m0, %0\n\ts_nop 0\n\tbuffer_load_dwordx4 %1, %2, %3 offen lds" :: "s"(f.m0a + slot + 1024u * j), "v"(f.vasrc), "s"(f.srdA), "s"(ao + 1024u * j) : "m0", "memory");
  #pragma unroll
  for (int j = 0; j < 2; ++j)
    asm volatile("s_mov_b32 m0, %0\n\ts_nop 0\n\tbuffer_load_dwordx4 %1, %2, %3 offen lds" :: "s"(f.m0w + slot + 1024u * j), "v"(f.vasrc), "s"(f.srdA), "s"(wo + 1024u * j) : "m0", "memory");
}
DI void f_step(FC& f, const unsigned slot, f32x16 (&acc)[2]) {
  asm volatile("s_barrier" ::: "memory");
  const unsigned va = f.vard + slot, vw = f.vwrd + slot;
  u32x4 af[4], wf[4][2];
  #pragma unroll
  for (int ks = 0; ks < 4; ++ks) {
    asm volatile("ds_read_b128 %0, %1 offset:%2" : "=v"(af[ks]) : "v"(va), "i"(1024 * ks) : "memory");
    asm volatile("ds_read_b128 %0, %1 offset:%2" : "=v"(wf[ks][0]) : "v"(vw), "i"(2048 * ks) : "memory");
    asm volatile("ds_read_b128 %0, %1 offset:%2" : "=v"(wf[ks][1]) : "v"(vw), "i"(2048 * ks + 1024) : "memory");
  }
  #pragma unroll
  for (int ks = 0; ks < 4; ++ks) {
    asm volatile("s_waitcnt lgkmcnt(%c3)" : "+v"(af[ks]), "+v"(wf[ks][0]), "+v"(wf[ks][1]) : "i"(3 * (3 - ks)) : "memory");
    GAP();
    #pragma unroll
    for (int nb = 0; nb < 2; ++nb)
      acc[nb] = __builtin_amdgcn_mfma_f32_32x32x16_bf16(__builtin_bit_cast(bf16x8, wf[ks][nb]), __builtin_bit_cast(bf16x8, af[ks]), acc[nb], 0, 0, 0);
    GAP();
  }
}

extern "C" __global__ __launch_bounds__(512, 2)
void k_final(const unsigned short* __restrict__ X, char* __restrict__ ws, unsigned short* __restrict__ Out) {
  extern __shared__ __attribute__((aligned(16))) char lds[];
  const unsigned tid = threadIdx.x, lane = tid & 63u, wid8 = rfl(tid >> 6), wid = wid8 & 3u, l32 = lane & 31u, hi = lane >> 5;
  const bool loader = wid8 >= 4u;
  const unsigned blk = blockIdx.x;
  const unsigned fin = blk & 1u, mt = ((blk >> 1) & 3u) * 4u + ((blk >> 3) & 3u), nt = blk >> 5, tile = mt * 8u + nt;
  const unsigned m0 = 128u * mt, n0 = 64u * nt;
  const unsigned u0 = fin ? (unsigned)FH : 0u, n = fin ? (unsigned)(FTOT - FH) : (unsigned)FH;
  const unsigned ldsb = (unsigned)(size_t)lds;
  FC f;
  f.vasrc = 16u * lane;
  f.vard = ldsb + 4096u * wid + 16u * lane;
  f.vwrd = ldsb + 16384u + 16u * lane;
  f.m0a = rfl(ldsb + 4096u * wid); f.m0w = rfl(ldsb + 16384u + 2048u * wid);
  f.aoO = (unsigned)WS_O + ((m0 >> 5) + wid) * 262144u; f.aoH = (unsigned)WS_H + ((m0 >> 5) + wid) * 114688u;
  f.wo0 = (unsigned)WS_WT + nt * (46u * 16384u) + 2048u * wid;
  f.srdA = __builtin_amdgcn_make_buffer_rsrc((void*)ws, (short)0, (int)WS_SLAB, SRD_WORD3);
  const __amdgpu_buffer_rsrc_t srdS = __builtin_amdgcn_make_buffer_rsrc((void*)(ws + WS_SLAB + (size_t)tile * 32768), (short)0, 32768, SRD_WORD3);
  gu32* flag = (gu32*)(ws + WS_CNT) + tile * 64u;
  if (loader) {
    asm volatile("s_nop 4" ::: "memory");
    #pragma unroll
    for (int j = 0; j < FSL - 1; ++j) f_issue(f, u0 + j, (unsigned)FSLB * j);
    unsigned slot = (FSL - 1) * FSLB;
    for (unsigned i = 0; i < n; ++i) {
      const unsigned left = n - 1u - i;
      if (left >= 4u)      asm volatile("s_waitcnt vmcnt(24)\n\ts_barrier" ::: "memory");
      else if (left == 3u) asm volatile("s_waitcnt vmcnt(18)\n\ts_barrier" ::: "memory");
      else if (left == 2u) asm volatile("s_waitcnt vmcnt(12)\n\ts_barrier" ::: "memory");
      else if (left == 1u) asm volatile("s_waitcnt vmcnt(6)\n\ts_barrier" ::: "memory");
      else                 asm volatile("s_waitcnt vmcnt(0)\n\ts_barrier" ::: "memory");
      if (i + FSL - 1 < n) f_issue(f, u0 + i + FSL - 1, slot);
      slot = slot == (FSL - 1) * FSLB ? 0u : slot + FSLB;
    }
    if (!fin) __syncthreads();
  } else {
    f32x16 accS[2] = {}, accF[2] = {};
    const unsigned nS = fin ? (unsigned)(FS - FH) : (unsigned)FH;
    unsigned i = 0, slot = 0;
    for (; i < nS; ++i) { f_step(f, slot, accS); slot = slot == (FSL - 1) * FSLB ? 0u : slot + FSLB; }
    const unsigned voff = 16u * lane + 8192u * wid;
    const unsigned n1 = fin ? n - 3u : n;
    for (; i < n1; ++i) { f_step(f, slot, accF); slot = slot == (FSL - 1) * FSLB ? 0u : slot + FSLB; }
    if (!fin) {
      #pragma unroll
      for (int nb = 0; nb < 2; ++nb)
        #pragma unroll
        for (int r4 = 0; r4 < 4; ++r4) {
          const f32x4 v = {accS[nb][4 * r4], accS[nb][4 * r4 + 1], accS[nb][4 * r4 + 2], accS[nb][4 * r4 + 3]};
          __builtin_amdgcn_raw_buffer_store_b128(__builtin_bit_cast(u32x4, v), srdS, voff + 1024u * r4, 4096 * nb, 16);
        }
      asm volatile("s_waitcnt vmcnt(0)" ::: "memory");
      __syncthreads();
      if (tid == 0) __hip_atomic_store(flag, 1u, __ATOMIC_RELAXED, __HIP_MEMORY_SCOPE_AGENT);
    } else {
      const size_t off = (size_t)(m0 + 32u * wid + l32) * DM + n0 + 8u * hi;
      for (unsigned spins = 0; spins < (1u << 22); ++spins) {
        if (rfl(__hip_atomic_load(flag, __ATOMIC_RELAXED, __HIP_MEMORY_SCOPE_AGENT)) != 0u) break;
        __builtin_amdgcn_s_sleep(1);
      }
      __builtin_amdgcn_fence(__ATOMIC_ACQUIRE, "wavefront");
      u32x4 ld[2][4], xw[4];
      #pragma unroll
      for (int nb = 0; nb < 2; ++nb)
        #pragma unroll
        for (int r4 = 0; r4 < 4; ++r4) ld[nb][r4] = __builtin_amdgcn_raw_buffer_load_b128(srdS, voff + 1024u * r4, 4096 * nb, 16);
      #pragma unroll
      for (int j = 0; j < 4; ++j) xw[j] = *(const u32x4*)(X + off + 16 * j);
      #pragma unroll
      for (int j = 0; j < 3; ++j) { f_step(f, slot, accF); slot = slot == (FSL - 1) * FSLB ? 0u : slot + FSLB; }
      #pragma unroll
      for (int nb = 0; nb < 2; ++nb)
        #pragma unroll
        for (int ss = 0; ss < 2; ++ss) {
          float o[8];
          #pragma unroll
          for (int e = 0; e < 8; ++e) {
            const int r = 8 * ss + e;
            const float sv = accS[nb][r] + __uint_as_float(ld[nb][r >> 2][r & 3]), fv = accF[nb][r];
            const float gate = fv * __builtin_amdgcn_rcpf(1.0f + __builtin_amdgcn_exp2f(-fv * 1.4426950408889634f));
            const unsigned xd = xw[2 * nb + ss][e >> 1];
            o[e] = ((e & 1) ? bfhi(xd) : bflo(xd)) + sv * gate;
          }
          u32x4 w;
          #pragma unroll
          for (int e = 0; e < 4; ++e) w[e] = cvtpk(o[2 * e], o[2 * e + 1]);
          *(u32x4*)(Out + off + 32 * nb + 16 * ss) = w;
        }
    }
  }
}

extern "C" void kernel_launch(void* const* d_in, const int* in_sizes, int n_in,
                              void* d_out, int out_size, void* d_ws, size_t ws_size,
                              hipStream_t stream) {
  const unsigned short* x  = (const unsigned short*)d_in[0];
  const unsigned short* kc = (const unsigned short*)d_in[1];
  const unsigned short* vc = (const unsigned short*)d_in[2];
  const unsigned short* wq = (const unsigned short*)d_in[3];
  const unsigned short* wo = (const unsigned short*)d_in[4];
  const unsigned short* wg = (const unsigned short*)d_in[6];
  const unsigned short* wu = (const unsigned short*)d_in[7];
  const unsigned short* wd = (const unsigned short*)d_in[8];
  unsigned short* xn = (unsigned short*)((char*)d_ws + WS_XN);
  unsigned short* ob = (unsigned short*)((char*)d_ws + WS_O);
  unsigned short* hb = (unsigned short*)((char*)d_ws + WS_H);
  (void)hipFuncSetAttribute((const void*)k_attn, hipFuncAttributeMaxDynamicSharedMemorySize, (int)SHM_ATTN);
  (void)hipFuncSetAttribute((const void*)k_final, hipFuncAttributeMaxDynamicSharedMemorySize, (int)SHM_FIN);
  hipLaunchKernelGGL(k_ln, dim3(S / 8 + 256), dim3(64), 0, stream, x, xn, (unsigned*)((char*)d_ws + WS_CNT), (const u32x4*)wq, (const u32x4*)wg, (const u32x4*)wu);
  hipLaunchKernelGGL(k_attn, dim3(256), dim3(256), SHM_ATTN, stream, xn, kc, vc, wq, wg, wu, ob, hb, wo, wd, (char*)d_ws + WS_WT);
  hipLaunchKernelGGL(k_final, dim3(256), dim3(512), SHM_FIN, stream, x, (char*)d_ws, (unsigned short*)d_out);
}
```
